```python
import torch

def _softmax(x, axis=-1):
    return torch.softmax(x, dim=axis)

def _gelu(x):
    return torch.nn.functional.gelu(x, approximate='tanh')

def _silu(x):
    return torch.nn.functional.silu(x)

def _sigmoid(x):
    return torch.sigmoid(x)

def _layernorm(x, axis=-1, eps=1e-05):
    m = x.mean(dim=axis, keepdim=True)
    v = x.var(dim=axis, keepdim=True, unbiased=False)
    return (x - m) / torch.sqrt(v + eps)

def _scatter_add(tgt, idx, src, axis=0):
    return tgt.clone().index_add_(axis, idx, src)

def _segment_sum(src, seg, n):
    out = torch.zeros((n,) + src.shape[1:], dtype=src.dtype)
    return out.index_add_(0, seg, src)

def _pad(x, pw):
    flat = tuple((v for pair in reversed(pw) for v in pair))
    return torch.nn.functional.pad(x, flat)
kernel_name = 'sequential_preLN__linear_kernel_hedgehog__mixer_gmlp__prompt'

def setup_inputs():

    _w = lambda a: (lambda t: t.to(torch.bfloat16) if t.is_floating_point() else t)(torch.as_tensor(a))

    def _A_setup():
        B, S, D, H = (1, 2048, 1024, 32)
        Dh = D // H
        return {'x': _w((lambda *s: torch.randn(*s, dtype=torch.bfloat16) * 0.05)(B, S, D)), 'wqkv': _w((lambda *s: torch.randn(*s, dtype=torch.bfloat16) * 0.2)(D, 3 * D)), 'w_phi': _w((lambda *s: torch.randn(*s, dtype=torch.bfloat16) * 0.2)(H, Dh, Dh) * Dh ** (-0.5)), 'wo': _w((lambda *s: torch.randn(*s, dtype=torch.bfloat16) * 0.05)(D, D))}
    _A_in = _A_setup()

    def _B_setup():
        B, S, D, F = (1, 2048, 1024, 3584)
        Fh = F // 2
        return {'x': _w((lambda *s: torch.randn(*s, dtype=torch.bfloat16) * 0.05)(B, S, D)), 'w_in': _w((lambda *s: torch.randn(*s, dtype=torch.bfloat16) * 0.2)(D, 2 * Fh)), 'w_sp': _w(0.01 * (lambda *s: torch.randn(*s, dtype=torch.bfloat16) * 0.2)(S, S)), 'b_sp': _w(1.0 + 0.01 * (lambda *s: torch.randn(*s, dtype=torch.bfloat16) * 0.05)(S)), 'w_out': _w((lambda *s: torch.randn(*s, dtype=torch.bfloat16) * 0.05)(Fh, D))}
    _B_in = _B_setup()
    return {'A_x': _A_in['x'], 'A_wqkv': _A_in['wqkv'], 'A_w_phi': _A_in['w_phi'], 'A_wo': _A_in['wo'], 'B_x': _B_in['x'], 'B_w_in': _B_in['w_in'], 'B_w_sp': _B_in['w_sp'], 'B_b_sp': _B_in['b_sp'], 'B_w_out': _B_in['w_out']}

def reference(A_x, A_wqkv, A_w_phi, A_wo, B_x, B_w_in, B_w_sp, B_b_sp, B_w_out):

    def _A_ref(x, wqkv, w_phi, wo):
        B, S, D = x.shape
        H = 32
        Dh = D // H
        qkv = (lambda x, *d: x.permute(*d))((x @ wqkv).reshape(B, S, 3, H, Dh), 2, 0, 3, 1, 4)
        q, k, v = (qkv[0], qkv[1], qkv[2])
        pq = torch.einsum('bhsd,hdf->bhsf', q, w_phi)
        pk = torch.einsum('bhsd,hdf->bhsf', k, w_phi)
        phi_q = torch.concatenate([_softmax(pq, axis=-1), _softmax(-pq, axis=-1)], axis=-1)
        phi_k = torch.concatenate([_softmax(pk, axis=-1), _softmax(-pk, axis=-1)], axis=-1)
        kv = torch.einsum('bhsf,bhsd->bhfd', phi_k, v)
        z = torch.einsum('bhsf->bhf', phi_k)
        num = torch.einsum('bhqf,bhfd->bhqd', phi_q, kv)
        den = torch.einsum('bhqf,bhf->bhq', phi_q, z)[..., None] + 1e-06
        o = num / den
        return (lambda x, *d: x.permute(*d))(o, 0, 2, 1, 3).reshape(B, S, D) @ wo

    def _B_ref(x, w_in, w_sp, b_sp, w_out):
        B, S, D = x.shape
        F = w_in.shape[-1]
        Fh = F // 2
        z = _gelu(torch.einsum('bsd,df->bsf', x, w_in))
        z1 = z[..., :Fh].clone()
        z2 = _layernorm(z[..., Fh:], axis=-1)
        g = torch.einsum('ts,bsf->btf', w_sp, z2) + b_sp[None, :, None]
        return torch.einsum('bsf,fd->bsd', z1 * g, w_out)
    x = A_x
    h = x + _A_ref(_layernorm(x, axis=-1), A_wqkv, A_w_phi, A_wo)
    h2 = h + _B_ref(_layernorm(h, axis=-1), B_w_in, B_w_sp, B_b_sp, B_w_out)
    return h2
```

```cpp
#include <hip/hip_runtime.h>
#include <hip/hip_bf16.h>
#include <utility>

namespace {
typedef short s16x4 __attribute__((ext_vector_type(4)));
typedef short s16x8 __attribute__((ext_vector_type(8)));
typedef __bf16 bf16x8 __attribute__((ext_vector_type(8)));
typedef __bf16 bf16x2 __attribute__((ext_vector_type(2)));
typedef float f32x2 __attribute__((ext_vector_type(2)));
typedef float f32x4 __attribute__((ext_vector_type(4)));
typedef int i32x4 __attribute__((ext_vector_type(4)));
typedef unsigned u32x4 __attribute__((ext_vector_type(4)));
typedef unsigned u32x2 __attribute__((ext_vector_type(2)));
typedef unsigned short u16;
#define LAS __attribute__((address_space(3)))
#define GAS __attribute__((address_space(1)))
#define DI __device__ __forceinline__
#define FENCE() __builtin_amdgcn_sched_barrier(0)
#define RLX_AGENT __ATOMIC_RELAXED, __HIP_MEMORY_SCOPE_AGENT

constexpr int SEQ = 2048, DM = 1024, FH = 1792, FF = 3584;
constexpr size_t WS_CTL = 0;
constexpr size_t CTL_BYTES = 64 * 256;
constexpr size_t WS_XN = CTL_BYTES;
constexpr size_t WS_CSUM = WS_XN + (size_t)SEQ * DM * 2;
constexpr size_t WS_KVP = WS_CSUM + 4 * FF * 4;
constexpr size_t WS_ZP = WS_KVP + (size_t)16 * 16 * 8192;
constexpr size_t WS_O = WS_ZP + 16 * 16 * 512;
constexpr size_t WS_H = WS_O + (size_t)SEQ * DM * 2;
constexpr size_t WS_HST = WS_H + (size_t)SEQ * DM * 2;
constexpr size_t WS_Z1 = WS_HST + 16 * 16 * 128 * 8;
constexpr size_t WS_Z2 = WS_Z1 + (size_t)SEQ * FH * 2;
constexpr size_t WS_ZST = WS_Z2 + (size_t)SEQ * FH * 2;
constexpr size_t WS_G = WS_ZST + 16 * 16 * 128 * 8;
constexpr size_t WS_END = WS_G + (size_t)SEQ * FH * 2;

DI i32x4 make_srd(const void* p, unsigned bytes) {
  const unsigned long long a = (unsigned long long)p; i32x4 rs;
  rs[0] = __builtin_amdgcn_readfirstlane((int)(unsigned)a); rs[1] = __builtin_amdgcn_readfirstlane((int)(unsigned)(a >> 32)) & 0xffff;
  rs[2] = (int)bytes; rs[3] = 0x00020000; return rs;
}
DI __amdgpu_buffer_rsrc_t make_rsrc(const void* p, unsigned bytes) { return __builtin_amdgcn_make_buffer_rsrc(const_cast<void*>(p), 0, bytes, 0x00020000); }
DI void dma16(unsigned ldsaddr, unsigned voff, i32x4 rs, unsigned soff) {
  asm volatile("s_mov_b32 m0, %0\n\ts_nop 0\n\tbuffer_load_dwordx4 %1, %2, %3 offen lds" :: "s"(ldsaddr), "v"(voff), "s"(rs), "s"(soff));
}
template<int N> DI void waitv() { asm volatile("s_waitcnt vmcnt(%0)" :: "i"(N) : "memory"); }
DI void waitl0() { asm volatile("" ::: "memory"); __builtin_amdgcn_s_waitcnt(0xC07F); asm volatile("" ::: "memory"); }
DI void bar() { asm volatile("s_barrier" ::: "memory"); }
DI void lbar() { waitl0(); bar(); }
DI unsigned cvtpk(float lo, float hi) { f32x2 v = {lo, hi}; bf16x2 b = __builtin_convertvector(v, bf16x2); return __builtin_bit_cast(unsigned, b); }
DI float bflo(unsigned u) { return __uint_as_float(u << 16); }
DI float bfhi(unsigned u) { return __uint_as_float(u & 0xffff0000u); }
DI bf16x8 asbf(u32x4 v) { return __builtin_bit_cast(bf16x8, v); }
DI f32x4 mfma(bf16x8 a, bf16x8 b, f32x4 c) { return __builtin_amdgcn_mfma_f32_16x16x32_bf16(a, b, c, 0, 0, 0); }
DI s16x4 trread(unsigned addr) { return __builtin_amdgcn_ds_read_tr16_b64_v4i16((LAS s16x4*)addr); }
DI bf16x8 join(s16x4 lo, s16x4 hi) { return __builtin_bit_cast(bf16x8, __builtin_shufflevector(lo, hi, 0, 1, 2, 3, 4, 5, 6, 7)); }
template<class T> DI T ldsld(unsigned addr) { return *(const LAS T*)addr; }
template<class T> DI void ldsst(unsigned addr, T v) { *(LAS T*)addr = v; }
template<class F> DI float red4(float x, F f) {
  const unsigned u = __float_as_uint(x);
  auto a = __builtin_amdgcn_permlane16_swap(u, u, false, false);
  const float y = f(__uint_as_float(a[0]), __uint_as_float(a[1]));
  const unsigned v = __float_as_uint(y);
  auto b = __builtin_amdgcn_permlane32_swap(v, v, false, false);
  return f(__uint_as_float(b[0]), __uint_as_float(b[1]));
}
DI float sum4(float x) { return red4(x, [](float a, float b) { return a + b; }); }
DI float max4(float x) { return red4(x, [](float a, float b) { return fmaxf(a, b); }); }
DI float min4(float x) { return red4(x, [](float a, float b) { return fminf(a, b); }); }

DI void wait_count(GAS unsigned* cnt, unsigned want, GAS unsigned* tmo) {
  for (unsigned spins = 0;; ++spins) {
    if ((unsigned)__builtin_amdgcn_readfirstlane(__hip_atomic_load(cnt, RLX_AGENT)) >= want) break;
    if (spins > (1u << 16)) { __hip_atomic_store(tmo, 1u, RLX_AGENT); break; }
    if ((spins & 255u) == 255u && __builtin_amdgcn_readfirstlane(__hip_atomic_load(tmo, RLX_AGENT))) break;
    __builtin_amdgcn_s_sleep(1);
  }
}

template<int SW> __host__ __device__ constexpr int bmask(int pos) { return SW == 64 ? ((pos >> 1) & 3) : 0; }
__host__ __device__ constexpr int kswap(int k) { return (k & ~12) | ((k & 4) << 1) | ((k & 8) >> 1); }

template<int BM_, int SW_, int NS_, int WM_, int WN_, int ST_, int K_, int LDA_, int LDB_> struct Cfg {
  static constexpr int BM = BM_, SW = SW_, NS = NS_, BN = SW_ * NS_, WM = WM_, WN = WN_, ST = ST_, K = K_, LDA = LDA_, LDB = LDB_;
  static constexpr int NKT = K / 64;
  static constexpr int MT = BM / WM / 16, NT = BN / WN / 16;
  static constexpr int A_BYTES = BM * 128, ROWB = SW * 2, STRIP_BYTES = 64 * ROWB, B_BYTES = NS * STRIP_BYTES, STAGE = A_BYTES + B_BYTES, RING = ST * STAGE;
  static constexpr int PA = A_BYTES / 1024, PB = B_BYTES / 1024, PT = PA + PB, NL = 4;
  static constexpr int PMAX = (PT + NL - 1) / NL, PMIN = PT / NL, NBIG = PT - PMIN * NL;
  static constexpr bool STRIP_PER_WAVE = (NS == WN) && (WN > 1);
  static constexpr int TPS = STRIP_PER_WAVE ? SW / 16 : SW / 16 / WN;
  static constexpr bool SWZ = (SW == 64);
  static constexpr int NV = SWZ ? TPS : 1;
  static constexpr int NM = MT * NT, NR = MT + 2 * NT;
  static_assert(WM * WN == 4 && PA % NL == 0 && STRIP_BYTES % 1024 == 0 && NT == (STRIP_PER_WAVE ? TPS : TPS * NS) && ST >= 3 && NKT >= ST, "config");
  static_assert((ST - 1) * PMAX <= 63, "vmcnt range");
};
template<class C> struct Frags { bf16x8 a[C::MT]; s16x4 bl[C::NT], bh[C::NT]; };

template<class C, int I> DI void read_item(Frags<C>& f, unsigned aA, const unsigned (&aB)[C::NV], int ksimm) {
  if constexpr (I >= 2 && I < 2 + C::MT) {
    constexpr int m = I - 2;
    f.a[m] = ldsld<bf16x8>(aA + m * 2048);
  } else {
    constexpr int r = I < 2 ? I : I - C::MT, j = r >> 1, u = r & 1;
    constexpr int strip = C::STRIP_PER_WAVE ? 0 : j / C::TPS, tj = C::STRIP_PER_WAVE ? j : j % C::TPS;
    constexpr int imm = strip * C::STRIP_BYTES + u * 8 * C::ROWB + (C::SWZ ? 0 : tj * 32);
    const s16x4 v = trread(aB[C::SWZ ? tj : 0] + imm + ksimm);
    if constexpr (u == 0) f.bl[j] = v; else f.bh[j] = v;
  }
}
template<class C, int I> DI void mfma_item(f32x4 (&acc)[C::NT][C::MT], const Frags<C>& f) {
  constexpr int n = I / C::MT, m = I % C::MT;
  acc[n][m] = mfma(join(f.bl[n], f.bh[n]), f.a[m], acc[n][m]);
}
template<class C, int... Is>
DI void step_impl(f32x4 (&acc)[C::NT][C::MT], const Frags<C>& cur, Frags<C>& nxt, unsigned aA, const unsigned (&aB)[C::NV], int ksimm, std::integer_sequence<int, Is...>) {
  ((mfma_item<C, Is>(acc, cur), FENCE(),
    [&] { if constexpr (C::NR <= C::NM) { if constexpr (Is < C::NR) read_item<C, Is>(nxt, aA, aB, ksimm); }
          else { if constexpr (2 * Is < C::NR) read_item<C, 2 * Is>(nxt, aA, aB, ksimm); if constexpr (2 * Is + 1 < C::NR) read_item<C, 2 * Is + 1>(nxt, aA, aB, ksimm); } }(),
    FENCE()), ...);
}
template<class C> DI void step(f32x4 (&acc)[C::NT][C::MT], const Frags<C>& cur, Frags<C>& nxt, unsigned aA, const unsigned (&aB)[C::NV], int ksimm) {
  static_assert(2 * C::NM >= C::NR, "reads per mfma");
  step_impl<C>(acc, cur, nxt, aA, aB, ksimm, std::make_integer_sequence<int, C::NM>{});
}
template<class C, int... Is> DI void read_all_impl(Frags<C>& f, unsigned aA, const unsigned (&aB)[C::NV], std::integer_sequence<int, Is...>) { (read_item<C, Is>(f, aA, aB, 0), ...); }

template<class C> DI void consumer_loop(f32x4 (&acc)[C::NT][C::MT], unsigned lds0, int wid, int lane) {
  const int wc = wid % C::WN, wr = wid / C::WN;
  const int g = lane >> 4, i16 = lane & 15, q = i16 >> 2, p = lane & 3;
  unsigned aA0, aA1, aB[C::NV];
  { const int row = wr * (C::BM / C::WM) + i16, f = (row >> 1) & 7; aA0 = lds0 + row * 128 + (g ^ f) * 16; aA1 = aA0 ^ 64; }
  { const int pos = (g >> 1) * 16 + (g & 1) * 4 + q;
    const unsigned base = lds0 + C::A_BYTES + (C::STRIP_PER_WAVE ? wc * C::STRIP_BYTES : 0) + pos * C::ROWB + (p >> 1) * 16 + (p & 1) * 8;
    const int t0 = C::STRIP_PER_WAVE ? 0 : wc * C::TPS;
#pragma unroll
    for (int v = 0; v < C::NV; ++v) aB[v] = base + (C::SWZ ? (((t0 + v) ^ bmask<C::SW>(pos)) * 32) : t0 * 32); }
#pragma unroll
  for (int n = 0; n < C::NT; ++n)
#pragma unroll
    for (int m = 0; m < C::MT; ++m) acc[n][m] = (f32x4){0.f, 0.f, 0.f, 0.f};
  Frags<C> F0, F1;
  waitl0(); bar();
  read_all_impl<C>(F0, aA0, aB, std::make_integer_sequence<int, C::NR>{});
  FENCE();
  int slot = 0;
#pragma unroll 1
  for (int t = 0; t < C::NKT; ++t) {
    unsigned a, b[C::NV];
    unsigned o = slot * C::STAGE;
    a = aA1 + o;
#pragma unroll
    for (int v = 0; v < C::NV; ++v) b[v] = aB[v] + o;
    step<C>(acc, F0, F1, a, b, 32 * C::ROWB);
    slot = slot + 1 == C::ST ? 0 : slot + 1;
    waitl0(); bar();
    o = slot * C::STAGE;
    a = aA0 + o;
#pragma unroll
    for (int v = 0; v < C::NV; ++v) b[v] = aB[v] + o;
    step<C>(acc, F1, F0, a, b, 0);
  }
  waitl0();
}

template<class C> struct Loader {
  unsigned voff[C::PMAX]; int lw; unsigned lds0; i32x4 rsA, rsB;
  template<class ColFn> DI void init(int lw_, int lane, unsigned lds0_, unsigned a_row0_bytes, ColFn colbyte) {
    lw = lw_; lds0 = lds0_;
#pragma unroll
    for (int i = 0; i < C::PMAX; ++i) {
      const int pi = lw + i * C::NL;
      unsigned v;
      if (i < C::PA / C::NL) { const int row = pi * 8 + (lane >> 3), ch = (lane & 7) ^ ((row >> 1) & 7); v = a_row0_bytes + row * C::LDA + ch * 16; }
      else {
        const int lin = (pi - C::PA) * 64 + lane;
        constexpr int CPR = C::SW / 8, CPS = 64 * CPR;
        const int strip = lin / CPS, r = lin % CPS, pos = r / CPR, chp = r % CPR;
        const int k = kswap(pos), ch = chp ^ (bmask<C::SW>(pos) << 1);
        v = k * C::LDB + colbyte(strip) + ch * 16;
      }
      voff[i] = v;
    }
  }
  template<int I> DI void piece(unsigned slotaddr, unsigned soffA, unsigned soffB) const {
    const int pi = lw + I * C::NL;
    if constexpr (I < C::PA / C::NL) dma16(slotaddr + pi * 1024, voff[I], rsA, soffA);
    else if constexpr (I < C::PMIN) dma16(slotaddr + pi * 1024, voff[I], rsB, soffB);
    else { if (lw < C::NBIG) dma16(slotaddr + pi * 1024, voff[I], rsB, soffB); }
  }
  template<int... Is> DI void tile_impl(int t, int slot, std::integer_sequence<int, Is...>) const { const unsigned sa = lds0 + slot * C::STAGE; (piece<Is>(sa, t * 128, t * (64 * C::LDB)), ...); }
  DI void tile(int t, int slot) const { tile_impl(t, slot, std::make_integer_sequence<int, C::PMAX>{}); }
  template<int TILES> DI void wait() const {
    if constexpr (C::NBIG == 0) waitv<TILES * C::PMIN>();
    else { if (lw < C::NBIG) waitv<TILES * C::PMAX>(); else waitv<TILES * C::PMIN>(); }
  }
};
template<class C, class ColFn> DI void producer_loop(unsigned lds0, int lw, int lane, i32x4 rsA, i32x4 rsB, unsigned a_row0_bytes, ColFn colbyte) {
  Loader<C> L; L.rsA = rsA; L.rsB = rsB;
  L.init(lw, lane, lds0, a_row0_bytes, colbyte);
  L.tile(0, 0); L.tile(1, 1);
  L.template wait<1>(); bar();
#pragma unroll
  for (int s = 2; s < C::ST; ++s) L.tile(s, s);
  int slot = 0;
#pragma unroll 1
  for (int t = 1; t <= C::NKT - C::ST; ++t) {
    L.template wait<C::ST - 2>(); bar();
    L.tile(t - 1 + C::ST, slot);
    slot = slot + 1 == C::ST ? 0 : slot + 1;
  }
  [&]<int... Is>(std::integer_sequence<int, Is...>) {
    ((L.template wait<(C::ST - 2 - Is > 0 ? C::ST - 2 - Is : 0)>(), bar()), ...);
  }(std::make_integer_sequence<int, C::ST>{});
}
DI void tile_of_block(int b, int rx, int& pm, int& pn) {
  const int xcd = b & 7, j = b >> 3, px = 16 / rx, cx = 32 / rx;
  pm = (xcd % px) * rx + j % rx; pn = (xcd / px) * cx + j / rx;
}

__global__ __launch_bounds__(256) void k0_prep(const u16* __restrict__ x, const u16* __restrict__ w_in, unsigned char* __restrict__ ws) {
  const int tid = threadIdx.x, lane = tid & 63, wid = tid >> 6, b = blockIdx.x;
  if (b == 255 && tid < 64) *(unsigned*)(ws + WS_CTL + tid * 256) = 0u;
  u32x4 v[2][2];
#pragma unroll
  for (int r = 0; r < 2; ++r)
#pragma unroll
    for (int c = 0; c < 2; ++c) v[r][c] = *(const u32x4*)(x + (size_t)(b * 8 + wid * 2 + r) * DM + c * 512 + lane * 8);
  f32x4 cs0 = {0, 0, 0, 0}, cs1 = {0, 0, 0, 0};
  const int cb = b % 56, kq = b / 56;
  if (b < 224) {
#pragma unroll
    for (int i = 0; i < 8; ++i) {
      const u32x4 w = *(const u32x4*)(w_in + (size_t)(kq * 256 + i * 32 + (tid >> 3)) * FF + cb * 64 + (tid & 7) * 8);
      cs0 += (f32x4){bflo(w[0]), bfhi(w[0]), bflo(w[1]), bfhi(w[1])}; cs1 += (f32x4){bflo(w[2]), bfhi(w[2]), bflo(w[3]), bfhi(w[3])};
    }
  }
#pragma unroll
  for (int r = 0; r < 2; ++r) {
    float f[16]; float s = 0;
#pragma unroll
    for (int c = 0; c < 2; ++c)
#pragma unroll
      for (int j = 0; j < 4; ++j) { f[c * 8 + 2 * j] = bflo(v[r][c][j]); f[c * 8 + 2 * j + 1] = bfhi(v[r][c][j]); }
#pragma unroll
    for (int j = 0; j < 16; ++j) s += f[j];
#pragma unroll
    for (int o = 1; o < 64; o <<= 1) s += __shfl_xor(s, o);
    const float mean = s * (1.0f / DM); float q = 0;
#pragma unroll
    for (int j = 0; j < 16; ++j) { f[j] -= mean; q += f[j] * f[j]; }
#pragma unroll
    for (int o = 1; o < 64; o <<= 1) q += __shfl_xor(q, o);
    const float rstd = 1.0f / sqrtf(q * (1.0f / DM) + 1e-5f);
#pragma unroll
    for (int c = 0; c < 2; ++c) { u32x4 o;
#pragma unroll
      for (int j = 0; j < 4; ++j) o[j] = cvtpk(f[c * 8 + 2 * j] * rstd, f[c * 8 + 2 * j + 1] * rstd);
      *(u32x4*)((u16*)(ws + WS_XN) + (size_t)(b * 8 + wid * 2 + r) * DM + c * 512 + lane * 8) = o; }
  }
  if (b < 224) {
    __shared__ f32x4 red[4][8][2];
#pragma unroll
    for (int o = 8; o < 64; o <<= 1)
#pragma unroll
      for (int j = 0; j < 4; ++j) { cs0[j] += __shfl_xor(cs0[j], o); cs1[j] += __shfl_xor(cs1[j], o); }
    if (lane < 8) { red[wid][lane][0] = cs0; red[wid][lane][1] = cs1; }
    __syncthreads();
    if (tid < 16) { const f32x4 s = red[0][tid >> 1][tid & 1] + red[1][tid >> 1][tid & 1] + red[2][tid >> 1][tid & 1] + red[3][tid >> 1][tid & 1];
      *(f32x4*)(ws + WS_CSUM + ((size_t)kq * FF + cb * 64 + tid * 4) * 4) = s; }
  }
}

using C1 = Cfg<128, 64, 3, 2, 2, 3, DM, DM * 2, 3 * DM * 2>;
constexpr int K1_WPHI = C1::RING;
constexpr int K1_LDS = K1_WPHI + 4096;
constexpr int K1_QX = 0;
constexpr int K1_PHI = K1_QX + 16384, PHI_RS = 136, PHI_HEAD = 128 * PHI_RS;
constexpr int K1_V = K1_PHI + 2 * PHI_HEAD, V_RS = 72, V_HEAD = 128 * V_RS;
constexpr int K1_KVT = K1_V + 2 * V_HEAD;
constexpr int K1_ZT = K1_KVT + 8192;
constexpr int K1_OST = K1_ZT + 256, OST_RS = 144;
static_assert(K1_OST + 128 * OST_RS <= C1::RING && K1_OST % 16 == 0 && K1_V % 8 == 0, "K1 epilogue map");

DI void feature_map(const f32x4 p0, const f32x4 p1, u32x4& pos, u32x4& neg) {
  constexpr float L2E = 1.4426950408889634f;
  const float mx = max4(fmaxf(fmaxf(fmaxf(p0[0], p0[1]), fmaxf(p0[2], p0[3])), fmaxf(fmaxf(p1[0], p1[1]), fmaxf(p1[2], p1[3]))));
  const float mn = min4(fminf(fminf(fminf(p0[0], p0[1]), fminf(p0[2], p0[3])), fminf(fminf(p1[0], p1[1]), fminf(p1[2], p1[3]))));
  const float cx = -mx * L2E, cn = mn * L2E;
  float ep[8], en[8], sp = 0.f, sn = 0.f;
#pragma unroll
  for (int j = 0; j < 8; ++j) { const float xv = j < 4 ? p0[j & 3] : p1[j & 3];
    ep[j] = __builtin_amdgcn_exp2f(__builtin_fmaf(xv, L2E, cx)); en[j] = __builtin_amdgcn_exp2f(__builtin_fmaf(xv, -L2E, cn)); sp += ep[j]; sn += en[j]; }
  const float rp = __builtin_amdgcn_rcpf(sum4(sp)), rn = __builtin_amdgcn_rcpf(sum4(sn));
#pragma unroll
  for (int j = 0; j < 4; ++j) { pos[j] = cvtpk(ep[2 * j] * rp, ep[2 * j + 1] * rp); neg[j] = cvtpk(en[2 * j] * rn, en[2 * j + 1] * rn); }
}
DI u32x4 pack8(const f32x4 a, const f32x4 b) { return (u32x4){cvtpk(a[0], a[1]), cvtpk(a[2], a[3]), cvtpk(b[0], b[1]), cvtpk(b[2], b[3])}; }

__global__ __launch_bounds__(512, 2) void k1_qkv(const u16* __restrict__ wqkv, const u16* __restrict__ wphi, unsigned char* __restrict__ ws) {
  extern __shared__ __attribute__((aligned(16))) unsigned char shm[];
  const unsigned lds0 = (unsigned)(size_t)(LAS unsigned char*)shm;
  const int tid = threadIdx.x, lane = tid & 63, wid = __builtin_amdgcn_readfirstlane(tid >> 6);
  const int g = lane >> 4, i16 = lane & 15, q4 = i16 >> 2, p4 = lane & 3;
  int pm, pn; tile_of_block(blockIdx.x, 8, pm, pn);
  const int w4 = wid & 3, wr = w4 >> 1, wc = w4 & 1;
  GAS unsigned* cnt = (GAS unsigned*)(ws + WS_CTL) + pn * 64;
  GAS unsigned* tmo = (GAS unsigned*)(ws + WS_CTL) + 32 * 64;
  const __amdgpu_buffer_rsrc_t rsKV = make_rsrc(ws + WS_KVP, 16 * 16 * 8192), rsZ = make_rsrc(ws + WS_ZP, 16 * 16 * 512);
  u32x4 phiq[4][2];
  const unsigned wph = lds0 + K1_WPHI + wc * 2048 + (4 * g + q4) * 64 + p4 * 8;
  if (wid < 4) {
    ldsst<u32x4>(lds0 + K1_WPHI + tid * 16, *(const u32x4*)(wphi + (size_t)pn * 2048 + tid * 8));
    f32x4 acc[C1::NT][C1::MT];
    consumer_loop<C1>(acc, lds0, wid, lane);
#pragma unroll
    for (int m = 0; m < 4; ++m) ldsst<u32x4>(lds0 + K1_QX + (w4 * 4 + m) * 1024 + lane * 16, pack8(acc[0][m], acc[1][m]));
    lbar();
    const bf16x8 wf0 = join(trread(wph), trread(wph + 1024)), wf1 = join(trread(wph + 32), trread(wph + 1024 + 32));
#pragma unroll
    for (int m = 0; m < 4; ++m) {
      const bf16x8 kf = asbf(pack8(acc[2][m], acc[3][m]));
      const f32x4 z4 = {0.f, 0.f, 0.f, 0.f};
      u32x4 pos, neg; feature_map(mfma(wf0, kf, z4), mfma(wf1, kf, z4), pos, neg);
      const int tok = wr * 64 + m * 16 + i16;
      const unsigned pa = lds0 + K1_PHI + wc * PHI_HEAD + tok * PHI_RS + g * 8;
      ldsst<u32x2>(pa, (u32x2){pos[0], pos[1]}); ldsst<u32x2>(pa + 32, (u32x2){pos[2], pos[3]});
      ldsst<u32x2>(pa + 64, (u32x2){neg[0], neg[1]}); ldsst<u32x2>(pa + 96, (u32x2){neg[2], neg[3]});
      const u32x4 vv = pack8(acc[4][m], acc[5][m]);
      const unsigned va = lds0 + K1_V + wc * V_HEAD + tok * V_RS + g * 8;
      ldsst<u32x2>(va, (u32x2){vv[0], vv[1]}); ldsst<u32x2>(va + 32, (u32x2){vv[2], vv[3]});
    }
    lbar();
    f32x4 d[2][2], dz[2];
#pragma unroll
    for (int a = 0; a < 2; ++a) { dz[a] = (f32x4){0.f, 0.f, 0.f, 0.f}; d[a][0] = dz[a]; d[a][1] = dz[a]; }
    const bf16x8 ones = asbf((u32x4){0x3f803f80u, 0x3f803f80u, 0x3f803f80u, 0x3f803f80u});
    const unsigned pr = lds0 + K1_PHI + wc * PHI_HEAD + (4 * g + q4) * PHI_RS + wr * 64 + p4 * 8;
    const unsigned vr = lds0 + K1_V + wc * V_HEAD + (4 * g + q4) * V_RS + p4 * 8;
#pragma unroll
    for (int s = 0; s < 4; ++s) {
      bf16x8 pf[2], vf[2];
#pragma unroll
      for (int a = 0; a < 2; ++a) { pf[a] = join(trread(pr + s * 32 * PHI_RS + a * 32), trread(pr + (s * 32 + 16) * PHI_RS + a * 32));
                                    vf[a] = join(trread(vr + s * 32 * V_RS + a * 32), trread(vr + (s * 32 + 16) * V_RS + a * 32)); }
#pragma unroll
      for (int a = 0; a < 2; ++a) { d[a][0] = mfma(pf[a], vf[0], d[a][0]); d[a][1] = mfma(pf[a], vf[1], d[a][1]); dz[a] = mfma(pf[a], ones, dz[a]); }
    }
    const unsigned kvoff = ((pn * 16 + pm) * 2 + wc) * 4096 + wr * 2048 + lane * 16;
#pragma unroll
    for (int dt = 0; dt < 2; ++dt) __builtin_amdgcn_raw_buffer_store_b128(pack8(d[0][dt], d[1][dt]), rsKV, kvoff + dt * 1024, 0, 16);
    if (i16 < 2) __builtin_amdgcn_raw_buffer_store_b128(__builtin_bit_cast(u32x4, i16 == 0 ? dz[0] : dz[1]), rsZ, ((pn * 16 + pm) * 2 + wc) * 256 + wr * 128 + g * 32 + i16 * 16, 0, 16);
    waitv<0>(); bar();
    if (wid == 0) {
      if (lane == 0) __hip_atomic_fetch_add(cnt, 1u, RLX_AGENT);
      wait_count(cnt, 16u, tmo);
    }
    bar();
  } else {
    producer_loop<C1>(lds0, w4, lane, make_srd(ws + WS_XN, SEQ * DM * 2), make_srd(wqkv, DM * 3 * DM * 2), (unsigned)pm * 128 * C1::LDA,
                      [&](int strip) { return (unsigned)(strip * DM + pn * 64) * 2; });
    bar();
    const bf16x8 wf0 = join(trread(wph), trread(wph + 1024)), wf1 = join(trread(wph + 32), trread(wph + 1024 + 32));
#pragma unroll
    for (int m = 0; m < 4; ++m) {
      const bf16x8 qf = ldsld<bf16x8>(lds0 + K1_QX + (w4 * 4 + m) * 1024 + lane * 16);
      const f32x4 z4 = {0.f, 0.f, 0.f, 0.f};
      feature_map(mfma(wf0, qf, z4), mfma(wf1, qf, z4), phiq[m][0], phiq[m][1]);
    }
    bar(); bar(); bar();
  }
  { const int head = wid & 1, fr = wid >> 1;
    u32x4 pv[16];
#pragma unroll
    for (int p = 0; p < 16; ++p) pv[p] = __builtin_amdgcn_raw_buffer_load_b128(rsKV, ((pn * 16 + p) * 2 + head) * 4096 + fr * 1024 + lane * 16, 0, 16);
    float zs = 0.f;
    if (wid < 2) {
#pragma unroll
      for (int p = 0; p < 16; ++p) zs += __uint_as_float(__builtin_amdgcn_raw_buffer_load_b32(rsZ, ((pn * 16 + p) * 2 + head) * 256 + lane * 4, 0, 16));
    }
    f32x4 s0 = {0.f, 0.f, 0.f, 0.f}, s1 = s0;
#pragma unroll
    for (int p = 0; p < 16; ++p) { s0 += (f32x4){bflo(pv[p][0]), bfhi(pv[p][0]), bflo(pv[p][1]), bfhi(pv[p][1])}; s1 += (f32x4){bflo(pv[p][2]), bfhi(pv[p][2]), bflo(pv[p][3]), bfhi(pv[p][3])}; }
    ldsst<u32x4>(lds0 + K1_KVT + (head * 4 + fr) * 1024 + lane * 16, pack8(s0, s1));
    if (wid < 2) ldsst<u16>(lds0 + K1_ZT + head * 128 + lane * 2, (u16)cvtpk(zs, 0.f));
  }
  lbar();
  if (wid >= 4) {
    bf16x8 kv[2][2], zf[2];
#pragma unroll
    for (int ks = 0; ks < 2; ++ks) { zf[ks] = ldsld<bf16x8>(lds0 + K1_ZT + wc * 128 + ks * 64 + g * 16);
#pragma unroll
      for (int dt = 0; dt < 2; ++dt) kv[ks][dt] = ldsld<bf16x8>(lds0 + K1_KVT + (wc * 4 + ks * 2 + dt) * 1024 + lane * 16); }
#pragma unroll
    for (int m = 0; m < 4; ++m) {
      const f32x4 z4 = {0.f, 0.f, 0.f, 0.f};
      const bf16x8 b0 = asbf(phiq[m][0]), b1 = asbf(phiq[m][1]);
      const f32x4 den = mfma(zf[1], b1, mfma(zf[0], b0, z4));
      const f32x4 n0 = mfma(kv[1][0], b1, mfma(kv[0][0], b0, z4)), n1 = mfma(kv[1][1], b1, mfma(kv[0][1], b0, z4));
      const float r = 1.0f / (den[0] + 1e-6f);
      const unsigned oa = lds0 + K1_OST + (wr * 64 + m * 16 + i16) * OST_RS + wc * 64 + g * 8;
      ldsst<u32x2>(oa, (u32x2){cvtpk(n0[0] * r, n0[1] * r), cvtpk(n0[2] * r, n0[3] * r)});
      ldsst<u32x2>(oa + 32, (u32x2){cvtpk(n1[0] * r, n1[1] * r), cvtpk(n1[2] * r, n1[3] * r)});
    }
  }
  lbar();
#pragma unroll
  for (int i = 0; i < 2; ++i) { const int c = tid + i * 512, row = c >> 3, ch = c & 7;
    *(u32x4*)(ws + WS_O + (size_t)(pm * 128 + row) * (DM * 2) + pn * 128 + ch * 16) = ldsld<u32x4>(lds0 + K1_OST + row * OST_RS + ch * 16); }
}

template<class C, bool STATS> DI void res_gemm(const void* A, unsigned a_bytes, const u16* W, unsigned w_bytes, const u16* res, u16* out, f32x2* stats, int rx) {
  extern __shared__ __attribute__((aligned(16))) unsigned char shm[];
  const unsigned lds0 = (unsigned)(size_t)(LAS unsigned char*)shm;
  const int tid = threadIdx.x, lane = tid & 63, wid = __builtin_amdgcn_readfirstlane(tid >> 6);
  int pm, pn; tile_of_block(blockIdx.x, rx, pm, pn);
  if (wid >= 4) {
    producer_loop<C>(lds0, wid - 4, lane, make_srd(A, a_bytes), make_srd(W, w_bytes), (unsigned)pm * 128 * C::LDA, [&](int) { return (unsigned)pn * 128; });
    bar();
    return;
  }
  u32x4 rv[4];
#pragma unroll
  for (int i = 0; i < 4; ++i) { const int c = tid + i * 256; rv[i] = *(const u32x4*)(res + (size_t)(pm * 128 + (c >> 3)) * DM + pn * 64 + (c & 7) * 8); }
  f32x4 acc[C::NT][C::MT];
  consumer_loop<C>(acc, lds0, wid, lane);
  const int g = lane >> 4, i16 = lane & 15, wr = wid >> 1, wc = wid & 1;
#pragma unroll
  for (int n = 0; n < 2; ++n)
#pragma unroll
    for (int m = 0; m < 4; ++m)
      ldsst<u32x2>(lds0 + (wr * 64 + m * 16 + i16) * OST_RS + (wc * 32 + n * 16 + g * 4) * 2, (u32x2){cvtpk(acc[n][m][0], acc[n][m][1]), cvtpk(acc[n][m][2], acc[n][m][3])});
  lbar();
#pragma unroll
  for (int i = 0; i < 4; ++i) { const int c = tid + i * 256, row = c >> 3, ch = c & 7;
    const u32x4 a = ldsld<u32x4>(lds0 + row * OST_RS + ch * 16); u32x4 o; float s = 0.f, qq = 0.f;
#pragma unroll
    for (int j = 0; j < 4; ++j) { o[j] = cvtpk(bflo(rv[i][j]) + bflo(a[j]), bfhi(rv[i][j]) + bfhi(a[j]));
      if (STATS) { const float lo = bflo(o[j]), hi = bfhi(o[j]); s += lo + hi; qq += lo * lo + hi * hi; } }
    *(u32x4*)(out + (size_t)(pm * 128 + row) * DM + pn * 64 + ch * 8) = o;
    if (STATS) {
#pragma unroll
      for (int of = 1; of < 8; of <<= 1) { s += __shfl_xor(s, of); qq += __shfl_xor(qq, of); }
      if (ch == 0) stats[(pm * 16 + pn) * 128 + row] = (f32x2){s, qq};
    }
  }
}
using C2 = Cfg<128, 64, 1, 2, 2, 5, DM, DM * 2, DM * 2>;
using C5 = Cfg<128, 64, 1, 2, 2, 5, FH, FH * 2, DM * 2>;
__global__ __launch_bounds__(512, 2) void k2_wo(const u16* __restrict__ x, const u16* __restrict__ wo, unsigned char* __restrict__ ws) {
  res_gemm<C2, true>(ws + WS_O, SEQ * DM * 2, wo, DM * DM * 2, x, (u16*)(ws + WS_H), (f32x2*)(ws + WS_HST), 4);
}
__global__ __launch_bounds__(512, 2) void k5_out(const u16* __restrict__ w_out, unsigned char* __restrict__ ws, u16* __restrict__ out) {
  res_gemm<C5, false>(ws + WS_G, SEQ * FH * 2, w_out, FH * DM * 2, (const u16*)(ws + WS_H), out, nullptr, 4);
}

using C3 = Cfg<128, 112, 2, 2, 2, 3, DM, DM * 2, FF * 2>;
constexpr int K3_RST = C3::RING;
constexpr int K3_CS = K3_RST + 1024;
constexpr int K3_ZS = K3_CS + 1024;
constexpr int K3_LDS = K3_ZS + 1024;
constexpr int K3_XF = 0;
constexpr int K3_STG = 32768, STG_RS = 240;
DI float gelu_tanh(float x) {
  constexpr float C0 = -2.0f * 0.7978845608028654f * 1.4426950408889634f, C1_ = C0 * 0.044715f;
  const float e = __builtin_amdgcn_exp2f(x * __builtin_fmaf(x * x, C1_, C0));
  return x * __builtin_amdgcn_rcpf(1.0f + e);
}
__global__ __launch_bounds__(512, 2) void k3_in(const u16* __restrict__ w_in, unsigned char* __restrict__ ws) {
  extern __shared__ __attribute__((aligned(16))) unsigned char shm[];
  const unsigned lds0 = (unsigned)(size_t)(LAS unsigned char*)shm;
  const int tid = threadIdx.x, lane = tid & 63, wid = __builtin_amdgcn_readfirstlane(tid >> 6);
  const int g = lane >> 4, i16 = lane & 15;
  int pm, pn; tile_of_block(blockIdx.x, 8, pm, pn);
  const int w4 = wid & 3, wr = w4 >> 1, wc = w4 & 1, mp = wid >> 2;
  GAS unsigned* cnt = (GAS unsigned*)(ws + WS_CTL) + (16 + pm) * 64;
  GAS unsigned* tmo = (GAS unsigned*)(ws + WS_CTL) + 32 * 64;
  u32x2 z[2][7];
  if (wid < 4) {
    if (tid < 128) { const f32x2* st = (const f32x2*)(ws + WS_HST) + (size_t)pm * 16 * 128 + tid; f32x2 s = {0.f, 0.f};
#pragma unroll
      for (int p = 0; p < 16; ++p) s += st[p * 128];
      const float mean = s[0] * (1.0f / DM), var = fmaxf(s[1] * (1.0f / DM) - mean * mean, 0.f);
      ldsst<f32x2>(lds0 + K3_RST + tid * 8, (f32x2){mean, 1.0f / sqrtf(var + 1e-5f)}); }
    if (tid < 224) { const float* cs = (const float*)(ws + WS_CSUM) + (tid < 112 ? pn * 112 + tid : FH + pn * 112 + tid - 112);
      ldsst<float>(lds0 + K3_CS + (tid < 112 ? tid : tid + 16) * 4, cs[0] + cs[FF] + cs[2 * FF] + cs[3 * FF]); }
    f32x4 acc[C3::NT][C3::MT];
    consumer_loop<C3>(acc, lds0, wid, lane);
    f32x2 rs[4];
#pragma unroll
    for (int m = 0; m < 4; ++m) rs[m] = ldsld<f32x2>(lds0 + K3_RST + (wr * 64 + m * 16 + i16) * 8);
#pragma unroll
    for (int n = 0; n < 7; ++n) { const f32x4 cs = ldsld<f32x4>(lds0 + K3_CS + wc * 512 + (n * 16 + g * 4) * 4);
#pragma unroll
      for (int m = 0; m < 4; ++m) { const f32x4 v = (acc[n][m] - cs * rs[m][0]) * rs[m][1]; const u32x2 w = {cvtpk(v[0], v[1]), cvtpk(v[2], v[3])};
        if (m < 2) z[m][n] = w; else ldsst<u32x2>(lds0 + K3_XF + ((w4 * 2 + m - 2) * 7 + n) * 512 + lane * 8, w); } }
    lbar();
  } else {
    producer_loop<C3>(lds0, w4, lane, make_srd(ws + WS_H, SEQ * DM * 2), make_srd(w_in, DM * FF * 2), (unsigned)pm * 128 * C3::LDA,
                      [&](int strip) { return (unsigned)(strip * FH + pn * 112) * 2; });
    bar();
#pragma unroll
    for (int m = 0; m < 2; ++m)
#pragma unroll
      for (int n = 0; n < 7; ++n) z[m][n] = ldsld<u32x2>(lds0 + K3_XF + ((w4 * 2 + m) * 7 + n) * 512 + lane * 8);
  }
  float s[2] = {0.f, 0.f}, qq[2] = {0.f, 0.f};
#pragma unroll
  for (int m = 0; m < 2; ++m)
#pragma unroll
    for (int n = 0; n < 7; ++n) {
      const u32x2 w = {cvtpk(gelu_tanh(bflo(z[m][n][0])), gelu_tanh(bfhi(z[m][n][0]))), cvtpk(gelu_tanh(bflo(z[m][n][1])), gelu_tanh(bfhi(z[m][n][1])))};
      z[m][n] = w;
      if (wc == 1) { const float a = bflo(w[0]), b = bfhi(w[0]), c = bflo(w[1]), d = bfhi(w[1]); s[m] += (a + b) + (c + d); qq[m] += (a * a + b * b) + (c * c + d * d); }
    }
  const int row0 = wr * 64 + mp * 32;
  if (wc == 0) {
    unsigned char* z1 = ws + WS_Z1 + ((size_t)((pm * 16 + pn) * 4 + wr * 2 + mp) * 7) * 1024 + lane * 16;
#pragma unroll
    for (int n = 0; n < 7; ++n) *(u32x4*)(z1 + n * 1024) = (u32x4){z[0][n][0], z[0][n][1], z[1][n][0], z[1][n][1]};
  } else {
#pragma unroll
    for (int m = 0; m < 2; ++m) { const float a = sum4(s[m]), b = sum4(qq[m]);
      if (g == 0) __hip_atomic_store((GAS unsigned long long*)(ws + WS_ZST) + (pm * 16 + pn) * 128 + row0 + m * 16 + i16,
                                     ((unsigned long long)__float_as_uint(b) << 32) | __float_as_uint(a), RLX_AGENT); }
    waitv<0>();
  }
  bar();
  if (wid == 0) {
    if (lane == 0) __hip_atomic_fetch_add(cnt, 1u, RLX_AGENT);
    wait_count(cnt, 16u, tmo);
  }
  bar();
  if (tid < 128) { f32x2 t = {0.f, 0.f};
#pragma unroll
    for (int p = 0; p < 16; ++p) { const unsigned long long w = __hip_atomic_load((GAS unsigned long long*)(ws + WS_ZST) + (pm * 16 + p) * 128 + tid, RLX_AGENT);
      t += (f32x2){__uint_as_float((unsigned)w), __uint_as_float((unsigned)(w >> 32))}; }
    const float mean = t[0] * (1.0f / FH), var = fmaxf(t[1] * (1.0f / FH) - mean * mean, 0.f);
    ldsst<f32x2>(lds0 + K3_ZS + tid * 8, (f32x2){mean, 1.0f / sqrtf(var + 1e-5f)}); }
  lbar();
  if (wc == 1) {
    const unsigned stg = lds0 + K3_STG + (wr * 2 + mp) * 32 * STG_RS;
#pragma unroll
    for (int m = 0; m < 2; ++m) { const f32x2 ms = ldsld<f32x2>(lds0 + K3_ZS + (row0 + m * 16 + i16) * 8);
#pragma unroll
      for (int n = 0; n < 7; ++n)
        ldsst<u32x2>(stg + (m * 16 + i16) * STG_RS + n * 32 + g * 8,
                     (u32x2){cvtpk((bflo(z[m][n][0]) - ms[0]) * ms[1], (bfhi(z[m][n][0]) - ms[0]) * ms[1]), cvtpk((bflo(z[m][n][1]) - ms[0]) * ms[1], (bfhi(z[m][n][1]) - ms[0]) * ms[1])}); }
    waitl0();
    unsigned char* z2 = ws + WS_Z2 + ((size_t)pn * SEQ + pm * 128 + row0) * 224;
#pragma unroll
    for (int i = 0; i < 7; ++i) { const int c = i * 64 + lane, r = c / 14, ch = c % 14;
      *(u32x4*)(z2 + c * 16) = ldsld<u32x4>(stg + r * STG_RS + ch * 16); }
  }
}

using C4 = Cfg<128, 112, 1, 4, 1, 4, SEQ, SEQ * 2, 224>;
__global__ __launch_bounds__(512, 2) void k4_sp(const u16* __restrict__ w_sp, const u16* __restrict__ b_sp, unsigned char* __restrict__ ws) {
  extern __shared__ __attribute__((aligned(16))) unsigned char shm[];
  const unsigned lds0 = (unsigned)(size_t)(LAS unsigned char*)shm;
  const int tid = threadIdx.x, lane = tid & 63, wid = __builtin_amdgcn_readfirstlane(tid >> 6);
  int pm, pn; tile_of_block(blockIdx.x, 4, pm, pn);
  if (wid >= 4) {
    producer_loop<C4>(lds0, wid - 4, lane, make_srd(w_sp, SEQ * SEQ * 2), make_srd(ws + WS_Z2, SEQ * FH * 2), (unsigned)pm * 128 * C4::LDA, [&](int) { return (unsigned)pn * (SEQ * 224); });
    return;
  }
  const int g = lane >> 4, i16 = lane & 15;
  u32x4 z1[7];
  const unsigned char* z1p = ws + WS_Z1 + ((size_t)((pm * 16 + pn) * 4 + wid) * 7) * 1024 + lane * 16;
#pragma unroll
  for (int n = 0; n < 7; ++n) z1[n] = *(const u32x4*)(z1p + n * 1024);
  float bs[2];
#pragma unroll
  for (int m = 0; m < 2; ++m) bs[m] = bflo(b_sp[pm * 128 + wid * 32 + m * 16 + i16]);
  f32x4 acc[C4::NT][C4::MT];
  consumer_loop<C4>(acc, lds0, wid, lane);
  bar();
  const unsigned stg = lds0 + wid * 32 * STG_RS;
#pragma unroll
  for (int m = 0; m < 2; ++m)
#pragma unroll
    for (int n = 0; n < 7; ++n) { const unsigned a = z1[n][2 * m], b = z1[n][2 * m + 1]; const f32x4 v = acc[n][m] + bs[m];
      ldsst<u32x2>(stg + (m * 16 + i16) * STG_RS + n * 32 + g * 8, (u32x2){cvtpk(bflo(a) * v[0], bfhi(a) * v[1]), cvtpk(bflo(b) * v[2], bfhi(b) * v[3])}); }
  waitl0();
  unsigned char* gp = ws + WS_G + (size_t)(pm * 128 + wid * 32) * (FH * 2) + pn * 224;
#pragma unroll
  for (int i = 0; i < 7; ++i) { const int c = i * 64 + lane, r = c / 14, ch = c % 14;
    *(u32x4*)(gp + (size_t)r * (FH * 2) + ch * 16) = ldsld<u32x4>(stg + r * STG_RS + ch * 16); }
}
}

extern "C" void kernel_launch(void* const* d_in, const int* in_sizes, int n_in, void* d_out, int out_size, void* d_ws, size_t ws_size, hipStream_t stream) {
  const u16* x = (const u16*)d_in[0];
  const u16* wqkv = (const u16*)d_in[1];
  const u16* wphi = (const u16*)d_in[2];
  const u16* wo = (const u16*)d_in[3];
  const u16* w_in = (const u16*)d_in[5];
  const u16* w_sp = (const u16*)d_in[6];
  const u16* b_sp = (const u16*)d_in[7];
  const u16* w_out = (const u16*)d_in[8];
  unsigned char* ws = (unsigned char*)d_ws;
  u16* out = (u16*)d_out;
  (void)in_sizes; (void)n_in; (void)out_size; (void)ws_size;
  (void)hipFuncSetAttribute((const void*)k1_qkv, hipFuncAttributeMaxDynamicSharedMemorySize, K1_LDS);
  (void)hipFuncSetAttribute((const void*)k2_wo, hipFuncAttributeMaxDynamicSharedMemorySize, C2::RING);
  (void)hipFuncSetAttribute((const void*)k3_in, hipFuncAttributeMaxDynamicSharedMemorySize, K3_LDS);
  (void)hipFuncSetAttribute((const void*)k4_sp, hipFuncAttributeMaxDynamicSharedMemorySize, C4::RING);
  (void)hipFuncSetAttribute((const void*)k5_out, hipFuncAttributeMaxDynamicSharedMemorySize, C5::RING);
  k0_prep<<<256, 256, 0, stream>>>(x, w_in, ws);
  k1_qkv<<<256, 512, K1_LDS, stream>>>(wqkv, wphi, ws);
  k2_wo<<<256, 512, C2::RING, stream>>>(x, wo, ws);
  k3_in<<<256, 512, K3_LDS, stream>>>(w_in, ws);
  k4_sp<<<256, 512, C4::RING, stream>>>(w_sp, b_sp, ws);
  k5_out<<<256, 512, C5::RING, stream>>>(w_out, ws, out);
}
```

```cpp
#include <hip/hip_runtime.h>
#include <hip/hip_bf16.h>
#include <utility>

namespace {
typedef short s16x4 __attribute__((ext_vector_type(4)));
typedef short s16x8 __attribute__((ext_vector_type(8)));
typedef __bf16 bf16x8 __attribute__((ext_vector_type(8)));
typedef __bf16 bf16x2 __attribute__((ext_vector_type(2)));
typedef float f32x2 __attribute__((ext_vector_type(2)));
typedef float f32x4 __attribute__((ext_vector_type(4)));
typedef int i32x4 __attribute__((ext_vector_type(4)));
typedef unsigned u32x4 __attribute__((ext_vector_type(4)));
typedef unsigned u32x2 __attribute__((ext_vector_type(2)));
typedef unsigned short u16;
#define LAS __attribute__((address_space(3)))
#define GAS __attribute__((address_space(1)))
#define DI __device__ __forceinline__
#define FENCE() __builtin_amdgcn_sched_barrier(0)
#define RLX_AGENT __ATOMIC_RELAXED, __HIP_MEMORY_SCOPE_AGENT

constexpr int SEQ = 2048, DM = 1024, FH = 1792, FF = 3584;
constexpr size_t WS_CTL = 0;
constexpr size_t CTL_BYTES = 64 * 256;
constexpr size_t WS_XN = CTL_BYTES;
constexpr size_t WS_CSUM = WS_XN + (size_t)SEQ * DM * 2;
constexpr size_t WS_KVP = WS_CSUM + 4 * FF * 4;
constexpr size_t WS_ZP = WS_KVP + (size_t)16 * 16 * 8192;
constexpr size_t WS_O = WS_ZP + 16 * 16 * 512;
constexpr size_t WS_H = WS_O + (size_t)SEQ * DM * 2;
constexpr size_t WS_HST = WS_H + (size_t)SEQ * DM * 2;
constexpr size_t WS_Z1 = WS_HST + 16 * 16 * 128 * 8;
constexpr size_t WS_Z2 = WS_Z1 + (size_t)SEQ * FH * 2;
constexpr size_t WS_ZST = WS_Z2 + (size_t)SEQ * FH * 2;
constexpr size_t WS_G = WS_ZST + 16 * 16 * 128 * 8;
constexpr size_t WS_END = WS_G + (size_t)SEQ * FH * 2;

DI i32x4 make_srd(const void* p, unsigned bytes) {
  const unsigned long long a = (unsigned long long)p; i32x4 rs;
  rs[0] = __builtin_amdgcn_readfirstlane((int)(unsigned)a); rs[1] = __builtin_amdgcn_readfirstlane((int)(unsigned)(a >> 32)) & 0xffff;
  rs[2] = (int)bytes; rs[3] = 0x00020000; return rs;
}
DI __amdgpu_buffer_rsrc_t make_rsrc(const void* p, unsigned bytes) { return __builtin_amdgcn_make_buffer_rsrc(const_cast<void*>(p), 0, bytes, 0x00020000); }
DI void dma16(unsigned ldsaddr, unsigned voff, i32x4 rs, unsigned soff) {
  asm volatile("s_mov_b32 m0, %0\n\ts_nop 0\n\tbuffer_load_dwordx4 %1, %2, %3 offen lds" :: "s"(ldsaddr), "v"(voff), "s"(rs), "s"(soff));
}
template<int N> DI void waitv() { asm volatile("s_waitcnt vmcnt(%0)" :: "i"(N) : "memory"); }
DI void waitl0() { asm volatile("" ::: "memory"); __builtin_amdgcn_s_waitcnt(0xC07F); asm volatile("" ::: "memory"); }
DI void bar() { asm volatile("s_barrier" ::: "memory"); }
DI void lbar() { waitl0(); bar(); }
DI unsigned cvtpk(float lo, float hi) { f32x2 v = {lo, hi}; bf16x2 b = __builtin_convertvector(v, bf16x2); return __builtin_bit_cast(unsigned, b); }
DI float bflo(unsigned u) { return __uint_as_float(u << 16); }
DI float bfhi(unsigned u) { return __uint_as_float(u & 0xffff0000u); }
DI bf16x8 asbf(u32x4 v) { return __builtin_bit_cast(bf16x8, v); }
DI f32x4 mfma(bf16x8 a, bf16x8 b, f32x4 c) { return __builtin_amdgcn_mfma_f32_16x16x32_bf16(a, b, c, 0, 0, 0); }
DI s16x4 trread(unsigned addr) { return __builtin_amdgcn_ds_read_tr16_b64_v4i16((LAS s16x4*)addr); }
DI bf16x8 join(s16x4 lo, s16x4 hi) { return __builtin_bit_cast(bf16x8, __builtin_shufflevector(lo, hi, 0, 1, 2, 3, 4, 5, 6, 7)); }
template<class T> DI T ldsld(unsigned addr) { return *(const LAS T*)addr; }
template<class T> DI void ldsst(unsigned addr, T v) { *(LAS T*)addr = v; }
template<class F> DI float red4(float x, F f) {
  const unsigned u = __float_as_uint(x);
  auto a = __builtin_amdgcn_permlane16_swap(u, u, false, false);
  const float y = f(__uint_as_float(a[0]), __uint_as_float(a[1]));
  const unsigned v = __float_as_uint(y);
  auto b = __builtin_amdgcn_permlane32_swap(v, v, false, false);
  return f(__uint_as_float(b[0]), __uint_as_float(b[1]));
}
DI float sum4(float x) { return red4(x, [](float a, float b) { return a + b; }); }
DI float max4(float x) { return red4(x, [](float a, float b) { return fmaxf(a, b); }); }
DI float min4(float x) { return red4(x, [](float a, float b) { return fminf(a, b); }); }

#define ST(k, i)
#define ST0(k, i)
#define ST4(k, i)
DI void wait_count(GAS unsigned* cnt, unsigned want, GAS unsigned* tmo) {
  for (unsigned spins = 0;; ++spins) {
    if ((unsigned)__builtin_amdgcn_readfirstlane(__hip_atomic_load(cnt, RLX_AGENT)) >= want) break;
    if (spins > (1u << 16)) { __hip_atomic_store(tmo, 1u, RLX_AGENT); break; }
    if ((spins & 255u) == 255u && __builtin_amdgcn_readfirstlane(__hip_atomic_load(tmo, RLX_AGENT))) break;
    __builtin_amdgcn_s_sleep(1);
  }
}

template<int SW> __host__ __device__ constexpr int bmask(int pos) { return SW == 64 ? ((pos >> 1) & 3) : 0; }
__host__ __device__ constexpr int kswap(int k) { return (k & ~12) | ((k & 4) << 1) | ((k & 8) >> 1); }

template<int BM_, int SW_, int NS_, int WM_, int WN_, int ST_, int K_, int LDA_, int LDB_> struct Cfg {
  static constexpr int BM = BM_, SW = SW_, NS = NS_, BN = SW_ * NS_, WM = WM_, WN = WN_, ST = ST_, K = K_, LDA = LDA_, LDB = LDB_;
  static constexpr int NKT = K / 64;
  static constexpr int MT = BM / WM / 16, NT = BN / WN / 16;
  static constexpr int A_BYTES = BM * 128, ROWB = SW * 2, STRIP_BYTES = 64 * ROWB, B_BYTES = NS * STRIP_BYTES, STAGE = A_BYTES + B_BYTES, RING = ST * STAGE;
  static constexpr int PA = A_BYTES / 1024, PB = B_BYTES / 1024, PT = PA + PB, NL = 4;
  static constexpr int PMAX = (PT + NL - 1) / NL, PMIN = PT / NL, NBIG = PT - PMIN * NL;
  static constexpr bool STRIP_PER_WAVE = (NS == WN) && (WN > 1);
  static constexpr int TPS = STRIP_PER_WAVE ? SW / 16 : SW / 16 / WN;
  static constexpr bool SWZ = (SW == 64);
  static constexpr int NV = SWZ ? TPS : 1;
  static constexpr int NM = MT * NT, NR = MT + 2 * NT;
  static_assert(WM * WN == 4 && PA % NL == 0 && STRIP_BYTES % 1024 == 0 && NT == (STRIP_PER_WAVE ? TPS : TPS * NS) && ST >= 3 && NKT >= ST, "config");
  static_assert((ST - 1) * PMAX <= 63, "vmcnt range");
};
template<class C> struct Frags { bf16x8 a[C::MT]; s16x4 bl[C::NT], bh[C::NT]; };

template<class C, int I> DI void read_item(Frags<C>& f, unsigned aA, const unsigned (&aB)[C::NV], int ksimm) {
  if constexpr (I >= 2 && I < 2 + C::MT) {
    constexpr int m = I - 2;
    f.a[m] = ldsld<bf16x8>(aA + m * 2048);
  } else {
    constexpr int r = I < 2 ? I : I - C::MT, j = r >> 1, u = r & 1;
    constexpr int strip = C::STRIP_PER_WAVE ? 0 : j / C::TPS, tj = C::STRIP_PER_WAVE ? j : j % C::TPS;
    constexpr int imm = strip * C::STRIP_BYTES + u * 8 * C::ROWB + (C::SWZ ? 0 : tj * 32);
    const s16x4 v = trread(aB[C::SWZ ? tj : 0] + imm + ksimm);
    if constexpr (u == 0) f.bl[j] = v; else f.bh[j] = v;
  }
}
template<class C, int I> DI void mfma_item(f32x4 (&acc)[C::NT][C::MT], const Frags<C>& f) {
  constexpr int n = I / C::MT, m = I % C::MT;
  acc[n][m] = mfma(join(f.bl[n], f.bh[n]), f.a[m], acc[n][m]);
}
template<class C, int... Is>
DI void step_impl(f32x4 (&acc)[C::NT][C::MT], const Frags<C>& cur, Frags<C>& nxt, unsigned aA, const unsigned (&aB)[C::NV], int ksimm, std::integer_sequence<int, Is...>) {
  ((mfma_item<C, Is>(acc, cur), FENCE(),
    [&] { if constexpr (C::NR <= C::NM) { if constexpr (Is < C::NR) read_item<C, Is>(nxt, aA, aB, ksimm); }
          else { if constexpr (2 * Is < C::NR) read_item<C, 2 * Is>(nxt, aA, aB, ksimm); if constexpr (2 * Is + 1 < C::NR) read_item<C, 2 * Is + 1>(nxt, aA, aB, ksimm); } }(),
    FENCE()), ...);
}
template<class C> DI void step(f32x4 (&acc)[C::NT][C::MT], const Frags<C>& cur, Frags<C>& nxt, unsigned aA, const unsigned (&aB)[C::NV], int ksimm) {
  static_assert(2 * C::NM >= C::NR, "reads per mfma");
  step_impl<C>(acc, cur, nxt, aA, aB, ksimm, std::make_integer_sequence<int, C::NM>{});
}
template<class C, int... Is> DI void read_all_impl(Frags<C>& f, unsigned aA, const unsigned (&aB)[C::NV], std::integer_sequence<int, Is...>) { (read_item<C, Is>(f, aA, aB, 0), ...); }

template<class C, int KID> DI void consumer_loop(f32x4 (&acc)[C::NT][C::MT], unsigned lds0, int wid, int lane, unsigned char* ws) {
  const int wc = wid % C::WN, wr = wid / C::WN;
  const int g = lane >> 4, i16 = lane & 15, q = i16 >> 2, p = lane & 3;
  unsigned aA0, aA1, aB[C::NV];
  { const int row = wr * (C::BM / C::WM) + i16, f = (row >> 1) & 7; aA0 = lds0 + row * 128 + (g ^ f) * 16; aA1 = aA0 ^ 64; }
  { const int pos = (g >> 1) * 16 + (g & 1) * 4 + q;
    const unsigned base = lds0 + C::A_BYTES + (C::STRIP_PER_WAVE ? wc * C::STRIP_BYTES : 0) + pos * C::ROWB + (p >> 1) * 16 + (p & 1) * 8;
    const int t0 = C::STRIP_PER_WAVE ? 0 : wc * C::TPS;
#pragma unroll
    for (int v = 0; v < C::NV; ++v) aB[v] = base + (C::SWZ ? (((t0 + v) ^ bmask<C::SW>(pos)) * 32) : t0 * 32); }
#pragma unroll
  for (int n = 0; n < C::NT; ++n)
#pragma unroll
    for (int m = 0; m < C::MT; ++m) acc[n][m] = (f32x4){0.f, 0.f, 0.f, 0.f};
  Frags<C> F0, F1;
  ST0(KID, 1);
  waitl0(); bar();
  ST0(KID, 2); waitl0();
  read_all_impl<C>(F0, aA0, aB, std::make_integer_sequence<int, C::NR>{});
  FENCE();
  int slot = 0;
#pragma unroll 1
  for (int t = 0; t < C::NKT; ++t) {
    unsigned a, b[C::NV];
    unsigned o = slot * C::STAGE;
    a = aA1 + o;
#pragma unroll
    for (int v = 0; v < C::NV; ++v) b[v] = aB[v] + o;
    step<C>(acc, F0, F1, a, b, 32 * C::ROWB);
    slot = slot + 1 == C::ST ? 0 : slot + 1;
    waitl0(); bar();
    o = slot * C::STAGE;
    a = aA0 + o;
#pragma unroll
    for (int v = 0; v < C::NV; ++v) b[v] = aB[v] + o;
    step<C>(acc, F1, F0, a, b, 0);
  }
  waitl0();
  ST0(KID, 3);
}

template<class C> struct Loader {
  unsigned voff[C::PMAX]; int lw; unsigned lds0; i32x4 rsA, rsB;
  template<class ColFn> DI void init(int lw_, int lane, unsigned lds0_, unsigned a_row0_bytes, ColFn colbyte) {
    lw = lw_; lds0 = lds0_;
#pragma unroll
    for (int i = 0; i < C::PMAX; ++i) {
      const int pi = lw + i * C::NL;
      unsigned v;
      if (i < C::PA / C::NL) { const int row = pi * 8 + (lane >> 3), ch = (lane & 7) ^ ((row >> 1) & 7); v = a_row0_bytes + row * C::LDA + ch * 16; }
      else {
        const int lin = (pi - C::PA) * 64 + lane;
        constexpr int CPR = C::SW / 8, CPS = 64 * CPR;
        const int strip = lin / CPS, r = lin % CPS, pos = r / CPR, chp = r % CPR;
        const int k = kswap(pos), ch = chp ^ (bmask<C::SW>(pos) << 1);
        v = k * C::LDB + colbyte(strip) + ch * 16;
      }
      voff[i] = v;
    }
  }
  template<int I> DI void piece(unsigned slotaddr, unsigned soffA, unsigned soffB) const {
    const int pi = lw + I * C::NL;
    if constexpr (I < C::PA / C::NL) dma16(slotaddr + pi * 1024, voff[I], rsA, soffA);
    else if constexpr (I < C::PMIN) dma16(slotaddr + pi * 1024, voff[I], rsB, soffB);
    else { if (lw < C::NBIG) dma16(slotaddr + pi * 1024, voff[I], rsB, soffB); }
  }
  template<int... Is> DI void tile_impl(int t, int slot, std::integer_sequence<int, Is...>) const { const unsigned sa = lds0 + slot * C::STAGE; (piece<Is>(sa, t * 128, t * (64 * C::LDB)), ...); }
  DI void tile(int t, int slot) const { tile_impl(t, slot, std::make_integer_sequence<int, C::PMAX>{}); }
  template<int TILES> DI void wait() const {
    if constexpr (C::NBIG == 0) waitv<TILES * C::PMIN>();
    else { if (lw < C::NBIG) waitv<TILES * C::PMAX>(); else waitv<TILES * C::PMIN>(); }
  }
};
template<class C, class ColFn> DI void producer_loop(unsigned lds0, int lw, int lane, i32x4 rsA, i32x4 rsB, unsigned a_row0_bytes, ColFn colbyte) {
  Loader<C> L; L.rsA = rsA; L.rsB = rsB;
  L.init(lw, lane, lds0, a_row0_bytes, colbyte);
  L.tile(0, 0); L.tile(1, 1);
  L.template wait<1>(); bar();
#pragma unroll
  for (int s = 2; s < C::ST; ++s) L.tile(s, s);
  int slot = 0;
#pragma unroll 1
  for (int t = 1; t <= C::NKT - C::ST; ++t) {
    L.template wait<C::ST - 2>(); bar();
    L.tile(t - 1 + C::ST, slot);
    slot = slot + 1 == C::ST ? 0 : slot + 1;
  }
  [&]<int... Is>(std::integer_sequence<int, Is...>) {
    ((L.template wait<(C::ST - 2 - Is > 0 ? C::ST - 2 - Is : 0)>(), bar()), ...);
  }(std::make_integer_sequence<int, C::ST>{});
}
DI void tile_of_block(int b, int rx, int& pm, int& pn) {
  const int xcd = b & 7, j = b >> 3, px = 16 / rx, cx = 32 / rx;
  pm = (xcd % px) * rx + j % rx; pn = (xcd / px) * cx + j / rx;
}

__global__ __launch_bounds__(256) void k0_prep(const u16* __restrict__ x, const u16* __restrict__ w_in, unsigned char* __restrict__ ws) {
  const int tid = threadIdx.x, lane = tid & 63, wid = tid >> 6, b = blockIdx.x;
  ST0(0, 0);
  if (b == 255 && tid < 64) *(unsigned*)(ws + WS_CTL + tid * 256) = 0u;
  u32x4 v[2][2];
#pragma unroll
  for (int r = 0; r < 2; ++r)
#pragma unroll
    for (int c = 0; c < 2; ++c) v[r][c] = *(const u32x4*)(x + (size_t)(b * 8 + wid * 2 + r) * DM + c * 512 + lane * 8);
  f32x4 cs0 = {0, 0, 0, 0}, cs1 = {0, 0, 0, 0};
  const int cb = b % 56, kq = b / 56;
  if (b < 224) {
#pragma unroll
    for (int i = 0; i < 8; ++i) {
      const u32x4 w = *(const u32x4*)(w_in + (size_t)(kq * 256 + i * 32 + (tid >> 3)) * FF + cb * 64 + (tid & 7) * 8);
      cs0 += (f32x4){bflo(w[0]), bfhi(w[0]), bflo(w[1]), bfhi(w[1])}; cs1 += (f32x4){bflo(w[2]), bfhi(w[2]), bflo(w[3]), bfhi(w[3])};
    }
  }
#pragma unroll
  for (int r = 0; r < 2; ++r) {
    float f[16]; float s = 0;
#pragma unroll
    for (int c = 0; c < 2; ++c)
#pragma unroll
      for (int j = 0; j < 4; ++j) { f[c * 8 + 2 * j] = bflo(v[r][c][j]); f[c * 8 + 2 * j + 1] = bfhi(v[r][c][j]); }
#pragma unroll
    for (int j = 0; j < 16; ++j) s += f[j];
#pragma unroll
    for (int o = 1; o < 64; o <<= 1) s += __shfl_xor(s, o);
    const float mean = s * (1.0f / DM); float q = 0;
#pragma unroll
    for (int j = 0; j < 16; ++j) { f[j] -= mean; q += f[j] * f[j]; }
#pragma unroll
    for (int o = 1; o < 64; o <<= 1) q += __shfl_xor(q, o);
    const float rstd = 1.0f / sqrtf(q * (1.0f / DM) + 1e-5f);
#pragma unroll
    for (int c = 0; c < 2; ++c) { u32x4 o;
#pragma unroll
      for (int j = 0; j < 4; ++j) o[j] = cvtpk(f[c * 8 + 2 * j] * rstd, f[c * 8 + 2 * j + 1] * rstd);
      *(u32x4*)((u16*)(ws + WS_XN) + (size_t)(b * 8 + wid * 2 + r) * DM + c * 512 + lane * 8) = o; }
  }
  if (b < 224) {
    __shared__ f32x4 red[4][8][2];
#pragma unroll
    for (int o = 8; o < 64; o <<= 1)
#pragma unroll
      for (int j = 0; j < 4; ++j) { cs0[j] += __shfl_xor(cs0[j], o); cs1[j] += __shfl_xor(cs1[j], o); }
    if (lane < 8) { red[wid][lane][0] = cs0; red[wid][lane][1] = cs1; }
    __syncthreads();
    if (tid < 16) { const f32x4 s = red[0][tid >> 1][tid & 1] + red[1][tid >> 1][tid & 1] + red[2][tid >> 1][tid & 1] + red[3][tid >> 1][tid & 1];
      *(f32x4*)(ws + WS_CSUM + ((size_t)kq * FF + cb * 64 + tid * 4) * 4) = s; }
  }
  ST0(0, 7);
}

using C1 = Cfg<128, 64, 3, 2, 2, 3, DM, DM * 2, 3 * DM * 2>;
constexpr int K1_WPHI = C1::RING;
constexpr int K1_LDS = K1_WPHI + 4096;
constexpr int K1_QX = 0;
constexpr int K1_PHI = K1_QX + 16384, PHI_RS = 136, PHI_HEAD = 128 * PHI_RS;
constexpr int K1_V = K1_PHI + 2 * PHI_HEAD, V_RS = 72, V_HEAD = 128 * V_RS;
constexpr int K1_KVT = K1_V + 2 * V_HEAD;
constexpr int K1_ZT = K1_KVT + 8192;
constexpr int K1_OST = K1_ZT + 256, OST_RS = 144;
static_assert(K1_OST + 128 * OST_RS <= C1::RING && K1_OST % 16 == 0 && K1_V % 8 == 0, "K1 epilogue map");

DI void feature_map(const f32x4 p0, const f32x4 p1, u32x4& pos, u32x4& neg) {
  constexpr float L2E = 1.4426950408889634f;
  const float mx = max4(fmaxf(fmaxf(fmaxf(p0[0], p0[1]), fmaxf(p0[2], p0[3])), fmaxf(fmaxf(p1[0], p1[1]), fmaxf(p1[2], p1[3]))));
  const float mn = min4(fminf(fminf(fminf(p0[0], p0[1]), fminf(p0[2], p0[3])), fminf(fminf(p1[0], p1[1]), fminf(p1[2], p1[3]))));
  const float cx = -mx * L2E, cn = mn * L2E;
  float ep[8], en[8], sp = 0.f, sn = 0.f;
#pragma unroll
  for (int j = 0; j < 8; ++j) { const float xv = j < 4 ? p0[j & 3] : p1[j & 3];
    ep[j] = __builtin_amdgcn_exp2f(__builtin_fmaf(xv, L2E, cx)); en[j] = __builtin_amdgcn_exp2f(__builtin_fmaf(xv, -L2E, cn)); sp += ep[j]; sn += en[j]; }
  const float rp = __builtin_amdgcn_rcpf(sum4(sp)), rn = __builtin_amdgcn_rcpf(sum4(sn));
#pragma unroll
  for (int j = 0; j < 4; ++j) { pos[j] = cvtpk(ep[2 * j] * rp, ep[2 * j + 1] * rp); neg[j] = cvtpk(en[2 * j] * rn, en[2 * j + 1] * rn); }
}
DI u32x4 pack8(const f32x4 a, const f32x4 b) { return (u32x4){cvtpk(a[0], a[1]), cvtpk(a[2], a[3]), cvtpk(b[0], b[1]), cvtpk(b[2], b[3])}; }

__global__ __launch_bounds__(512, 2) void k1_qkv(const u16* __restrict__ wqkv, const u16* __restrict__ wphi, unsigned char* __restrict__ ws) {
  extern __shared__ __attribute__((aligned(16))) unsigned char shm[];
  const unsigned lds0 = (unsigned)(size_t)(LAS unsigned char*)shm;
  const int tid = threadIdx.x, lane = tid & 63, wid = __builtin_amdgcn_readfirstlane(tid >> 6);
  const int g = lane >> 4, i16 = lane & 15, q4 = i16 >> 2, p4 = lane & 3;
  ST0(1, 0);
  int pm, pn; tile_of_block(blockIdx.x, 8, pm, pn);
  const int w4 = wid & 3, wr = w4 >> 1, wc = w4 & 1;
  GAS unsigned* cnt = (GAS unsigned*)(ws + WS_CTL) + pn * 64;
  GAS unsigned* tmo = (GAS unsigned*)(ws + WS_CTL) + 32 * 64;
  const __amdgpu_buffer_rsrc_t rsKV = make_rsrc(ws + WS_KVP, 16 * 16 * 8192), rsZ = make_rsrc(ws + WS_ZP, 16 * 16 * 512);
  u32x4 phiq[4][2];
  const unsigned wph = lds0 + K1_WPHI + wc * 2048 + (4 * g + q4) * 64 + p4 * 8;
  if (wid < 4) {
    ldsst<u32x4>(lds0 + K1_WPHI + tid * 16, *(const u32x4*)(wphi + (size_t)pn * 2048 + tid * 8));
    f32x4 acc[C1::NT][C1::MT];
    consumer_loop<C1, 1>(acc, lds0, wid, lane, ws);
#pragma unroll
    for (int m = 0; m < 4; ++m) ldsst<u32x4>(lds0 + K1_QX + (w4 * 4 + m) * 1024 + lane * 16, pack8(acc[0][m], acc[1][m]));
    lbar();
    const bf16x8 wf0 = join(trread(wph), trread(wph + 1024)), wf1 = join(trread(wph + 32), trread(wph + 1024 + 32));
#pragma unroll
    for (int m = 0; m < 4; ++m) {
      const bf16x8 kf = asbf(pack8(acc[2][m], acc[3][m]));
      const f32x4 z4 = {0.f, 0.f, 0.f, 0.f};
      u32x4 pos, neg; feature_map(mfma(wf0, kf, z4), mfma(wf1, kf, z4), pos, neg);
      const int tok = wr * 64 + m * 16 + i16;
      const unsigned pa = lds0 + K1_PHI + wc * PHI_HEAD + tok * PHI_RS + g * 8;
      ldsst<u32x2>(pa, (u32x2){pos[0], pos[1]}); ldsst<u32x2>(pa + 32, (u32x2){pos[2], pos[3]});
      ldsst<u32x2>(pa + 64, (u32x2){neg[0], neg[1]}); ldsst<u32x2>(pa + 96, (u32x2){neg[2], neg[3]});
      const u32x4 vv = pack8(acc[4][m], acc[5][m]);
      const unsigned va = lds0 + K1_V + wc * V_HEAD + tok * V_RS + g * 8;
      ldsst<u32x2>(va, (u32x2){vv[0], vv[1]}); ldsst<u32x2>(va + 32, (u32x2){vv[2], vv[3]});
    }
    lbar();
    f32x4 d[2][2], dz[2];
#pragma unroll
    for (int a = 0; a < 2; ++a) { dz[a] = (f32x4){0.f, 0.f, 0.f, 0.f}; d[a][0] = dz[a]; d[a][1] = dz[a]; }
    const bf16x8 ones = asbf((u32x4){0x3f803f80u, 0x3f803f80u, 0x3f803f80u, 0x3f803f80u});
    const unsigned pr = lds0 + K1_PHI + wc * PHI_HEAD + (4 * g + q4) * PHI_RS + wr * 64 + p4 * 8;
    const unsigned vr = lds0 + K1_V + wc * V_HEAD + (4 * g + q4) * V_RS + p4 * 8;
#pragma unroll
    for (int s = 0; s < 4; ++s) {
      bf16x8 pf[2], vf[2];
#pragma unroll
      for (int a = 0; a < 2; ++a) { pf[a] = join(trread(pr + s * 32 * PHI_RS + a * 32), trread(pr + (s * 32 + 16) * PHI_RS + a * 32));
                                    vf[a] = join(trread(vr + s * 32 * V_RS + a * 32), trread(vr + (s * 32 + 16) * V_RS + a * 32)); }
#pragma unroll
      for (int a = 0; a < 2; ++a) { d[a][0] = mfma(pf[a], vf[0], d[a][0]); d[a][1] = mfma(pf[a], vf[1], d[a][1]); dz[a] = mfma(pf[a], ones, dz[a]); }
    }
    const unsigned kvoff = ((pn * 16 + pm) * 2 + wc) * 4096 + wr * 2048 + lane * 16;
#pragma unroll
    for (int dt = 0; dt < 2; ++dt) __builtin_amdgcn_raw_buffer_store_b128(pack8(d[0][dt], d[1][dt]), rsKV, kvoff + dt * 1024, 0, 16);
    if (i16 < 2) __builtin_amdgcn_raw_buffer_store_b128(__builtin_bit_cast(u32x4, i16 == 0 ? dz[0] : dz[1]), rsZ, ((pn * 16 + pm) * 2 + wc) * 256 + wr * 128 + g * 32 + i16 * 16, 0, 16);
    ST0(1, 4);
    waitv<0>(); bar();
    if (wid == 0) {
      if (lane == 0) __hip_atomic_fetch_add(cnt, 1u, RLX_AGENT);
      wait_count(cnt, 16u, tmo);
    }
    bar();
    ST0(1, 5);
  } else {
    producer_loop<C1>(lds0, w4, lane, make_srd(ws + WS_XN, SEQ * DM * 2), make_srd(wqkv, DM * 3 * DM * 2), (unsigned)pm * 128 * C1::LDA,
                      [&](int strip) { return (unsigned)(strip * DM + pn * 64) * 2; });
    bar();
    const bf16x8 wf0 = join(trread(wph), trread(wph + 1024)), wf1 = join(trread(wph + 32), trread(wph + 1024 + 32));
#pragma unroll
    for (int m = 0; m < 4; ++m) {
      const bf16x8 qf = ldsld<bf16x8>(lds0 + K1_QX + (w4 * 4 + m) * 1024 + lane * 16);
      const f32x4 z4 = {0.f, 0.f, 0.f, 0.f};
      feature_map(mfma(wf0, qf, z4), mfma(wf1, qf, z4), phiq[m][0], phiq[m][1]);
    }
    bar(); bar(); bar();
  }
  { const int head = wid & 1, fr = wid >> 1;
    u32x4 pv[16];
#pragma unroll
    for (int p = 0; p < 16; ++p) pv[p] = __builtin_amdgcn_raw_buffer_load_b128(rsKV, ((pn * 16 + p) * 2 + head) * 4096 + fr * 1024 + lane * 16, 0, 16);
    float zs = 0.f;
    if (wid < 2) {
#pragma unroll
      for (int p = 0; p < 16; ++p) zs += __uint_as_float(__builtin_amdgcn_raw_buffer_load_b32(rsZ, ((pn * 16 + p) * 2 + head) * 256 + lane * 4, 0, 16));
    }
    f32x4 s0 = {0.f, 0.f, 0.f, 0.f}, s1 = s0;
#pragma unroll
    for (int p = 0; p < 16; ++p) { s0 += (f32x4){bflo(pv[p][0]), bfhi(pv[p][0]), bflo(pv[p][1]), bfhi(pv[p][1])}; s1 += (f32x4){bflo(pv[p][2]), bfhi(pv[p][2]), bflo(pv[p][3]), bfhi(pv[p][3])}; }
    ldsst<u32x4>(lds0 + K1_KVT + (head * 4 + fr) * 1024 + lane * 16, pack8(s0, s1));
    if (wid < 2) ldsst<u16>(lds0 + K1_ZT + head * 128 + lane * 2, (u16)cvtpk(zs, 0.f));
  }
  lbar();
  ST0(1, 6);
  if (wid >= 4) {
    bf16x8 kv[2][2], zf[2];
#pragma unroll
    for (int ks = 0; ks < 2; ++ks) { zf[ks] = ldsld<bf16x8>(lds0 + K1_ZT + wc * 128 + ks * 64 + g * 16);
#pragma unroll
      for (int dt = 0; dt < 2; ++dt) kv[ks][dt] = ldsld<bf16x8>(lds0 + K1_KVT + (wc * 4 + ks * 2 + dt) * 1024 + lane * 16); }
#pragma unroll
    for (int m = 0; m < 4; ++m) {
      const f32x4 z4 = {0.f, 0.f, 0.f, 0.f};
      const bf16x8 b0 = asbf(phiq[m][0]), b1 = asbf(phiq[m][1]);
      const f32x4 den = mfma(zf[1], b1, mfma(zf[0], b0, z4));
      const f32x4 n0 = mfma(kv[1][0], b1, mfma(kv[0][0], b0, z4)), n1 = mfma(kv[1][1], b1, mfma(kv[0][1], b0, z4));
      const float r = 1.0f / (den[0] + 1e-6f);
      const unsigned oa = lds0 + K1_OST + (wr * 64 + m * 16 + i16) * OST_RS + wc * 64 + g * 8;
      ldsst<u32x2>(oa, (u32x2){cvtpk(n0[0] * r, n0[1] * r), cvtpk(n0[2] * r, n0[3] * r)});
      ldsst<u32x2>(oa + 32, (u32x2){cvtpk(n1[0] * r, n1[1] * r), cvtpk(n1[2] * r, n1[3] * r)});
    }
  }
  lbar();
#pragma unroll
  for (int i = 0; i < 2; ++i) { const int c = tid + i * 512, row = c >> 3, ch = c & 7;
    *(u32x4*)(ws + WS_O + (size_t)(pm * 128 + row) * (DM * 2) + pn * 128 + ch * 16) = ldsld<u32x4>(lds0 + K1_OST + row * OST_RS + ch * 16); }
  ST0(1, 7);
}

template<class C, bool STATS, int KID> DI void res_gemm(const void* A, unsigned a_bytes, const u16* W, unsigned w_bytes, const u16* res, u16* out, f32x2* stats, int rx, unsigned char* ws) {
  extern __shared__ __attribute__((aligned(16))) unsigned char shm[];
  const unsigned lds0 = (unsigned)(size_t)(LAS unsigned char*)shm;
  const int tid = threadIdx.x, lane = tid & 63, wid = __builtin_amdgcn_readfirstlane(tid >> 6);
  ST0(KID, 0);
  int pm, pn; tile_of_block(blockIdx.x, rx, pm, pn);
  if (wid >= 4) {
    producer_loop<C>(lds0, wid - 4, lane, make_srd(A, a_bytes), make_srd(W, w_bytes), (unsigned)pm * 128 * C::LDA, [&](int) { return (unsigned)pn * 128; });
    bar();
    return;
  }
  u32x4 rv[4];
#pragma unroll
  for (int i = 0; i < 4; ++i) { const int c = tid + i * 256; rv[i] = *(const u32x4*)(res + (size_t)(pm * 128 + (c >> 3)) * DM + pn * 64 + (c & 7) * 8); }
  f32x4 acc[C::NT][C::MT];
  consumer_loop<C, KID>(acc, lds0, wid, lane, ws);
  const int g = lane >> 4, i16 = lane & 15, wr = wid >> 1, wc = wid & 1;
#pragma unroll
  for (int n = 0; n < 2; ++n)
#pragma unroll
    for (int m = 0; m < 4; ++m)
      ldsst<u32x2>(lds0 + (wr * 64 + m * 16 + i16) * OST_RS + (wc * 32 + n * 16 + g * 4) * 2, (u32x2){cvtpk(acc[n][m][0], acc[n][m][1]), cvtpk(acc[n][m][2], acc[n][m][3])});
  lbar();
#pragma unroll
  for (int i = 0; i < 4; ++i) { const int c = tid + i * 256, row = c >> 3, ch = c & 7;
    const u32x4 a = ldsld<u32x4>(lds0 + row * OST_RS + ch * 16); u32x4 o; float s = 0.f, qq = 0.f;
#pragma unroll
    for (int j = 0; j < 4; ++j) { o[j] = cvtpk(bflo(rv[i][j]) + bflo(a[j]), bfhi(rv[i][j]) + bfhi(a[j]));
      if (STATS) { const float lo = bflo(o[j]), hi = bfhi(o[j]); s += lo + hi; qq += lo * lo + hi * hi; } }
    *(u32x4*)(out + (size_t)(pm * 128 + row) * DM + pn * 64 + ch * 8) = o;
    if (STATS) {
#pragma unroll
      for (int of = 1; of < 8; of <<= 1) { s += __shfl_xor(s, of); qq += __shfl_xor(qq, of); }
      if (ch == 0) stats[(pm * 16 + pn) * 128 + row] = (f32x2){s, qq};
    }
  }
  ST0(KID, 7);
}
using C2 = Cfg<128, 64, 1, 2, 2, 5, DM, DM * 2, DM * 2>;
using C5 = Cfg<128, 64, 1, 2, 2, 5, FH, FH * 2, DM * 2>;
__global__ __launch_bounds__(512, 2) void k2_wo(const u16* __restrict__ x, const u16* __restrict__ wo, unsigned char* __restrict__ ws) {
  res_gemm<C2, true, 2>(ws + WS_O, SEQ * DM * 2, wo, DM * DM * 2, x, (u16*)(ws + WS_H), (f32x2*)(ws + WS_HST), 4, ws);
}
__global__ __launch_bounds__(512, 2) void k5_out(const u16* __restrict__ w_out, unsigned char* __restrict__ ws, u16* __restrict__ out) {
  res_gemm<C5, false, 5>(ws + WS_G, SEQ * FH * 2, w_out, FH * DM * 2, (const u16*)(ws + WS_H), out, nullptr, 4, ws);
}

using C3 = Cfg<128, 112, 2, 2, 2, 3, DM, DM * 2, FF * 2>;
constexpr int K3_RST = C3::RING;
constexpr int K3_CS = K3_RST + 1024;
constexpr int K3_ZS = K3_CS + 1024;
constexpr int K3_LDS = K3_ZS + 1024;
constexpr int K3_XF = 0;
constexpr int K3_STG = 32768, STG_RS = 240;
DI float gelu_tanh(float x) {
  constexpr float C0 = -2.0f * 0.7978845608028654f * 1.4426950408889634f, C1_ = C0 * 0.044715f;
  const float e = __builtin_amdgcn_exp2f(x * __builtin_fmaf(x * x, C1_, C0));
  return x * __builtin_amdgcn_rcpf(1.0f + e);
}
__global__ __launch_bounds__(512, 2) void k3_in(const u16* __restrict__ w_in, unsigned char* __restrict__ ws) {
  extern __shared__ __attribute__((aligned(16))) unsigned char shm[];
  const unsigned lds0 = (unsigned)(size_t)(LAS unsigned char*)shm;
  const int tid = threadIdx.x, lane = tid & 63, wid = __builtin_amdgcn_readfirstlane(tid >> 6);
  const int g = lane >> 4, i16 = lane & 15;
  ST0(3, 0);
  int pm, pn; tile_of_block(blockIdx.x, 8, pm, pn);
  const int w4 = wid & 3, wr = w4 >> 1, wc = w4 & 1, mp = wid >> 2;
  GAS unsigned* cnt = (GAS unsigned*)(ws + WS_CTL) + (16 + pm) * 64;
  GAS unsigned* tmo = (GAS unsigned*)(ws + WS_CTL) + 32 * 64;
  u32x2 z[2][7];
  if (wid < 4) {
    if (tid < 128) { const f32x2* st = (const f32x2*)(ws + WS_HST) + (size_t)pm * 16 * 128 + tid; f32x2 s = {0.f, 0.f};
#pragma unroll
      for (int p = 0; p < 16; ++p) s += st[p * 128];
      const float mean = s[0] * (1.0f / DM), var = fmaxf(s[1] * (1.0f / DM) - mean * mean, 0.f);
      ldsst<f32x2>(lds0 + K3_RST + tid * 8, (f32x2){mean, 1.0f / sqrtf(var + 1e-5f)}); }
    if (tid < 224) { const float* cs = (const float*)(ws + WS_CSUM) + (tid < 112 ? pn * 112 + tid : FH + pn * 112 + tid - 112);
      ldsst<float>(lds0 + K3_CS + (tid < 112 ? tid : tid + 16) * 4, cs[0] + cs[FF] + cs[2 * FF] + cs[3 * FF]); }
    f32x4 acc[C3::NT][C3::MT];
    consumer_loop<C3, 3>(acc, lds0, wid, lane, ws);
    f32x2 rs[4];
#pragma unroll
    for (int m = 0; m < 4; ++m) rs[m] = ldsld<f32x2>(lds0 + K3_RST + (wr * 64 + m * 16 + i16) * 8);
#pragma unroll
    for (int n = 0; n < 7; ++n) { const f32x4 cs = ldsld<f32x4>(lds0 + K3_CS + wc * 512 + (n * 16 + g * 4) * 4);
#pragma unroll
      for (int m = 0; m < 4; ++m) { const f32x4 v = (acc[n][m] - cs * rs[m][0]) * rs[m][1]; const u32x2 w = {cvtpk(v[0], v[1]), cvtpk(v[2], v[3])};
        if (m < 2) z[m][n] = w; else ldsst<u32x2>(lds0 + K3_XF + ((w4 * 2 + m - 2) * 7 + n) * 512 + lane * 8, w); } }
    lbar();
  } else {
    producer_loop<C3>(lds0, w4, lane, make_srd(ws + WS_H, SEQ * DM * 2), make_srd(w_in, DM * FF * 2), (unsigned)pm * 128 * C3::LDA,
                      [&](int strip) { return (unsigned)(strip * FH + pn * 112) * 2; });
    bar();
#pragma unroll
    for (int m = 0; m < 2; ++m)
#pragma unroll
      for (int n = 0; n < 7; ++n) z[m][n] = ldsld<u32x2>(lds0 + K3_XF + ((w4 * 2 + m) * 7 + n) * 512 + lane * 8);
  }
  float s[2] = {0.f, 0.f}, qq[2] = {0.f, 0.f};
#pragma unroll
  for (int m = 0; m < 2; ++m)
#pragma unroll
    for (int n = 0; n < 7; ++n) {
      const u32x2 w = {cvtpk(gelu_tanh(bflo(z[m][n][0])), gelu_tanh(bfhi(z[m][n][0]))), cvtpk(gelu_tanh(bflo(z[m][n][1])), gelu_tanh(bfhi(z[m][n][1])))};
      z[m][n] = w;
      if (wc == 1) { const float a = bflo(w[0]), b = bfhi(w[0]), c = bflo(w[1]), d = bfhi(w[1]); s[m] += (a + b) + (c + d); qq[m] += (a * a + b * b) + (c * c + d * d); }
    }
  const int row0 = wr * 64 + mp * 32;
  if (wc == 0) {
    unsigned char* z1 = ws + WS_Z1 + ((size_t)((pm * 16 + pn) * 4 + wr * 2 + mp) * 7) * 1024 + lane * 16;
#pragma unroll
    for (int n = 0; n < 7; ++n) *(u32x4*)(z1 + n * 1024) = (u32x4){z[0][n][0], z[0][n][1], z[1][n][0], z[1][n][1]};
  } else {
#pragma unroll
    for (int m = 0; m < 2; ++m) { const float a = sum4(s[m]), b = sum4(qq[m]);
      if (g == 0) __hip_atomic_store((GAS unsigned long long*)(ws + WS_ZST) + (pm * 16 + pn) * 128 + row0 + m * 16 + i16,
                                     ((unsigned long long)__float_as_uint(b) << 32) | __float_as_uint(a), RLX_AGENT); }
    waitv<0>();
  }
  ST0(3, 4);
  bar();
  if (wid == 0) {
    if (lane == 0) __hip_atomic_fetch_add(cnt, 1u, RLX_AGENT);
    wait_count(cnt, 16u, tmo);
  }
  bar();
  ST0(3, 5);
  if (tid < 128) { f32x2 t = {0.f, 0.f};
#pragma unroll
    for (int p = 0; p < 16; ++p) { const unsigned long long w = __hip_atomic_load((GAS unsigned long long*)(ws + WS_ZST) + (pm * 16 + p) * 128 + tid, RLX_AGENT);
      t += (f32x2){__uint_as_float((unsigned)w), __uint_as_float((unsigned)(w >> 32))}; }
    const float mean = t[0] * (1.0f / FH), var = fmaxf(t[1] * (1.0f / FH) - mean * mean, 0.f);
    ldsst<f32x2>(lds0 + K3_ZS + tid * 8, (f32x2){mean, 1.0f / sqrtf(var + 1e-5f)}); }
  lbar();
  ST0(3, 6);
  if (wc == 1) {
    const unsigned stg = lds0 + K3_STG + (wr * 2 + mp) * 32 * STG_RS;
#pragma unroll
    for (int m = 0; m < 2; ++m) { const f32x2 ms = ldsld<f32x2>(lds0 + K3_ZS + (row0 + m * 16 + i16) * 8);
#pragma unroll
      for (int n = 0; n < 7; ++n)
        ldsst<u32x2>(stg + (m * 16 + i16) * STG_RS + n * 32 + g * 8,
                     (u32x2){cvtpk((bflo(z[m][n][0]) - ms[0]) * ms[1], (bfhi(z[m][n][0]) - ms[0]) * ms[1]), cvtpk((bflo(z[m][n][1]) - ms[0]) * ms[1], (bfhi(z[m][n][1]) - ms[0]) * ms[1])}); }
    waitl0();
    unsigned char* z2 = ws + WS_Z2 + ((size_t)pn * SEQ + pm * 128 + row0) * 224;
#pragma unroll
    for (int i = 0; i < 7; ++i) { const int c = i * 64 + lane, r = c / 14, ch = c % 14;
      *(u32x4*)(z2 + c * 16) = ldsld<u32x4>(stg + r * STG_RS + ch * 16); }
  }
  if (wid == 1) ST(3, 7);
}

using C4 = Cfg<128, 112, 1, 4, 1, 4, SEQ, SEQ * 2, 224>;
__global__ __launch_bounds__(512, 2) void k4_sp(const u16* __restrict__ w_sp, const u16* __restrict__ b_sp, unsigned char* __restrict__ ws) {
  extern __shared__ __attribute__((aligned(16))) unsigned char shm[];
  const unsigned lds0 = (unsigned)(size_t)(LAS unsigned char*)shm;
  const int tid = threadIdx.x, lane = tid & 63, wid = __builtin_amdgcn_readfirstlane(tid >> 6);
  ST0(4, 0);
  int pm, pn; tile_of_block(blockIdx.x, 4, pm, pn);
  if (wid >= 4) {
    producer_loop<C4>(lds0, wid - 4, lane, make_srd(w_sp, SEQ * SEQ * 2), make_srd(ws + WS_Z2, SEQ * FH * 2), (unsigned)pm * 128 * C4::LDA, [&](int) { return (unsigned)pn * (SEQ * 224); });
    return;
  }
  const int g = lane >> 4, i16 = lane & 15;
  u32x4 z1[7];
  const unsigned char* z1p = ws + WS_Z1 + ((size_t)((pm * 16 + pn) * 4 + wid) * 7) * 1024 + lane * 16;
#pragma unroll
  for (int n = 0; n < 7; ++n) z1[n] = *(const u32x4*)(z1p + n * 1024);
  float bs[2];
#pragma unroll
  for (int m = 0; m < 2; ++m) bs[m] = bflo(b_sp[pm * 128 + wid * 32 + m * 16 + i16]);
  f32x4 acc[C4::NT][C4::MT];
  consumer_loop<C4, 4>(acc, lds0, wid, lane, ws);
  bar();
  const unsigned stg = lds0 + wid * 32 * STG_RS;
#pragma unroll
  for (int m = 0; m < 2; ++m)
#pragma unroll
    for (int n = 0; n < 7; ++n) { const unsigned a = z1[n][2 * m], b = z1[n][2 * m + 1]; const f32x4 v = acc[n][m] + bs[m];
      ldsst<u32x2>(stg + (m * 16 + i16) * STG_RS + n * 32 + g * 8, (u32x2){cvtpk(bflo(a) * v[0], bfhi(a) * v[1]), cvtpk(bflo(b) * v[2], bfhi(b) * v[3])}); }
  waitl0();
  unsigned char* gp = ws + WS_G + (size_t)(pm * 128 + wid * 32) * (FH * 2) + pn * 224;
#pragma unroll
  for (int i = 0; i < 7; ++i) { const int c = i * 64 + lane, r = c / 14, ch = c % 14;
    *(u32x4*)(gp + (size_t)r * (FH * 2) + ch * 16) = ldsld<u32x4>(stg + r * STG_RS + ch * 16); }
  ST0(4, 7);
}
}

extern "C" void kernel_launch(void* const* d_in, const int* in_sizes, int n_in, void* d_out, int out_size, void* d_ws, size_t ws_size, hipStream_t stream) {
  const u16* x = (const u16*)d_in[0];
  const u16* wqkv = (const u16*)d_in[1];
  const u16* wphi = (const u16*)d_in[2];
  const u16* wo = (const u16*)d_in[3];
  const u16* w_in = (const u16*)d_in[5];
  const u16* w_sp = (const u16*)d_in[6];
  const u16* b_sp = (const u16*)d_in[7];
  const u16* w_out = (const u16*)d_in[8];
  unsigned char* ws = (unsigned char*)d_ws;
  u16* out = (u16*)d_out;
  (void)in_sizes; (void)n_in; (void)out_size; (void)ws_size;
  (void)hipFuncSetAttribute((const void*)k1_qkv, hipFuncAttributeMaxDynamicSharedMemorySize, K1_LDS);
  (void)hipFuncSetAttribute((const void*)k2_wo, hipFuncAttributeMaxDynamicSharedMemorySize, C2::RING);
  (void)hipFuncSetAttribute((const void*)k3_in, hipFuncAttributeMaxDynamicSharedMemorySize, K3_LDS);
  (void)hipFuncSetAttribute((const void*)k4_sp, hipFuncAttributeMaxDynamicSharedMemorySize, C4::RING);
  (void)hipFuncSetAttribute((const void*)k5_out, hipFuncAttributeMaxDynamicSharedMemorySize, C5::RING);
  k0_prep<<<256, 256, 0, stream>>>(x, w_in, ws);
  k1_qkv<<<256, 512, K1_LDS, stream>>>(wqkv, wphi, ws);
  k2_wo<<<256, 512, C2::RING, stream>>>(x, wo, ws);
  k3_in<<<256, 512, K3_LDS, stream>>>(w_in, ws);
  k4_sp<<<256, 512, C4::RING, stream>>>(w_sp, b_sp, ws);
  k5_out<<<256, 512, C5::RING, stream>>>(w_out, ws, out);
}
```

```cpp
#include <hip/hip_runtime.h>
#include <hip/hip_bf16.h>
#include <utility>

namespace {
typedef short s16x4 __attribute__((ext_vector_type(4)));
typedef short s16x8 __attribute__((ext_vector_type(8)));
typedef __bf16 bf16x8 __attribute__((ext_vector_type(8)));
typedef __bf16 bf16x2 __attribute__((ext_vector_type(2)));
typedef float f32x2 __attribute__((ext_vector_type(2)));
typedef float f32x4 __attribute__((ext_vector_type(4)));
typedef int i32x4 __attribute__((ext_vector_type(4)));
typedef unsigned u32x4 __attribute__((ext_vector_type(4)));
typedef unsigned u32x2 __attribute__((ext_vector_type(2)));
typedef unsigned short u16;
#define LAS __attribute__((address_space(3)))
#define GAS __attribute__((address_space(1)))
#define DI __device__ __forceinline__
#define FENCE() __builtin_amdgcn_sched_barrier(0)
#define RLX_AGENT __ATOMIC_RELAXED, __HIP_MEMORY_SCOPE_AGENT

constexpr int SEQ = 2048, DM = 1024, FH = 1792, FF = 3584;
constexpr size_t WS_CTL = 0;
constexpr size_t CTL_BYTES = 64 * 256;
constexpr size_t WS_XN = CTL_BYTES;
constexpr size_t WS_CSUM = WS_XN + (size_t)SEQ * DM * 2;
constexpr size_t WS_KVP = WS_CSUM + 4 * FF * 4;
constexpr size_t WS_ZP = WS_KVP + (size_t)16 * 16 * 8192;
constexpr size_t WS_O = WS_ZP + 16 * 16 * 512;
constexpr size_t WS_H = WS_O + (size_t)SEQ * DM * 2;
constexpr size_t WS_HST = WS_H + (size_t)SEQ * DM * 2;
constexpr size_t WS_Z1 = WS_HST + 16 * 16 * 128 * 8;
constexpr size_t WS_Z2 = WS_Z1 + (size_t)SEQ * FH * 2;
constexpr size_t WS_ZST = WS_Z2 + (size_t)SEQ * FH * 2;
constexpr size_t WS_G = WS_ZST + 16 * 16 * 128 * 8;
constexpr size_t WS_END = WS_G + (size_t)SEQ * FH * 2;

DI i32x4 make_srd(const void* p, unsigned bytes) {
  const unsigned long long a = (unsigned long long)p; i32x4 rs;
  rs[0] = __builtin_amdgcn_readfirstlane((int)(unsigned)a); rs[1] = __builtin_amdgcn_readfirstlane((int)(unsigned)(a >> 32)) & 0xffff;
  rs[2] = (int)bytes; rs[3] = 0x00020000; return rs;
}
DI __amdgpu_buffer_rsrc_t make_rsrc(const void* p, unsigned bytes) { return __builtin_amdgcn_make_buffer_rsrc(const_cast<void*>(p), 0, bytes, 0x00020000); }
DI void dma16(unsigned ldsaddr, unsigned voff, i32x4 rs, unsigned soff) {
  asm volatile("s_mov_b32 m0, %0\n\ts_nop 0\n\tbuffer_load_dwordx4 %1, %2, %3 offen lds" :: "s"(ldsaddr), "v"(voff), "s"(rs), "s"(soff));
}
template<int N> DI void waitv() { asm volatile("s_waitcnt vmcnt(%0)" :: "i"(N) : "memory"); }
DI void waitl0() { asm volatile("" ::: "memory"); __builtin_amdgcn_s_waitcnt(0xC07F); asm volatile("" ::: "memory"); }
DI void bar() { asm volatile("s_barrier" ::: "memory"); }
DI void lbar() { waitl0(); bar(); }
DI unsigned cvtpk(float lo, float hi) { f32x2 v = {lo, hi}; bf16x2 b = __builtin_convertvector(v, bf16x2); return __builtin_bit_cast(unsigned, b); }
DI float bflo(unsigned u) { return __uint_as_float(u << 16); }
DI float bfhi(unsigned u) { return __uint_as_float(u & 0xffff0000u); }
DI bf16x8 asbf(u32x4 v) { return __builtin_bit_cast(bf16x8, v); }
DI f32x4 mfma(bf16x8 a, bf16x8 b, f32x4 c) { return __builtin_amdgcn_mfma_f32_16x16x32_bf16(a, b, c, 0, 0, 0); }
DI s16x4 trread(unsigned addr) { return __builtin_amdgcn_ds_read_tr16_b64_v4i16((LAS s16x4*)addr); }
DI bf16x8 join(s16x4 lo, s16x4 hi) { return __builtin_bit_cast(bf16x8, __builtin_shufflevector(lo, hi, 0, 1, 2, 3, 4, 5, 6, 7)); }
template<class T> DI T ldsld(unsigned addr) { return *(const LAS T*)addr; }
template<class T> DI void ldsst(unsigned addr, T v) { *(LAS T*)addr = v; }
template<class F> DI float red4(float x, F f) {
  const unsigned u = __float_as_uint(x);
  auto a = __builtin_amdgcn_permlane16_swap(u, u, false, false);
  const float y = f(__uint_as_float(a[0]), __uint_as_float(a[1]));
  const unsigned v = __float_as_uint(y);
  auto b = __builtin_amdgcn_permlane32_swap(v, v, false, false);
  return f(__uint_as_float(b[0]), __uint_as_float(b[1]));
}
DI float sum4(float x) { return red4(x, [](float a, float b) { return a + b; }); }
DI float max4(float x) { return red4(x, [](float a, float b) { return fmaxf(a, b); }); }
DI float min4(float x) { return red4(x, [](float a, float b) { return fminf(a, b); }); }

#define ST(k, i)
#define ST0(k, i)
#define ST4(k, i)
DI void wait_count(GAS unsigned* cnt, unsigned want, GAS unsigned* tmo) {
  for (unsigned spins = 0;; ++spins) {
    if ((unsigned)__builtin_amdgcn_readfirstlane(__hip_atomic_load(cnt, RLX_AGENT)) >= want) break;
    if (spins > (1u << 16)) { __hip_atomic_store(tmo, 1u, RLX_AGENT); break; }
    if ((spins & 255u) == 255u && __builtin_amdgcn_readfirstlane(__hip_atomic_load(tmo, RLX_AGENT))) break;
    __builtin_amdgcn_s_sleep(1);
  }
}

template<int SW> __host__ __device__ constexpr int bmask(int pos) { return SW == 64 ? ((pos >> 1) & 3) : 0; }
__host__ __device__ constexpr int kswap(int k) { return (k & ~12) | ((k & 4) << 1) | ((k & 8) >> 1); }

template<int BM_, int SW_, int NS_, int WM_, int WN_, int ST_, int K_, int LDA_, int LDB_> struct Cfg {
  static constexpr int BM = BM_, SW = SW_, NS = NS_, BN = SW_ * NS_, WM = WM_, WN = WN_, ST = ST_, K = K_, LDA = LDA_, LDB = LDB_;
  static constexpr int NKT = K / 64;
  static constexpr int MT = BM / WM / 16, NT = BN / WN / 16;
  static constexpr int A_BYTES = BM * 128, ROWB = SW * 2, STRIP_BYTES = 64 * ROWB, B_BYTES = NS * STRIP_BYTES, STAGE = A_BYTES + B_BYTES, RING = ST * STAGE;
  static constexpr int PA = A_BYTES / 1024, PB = B_BYTES / 1024, PT = PA + PB, NL = 4;
  static constexpr int PMAX = (PT + NL - 1) / NL, PMIN = PT / NL, NBIG = PT - PMIN * NL;
  static constexpr bool STRIP_PER_WAVE = (NS == WN) && (WN > 1);
  static constexpr int TPS = STRIP_PER_WAVE ? SW / 16 : SW / 16 / WN;
  static constexpr bool SWZ = (SW == 64);
  static constexpr int NV = SWZ ? TPS : 1;
  static constexpr int NM = MT * NT, NR = MT + 2 * NT;
  static_assert(WM * WN == 4 && PA % NL == 0 && STRIP_BYTES % 1024 == 0 && NT == (STRIP_PER_WAVE ? TPS : TPS * NS) && ST >= 3 && NKT >= ST, "config");
  static_assert((ST - 1) * PMAX <= 63, "vmcnt range");
};
template<class C> struct Frags { bf16x8 a[C::MT]; s16x4 bl[C::NT], bh[C::NT]; };

template<class C, int I> DI void read_item(Frags<C>& f, unsigned aA, const unsigned (&aB)[C::NV], int ksimm) {
  if constexpr (I >= 2 && I < 2 + C::MT) {
    constexpr int m = I - 2;
    f.a[m] = ldsld<bf16x8>(aA + m * 2048);
  } else {
    constexpr int r = I < 2 ? I : I - C::MT, j = r >> 1, u = r & 1;
    constexpr int strip = C::STRIP_PER_WAVE ? 0 : j / C::TPS, tj = C::STRIP_PER_WAVE ? j : j % C::TPS;
    constexpr int imm = strip * C::STRIP_BYTES + u * 8 * C::ROWB + (C::SWZ ? 0 : tj * 32);
    const s16x4 v = trread(aB[C::SWZ ? tj : 0] + imm + ksimm);
    if constexpr (u == 0) f.bl[j] = v; else f.bh[j] = v;
  }
}
template<class C, int I> DI void mfma_item(f32x4 (&acc)[C::NT][C::MT], const Frags<C>& f) {
  constexpr int n = I / C::MT, m = I % C::MT;
  acc[n][m] = mfma(join(f.bl[n], f.bh[n]), f.a[m], acc[n][m]);
}
template<class C, int... Is>
DI void step_impl(f32x4 (&acc)[C::NT][C::MT], const Frags<C>& cur, Frags<C>& nxt, unsigned aA, const unsigned (&aB)[C::NV], int ksimm, std::integer_sequence<int, Is...>) {
  ((mfma_item<C, Is>(acc, cur), FENCE(),
    [&] { if constexpr (C::NR <= C::NM) { if constexpr (Is < C::NR) read_item<C, Is>(nxt, aA, aB, ksimm); }
          else { if constexpr (2 * Is < C::NR) read_item<C, 2 * Is>(nxt, aA, aB, ksimm); if constexpr (2 * Is + 1 < C::NR) read_item<C, 2 * Is + 1>(nxt, aA, aB, ksimm); } }(),
    FENCE()), ...);
}
template<class C> DI void step(f32x4 (&acc)[C::NT][C::MT], const Frags<C>& cur, Frags<C>& nxt, unsigned aA, const unsigned (&aB)[C::NV], int ksimm) {
  static_assert(2 * C::NM >= C::NR, "reads per mfma");
  step_impl<C>(acc, cur, nxt, aA, aB, ksimm, std::make_integer_sequence<int, C::NM>{});
}
template<class C, int... Is> DI void read_all_impl(Frags<C>& f, unsigned aA, const unsigned (&aB)[C::NV], std::integer_sequence<int, Is...>) { (read_item<C, Is>(f, aA, aB, 0), ...); }

template<class C, int KID> DI void consumer_loop(f32x4 (&acc)[C::NT][C::MT], unsigned lds0, int wid, int lane, unsigned char* ws) {
  const int wc = wid % C::WN, wr = wid / C::WN;
  const int g = lane >> 4, i16 = lane & 15, q = i16 >> 2, p = lane & 3;
  unsigned aA0, aA1, aB[C::NV];
  { const int row = wr * (C::BM / C::WM) + i16, f = (row >> 1) & 7; aA0 = lds0 + row * 128 + (g ^ f) * 16; aA1 = aA0 ^ 64; }
  { const int pos = (g >> 1) * 16 + (g & 1) * 4 + q;
    const unsigned base = lds0 + C::A_BYTES + (C::STRIP_PER_WAVE ? wc * C::STRIP_BYTES : 0) + pos * C::ROWB + (p >> 1) * 16 + (p & 1) * 8;
    const int t0 = C::STRIP_PER_WAVE ? 0 : wc * C::TPS;
#pragma unroll
    for (int v = 0; v < C::NV; ++v) aB[v] = base + (C::SWZ ? (((t0 + v) ^ bmask<C::SW>(pos)) * 32) : t0 * 32); }
#pragma unroll
  for (int n = 0; n < C::NT; ++n)
#pragma unroll
    for (int m = 0; m < C::MT; ++m) acc[n][m] = (f32x4){0.f, 0.f, 0.f, 0.f};
  Frags<C> F0, F1;
  ST0(KID, 1);
  waitl0(); bar();
  ST0(KID, 2); waitl0();
  read_all_impl<C>(F0, aA0, aB, std::make_integer_sequence<int, C::NR>{});
  FENCE();
  int slot = 0;
#pragma unroll 1
  for (int t = 0; t < C::NKT; ++t) {
    unsigned a, b[C::NV];
    unsigned o = slot * C::STAGE;
    a = aA1 + o;
#pragma unroll
    for (int v = 0; v < C::NV; ++v) b[v] = aB[v] + o;
    step<C>(acc, F0, F1, a, b, 32 * C::ROWB);
    slot = slot + 1 == C::ST ? 0 : slot + 1;
    waitl0(); bar();
    o = slot * C::STAGE;
    a = aA0 + o;
#pragma unroll
    for (int v = 0; v < C::NV; ++v) b[v] = aB[v] + o;
    step<C>(acc, F1, F0, a, b, 0);
  }
  waitl0();
  ST0(KID, 3);
}

template<class C> struct Loader {
  unsigned voff[C::PMAX]; int lw; unsigned lds0; i32x4 rsA, rsB;
  template<class ColFn> DI void init(int lw_, int lane, unsigned lds0_, unsigned a_row0_bytes, ColFn colbyte) {
    lw = lw_; lds0 = lds0_;
#pragma unroll
    for (int i = 0; i < C::PMAX; ++i) {
      const int pi = lw + i * C::NL;
      unsigned v;
      if (i < C::PA / C::NL) { const int row = pi * 8 + (lane >> 3), ch = (lane & 7) ^ ((row >> 1) & 7); v = a_row0_bytes + row * C::LDA + ch * 16; }
      else {
        const int lin = (pi - C::PA) * 64 + lane;
        constexpr int CPR = C::SW / 8, CPS = 64 * CPR;
        const int strip = lin / CPS, r = lin % CPS, pos = r / CPR, chp = r % CPR;
        const int k = kswap(pos), ch = chp ^ (bmask<C::SW>(pos) << 1);
        v = k * C::LDB + colbyte(strip) + ch * 16;
      }
      voff[i] = v;
    }
  }
  template<int I> DI void piece(unsigned slotaddr, unsigned soffA, unsigned soffB) const {
    const int pi = lw + I * C::NL;
    if constexpr (I < C::PA / C::NL) dma16(slotaddr + pi * 1024, voff[I], rsA, soffA);
    else if constexpr (I < C::PMIN) dma16(slotaddr + pi * 1024, voff[I], rsB, soffB);
    else { if (lw < C::NBIG) dma16(slotaddr + pi * 1024, voff[I], rsB, soffB); }
  }
  template<int... Is> DI void tile_impl(int t, int slot, std::integer_sequence<int, Is...>) const { const unsigned sa = lds0 + slot * C::STAGE; (piece<Is>(sa, t * 128, t * (64 * C::LDB)), ...); }
  DI void tile(int t, int slot) const { tile_impl(t, slot, std::make_integer_sequence<int, C::PMAX>{}); }
  template<int TILES> DI void wait() const {
    if constexpr (C::NBIG == 0) waitv<TILES * C::PMIN>();
    else { if (lw < C::NBIG) waitv<TILES * C::PMAX>(); else waitv<TILES * C::PMIN>(); }
  }
};
template<class C, class ColFn> DI void producer_loop(unsigned lds0, int lw, int lane, i32x4 rsA, i32x4 rsB, unsigned a_row0_bytes, ColFn colbyte) {
  Loader<C> L; L.rsA = rsA; L.rsB = rsB;
  L.init(lw, lane, lds0, a_row0_bytes, colbyte);
  L.tile(0, 0); L.tile(1, 1);
  L.template wait<1>(); bar();
#pragma unroll
  for (int s = 2; s < C::ST; ++s) L.tile(s, s);
  int slot = 0;
#pragma unroll 1
  for (int t = 1; t <= C::NKT - C::ST; ++t) {
    L.template wait<C::ST - 2>(); bar();
    L.tile(t - 1 + C::ST, slot);
    slot = slot + 1 == C::ST ? 0 : slot + 1;
  }
  [&]<int... Is>(std::integer_sequence<int, Is...>) {
    ((L.template wait<(C::ST - 2 - Is > 0 ? C::ST - 2 - Is : 0)>(), bar()), ...);
  }(std::make_integer_sequence<int, C::ST>{});
}
DI void tile_of_block(int b, int rx, int& pm, int& pn) {
  const int xcd = b & 7, j = b >> 3, px = 16 / rx, cx = 32 / rx;
  pm = (xcd % px) * rx + j % rx; pn = (xcd / px) * cx + j / rx;
}

__global__ __launch_bounds__(256) void k0_prep(const u16* __restrict__ x, const u16* __restrict__ w_in, unsigned char* __restrict__ ws, const u16* wqkv, const u16* wo, const u16* w_sp, const u16* w_out) {
  unsigned pf = 0;
  { const unsigned gt = blockIdx.x * 256 + threadIdx.x;
    if (gt < 49152) pf += *(const unsigned*)((const char*)wqkv + (size_t)gt * 128);
    if (gt < 16384) pf += *(const unsigned*)((const char*)wo + (size_t)gt * 128);
    pf += *(const unsigned*)((const char*)w_sp + (size_t)gt * 128);
    if (gt < 28672) pf += *(const unsigned*)((const char*)w_out + (size_t)gt * 128);
  }
  const int tid = threadIdx.x, lane = tid & 63, wid = tid >> 6, b = blockIdx.x;
  ST0(0, 0);
  if (b == 255 && tid < 64) *(unsigned*)(ws + WS_CTL + tid * 256) = 0u;
  u32x4 v[2][2];
#pragma unroll
  for (int r = 0; r < 2; ++r)
#pragma unroll
    for (int c = 0; c < 2; ++c) v[r][c] = *(const u32x4*)(x + (size_t)(b * 8 + wid * 2 + r) * DM + c * 512 + lane * 8);
  f32x4 cs0 = {0, 0, 0, 0}, cs1 = {0, 0, 0, 0};
  const int cb = b % 56, kq = b / 56;
  if (b < 224) {
#pragma unroll
    for (int i = 0; i < 8; ++i) {
      const u32x4 w = *(const u32x4*)(w_in + (size_t)(kq * 256 + i * 32 + (tid >> 3)) * FF + cb * 64 + (tid & 7) * 8);
      cs0 += (f32x4){bflo(w[0]), bfhi(w[0]), bflo(w[1]), bfhi(w[1])}; cs1 += (f32x4){bflo(w[2]), bfhi(w[2]), bflo(w[3]), bfhi(w[3])};
    }
  }
#pragma unroll
  for (int r = 0; r < 2; ++r) {
    float f[16]; float s = 0;
#pragma unroll
    for (int c = 0; c < 2; ++c)
#pragma unroll
      for (int j = 0; j < 4; ++j) { f[c * 8 + 2 * j] = bflo(v[r][c][j]); f[c * 8 + 2 * j + 1] = bfhi(v[r][c][j]); }
#pragma unroll
    for (int j = 0; j < 16; ++j) s += f[j];
#pragma unroll
    for (int o = 1; o < 64; o <<= 1) s += __shfl_xor(s, o);
    const float mean = s * (1.0f / DM); float q = 0;
#pragma unroll
    for (int j = 0; j < 16; ++j) { f[j] -= mean; q += f[j] * f[j]; }
#pragma unroll
    for (int o = 1; o < 64; o <<= 1) q += __shfl_xor(q, o);
    const float rstd = 1.0f / sqrtf(q * (1.0f / DM) + 1e-5f);
#pragma unroll
    for (int c = 0; c < 2; ++c) { u32x4 o;
#pragma unroll
      for (int j = 0; j < 4; ++j) o[j] = cvtpk(f[c * 8 + 2 * j] * rstd, f[c * 8 + 2 * j + 1] * rstd);
      *(u32x4*)((u16*)(ws + WS_XN) + (size_t)(b * 8 + wid * 2 + r) * DM + c * 512 + lane * 8) = o; }
  }
  if (b < 224) {
    __shared__ f32x4 red[4][8][2];
#pragma unroll
    for (int o = 8; o < 64; o <<= 1)
#pragma unroll
      for (int j = 0; j < 4; ++j) { cs0[j] += __shfl_xor(cs0[j], o); cs1[j] += __shfl_xor(cs1[j], o); }
    if (lane < 8) { red[wid][lane][0] = cs0; red[wid][lane][1] = cs1; }
    __syncthreads();
    if (tid < 16) { const f32x4 s = red[0][tid >> 1][tid & 1] + red[1][tid >> 1][tid & 1] + red[2][tid >> 1][tid & 1] + red[3][tid >> 1][tid & 1];
      *(f32x4*)(ws + WS_CSUM + ((size_t)kq * FF + cb * 64 + tid * 4) * 4) = s; }
  }
  if (pf == 0x12345678u) *(unsigned*)(ws + WS_END) = pf;
  ST0(0, 7);
}

using C1 = Cfg<128, 64, 3, 2, 2, 3, DM, DM * 2, 3 * DM * 2>;
constexpr int K1_WPHI = C1::RING;
constexpr int K1_LDS = K1_WPHI + 4096;
constexpr int K1_QX = 0;
constexpr int K1_PHI = K1_QX + 16384, PHI_RS = 136, PHI_HEAD = 128 * PHI_RS;
constexpr int K1_V = K1_PHI + 2 * PHI_HEAD, V_RS = 72, V_HEAD = 128 * V_RS;
constexpr int K1_KVT = K1_V + 2 * V_HEAD;
constexpr int K1_ZT = K1_KVT + 8192;
constexpr int K1_OST = K1_ZT + 256, OST_RS = 144;
static_assert(K1_OST + 128 * OST_RS <= C1::RING && K1_OST % 16 == 0 && K1_V % 8 == 0, "K1 epilogue map");

DI void feature_map(const f32x4 p0, const f32x4 p1, u32x4& pos, u32x4& neg) {
  constexpr float L2E = 1.4426950408889634f;
  const float mx = max4(fmaxf(fmaxf(fmaxf(p0[0], p0[1]), fmaxf(p0[2], p0[3])), fmaxf(fmaxf(p1[0], p1[1]), fmaxf(p1[2], p1[3]))));
  const float mn = min4(fminf(fminf(fminf(p0[0], p0[1]), fminf(p0[2], p0[3])), fminf(fminf(p1[0], p1[1]), fminf(p1[2], p1[3]))));
  const float cx = -mx * L2E, cn = mn * L2E;
  float ep[8], en[8], sp = 0.f, sn = 0.f;
#pragma unroll
  for (int j = 0; j < 8; ++j) { const float xv = j < 4 ? p0[j & 3] : p1[j & 3];
    ep[j] = __builtin_amdgcn_exp2f(__builtin_fmaf(xv, L2E, cx)); en[j] = __builtin_amdgcn_exp2f(__builtin_fmaf(xv, -L2E, cn)); sp += ep[j]; sn += en[j]; }
  const float rp = __builtin_amdgcn_rcpf(sum4(sp)), rn = __builtin_amdgcn_rcpf(sum4(sn));
#pragma unroll
  for (int j = 0; j < 4; ++j) { pos[j] = cvtpk(ep[2 * j] * rp, ep[2 * j + 1] * rp); neg[j] = cvtpk(en[2 * j] * rn, en[2 * j + 1] * rn); }
}
DI u32x4 pack8(const f32x4 a, const f32x4 b) { return (u32x4){cvtpk(a[0], a[1]), cvtpk(a[2], a[3]), cvtpk(b[0], b[1]), cvtpk(b[2], b[3])}; }

__global__ __launch_bounds__(512, 2) void k1_qkv(const u16* __restrict__ wqkv, const u16* __restrict__ wphi, unsigned char* __restrict__ ws) {
  extern __shared__ __attribute__((aligned(16))) unsigned char shm[];
  const unsigned lds0 = (unsigned)(size_t)(LAS unsigned char*)shm;
  const int tid = threadIdx.x, lane = tid & 63, wid = __builtin_amdgcn_readfirstlane(tid >> 6);
  const int g = lane >> 4, i16 = lane & 15, q4 = i16 >> 2, p4 = lane & 3;
  ST0(1, 0);
  int pm, pn; tile_of_block(blockIdx.x, 8, pm, pn);
  const int w4 = wid & 3, wr = w4 >> 1, wc = w4 & 1;
  GAS unsigned* cnt = (GAS unsigned*)(ws + WS_CTL) + pn * 64;
  GAS unsigned* tmo = (GAS unsigned*)(ws + WS_CTL) + 32 * 64;
  const __amdgpu_buffer_rsrc_t rsKV = make_rsrc(ws + WS_KVP, 16 * 16 * 8192), rsZ = make_rsrc(ws + WS_ZP, 16 * 16 * 512);
  u32x4 phiq[4][2];
  const unsigned wph = lds0 + K1_WPHI + wc * 2048 + (4 * g + q4) * 64 + p4 * 8;
  if (wid < 4) {
    ldsst<u32x4>(lds0 + K1_WPHI + tid * 16, *(const u32x4*)(wphi + (size_t)pn * 2048 + tid * 8));
    f32x4 acc[C1::NT][C1::MT];
    consumer_loop<C1, 1>(acc, lds0, wid, lane, ws);
#pragma unroll
    for (int m = 0; m < 4; ++m) ldsst<u32x4>(lds0 + K1_QX + (w4 * 4 + m) * 1024 + lane * 16, pack8(acc[0][m], acc[1][m]));
    lbar();
    const bf16x8 wf0 = join(trread(wph), trread(wph + 1024)), wf1 = join(trread(wph + 32), trread(wph + 1024 + 32));
#pragma unroll
    for (int m = 0; m < 4; ++m) {
      const bf16x8 kf = asbf(pack8(acc[2][m], acc[3][m]));
      const f32x4 z4 = {0.f, 0.f, 0.f, 0.f};
      u32x4 pos, neg; feature_map(mfma(wf0, kf, z4), mfma(wf1, kf, z4), pos, neg);
      const int tok = wr * 64 + m * 16 + i16;
      const unsigned pa = lds0 + K1_PHI + wc * PHI_HEAD + tok * PHI_RS + g * 8;
      ldsst<u32x2>(pa, (u32x2){pos[0], pos[1]}); ldsst<u32x2>(pa + 32, (u32x2){pos[2], pos[3]});
      ldsst<u32x2>(pa + 64, (u32x2){neg[0], neg[1]}); ldsst<u32x2>(pa + 96, (u32x2){neg[2], neg[3]});
      const u32x4 vv = pack8(acc[4][m], acc[5][m]);
      const unsigned va = lds0 + K1_V + wc * V_HEAD + tok * V_RS + g * 8;
      ldsst<u32x2>(va, (u32x2){vv[0], vv[1]}); ldsst<u32x2>(va + 32, (u32x2){vv[2], vv[3]});
    }
    lbar();
    f32x4 d[2][2], dz[2];
#pragma unroll
    for (int a = 0; a < 2; ++a) { dz[a] = (f32x4){0.f, 0.f, 0.f, 0.f}; d[a][0] = dz[a]; d[a][1] = dz[a]; }
    const bf16x8 ones = asbf((u32x4){0x3f803f80u, 0x3f803f80u, 0x3f803f80u, 0x3f803f80u});
    const unsigned pr = lds0 + K1_PHI + wc * PHI_HEAD + (4 * g + q4) * PHI_RS + wr * 64 + p4 * 8;
    const unsigned vr = lds0 + K1_V + wc * V_HEAD + (4 * g + q4) * V_RS + p4 * 8;
#pragma unroll
    for (int s = 0; s < 4; ++s) {
      bf16x8 pf[2], vf[2];
#pragma unroll
      for (int a = 0; a < 2; ++a) { pf[a] = join(trread(pr + s * 32 * PHI_RS + a * 32), trread(pr + (s * 32 + 16) * PHI_RS + a * 32));
                                    vf[a] = join(trread(vr + s * 32 * V_RS + a * 32), trread(vr + (s * 32 + 16) * V_RS + a * 32)); }
#pragma unroll
      for (int a = 0; a < 2; ++a) { d[a][0] = mfma(pf[a], vf[0], d[a][0]); d[a][1] = mfma(pf[a], vf[1], d[a][1]); dz[a] = mfma(pf[a], ones, dz[a]); }
    }
    const unsigned kvoff = ((pn * 16 + pm) * 2 + wc) * 4096 + wr * 2048 + lane * 16;
#pragma unroll
    for (int dt = 0; dt < 2; ++dt) __builtin_amdgcn_raw_buffer_store_b128(pack8(d[0][dt], d[1][dt]), rsKV, kvoff + dt * 1024, 0, 16);
    if (i16 < 2) __builtin_amdgcn_raw_buffer_store_b128(__builtin_bit_cast(u32x4, i16 == 0 ? dz[0] : dz[1]), rsZ, ((pn * 16 + pm) * 2 + wc) * 256 + wr * 128 + g * 32 + i16 * 16, 0, 16);
    ST0(1, 4);
    waitv<0>(); bar();
    if (wid == 0) {
      if (lane == 0) __hip_atomic_fetch_add(cnt, 1u, RLX_AGENT);
      wait_count(cnt, 16u, tmo);
    }
    bar();
    ST0(1, 5);
  } else {
    producer_loop<C1>(lds0, w4, lane, make_srd(ws + WS_XN, SEQ * DM * 2), make_srd(wqkv, DM * 3 * DM * 2), (unsigned)pm * 128 * C1::LDA,
                      [&](int strip) { return (unsigned)(strip * DM + pn * 64) * 2; });
    bar();
    const bf16x8 wf0 = join(trread(wph), trread(wph + 1024)), wf1 = join(trread(wph + 32), trread(wph + 1024 + 32));
#pragma unroll
    for (int m = 0; m < 4; ++m) {
      const bf16x8 qf = ldsld<bf16x8>(lds0 + K1_QX + (w4 * 4 + m) * 1024 + lane * 16);
      const f32x4 z4 = {0.f, 0.f, 0.f, 0.f};
      feature_map(mfma(wf0, qf, z4), mfma(wf1, qf, z4), phiq[m][0], phiq[m][1]);
    }
    bar(); bar(); bar();
  }
  { const int head = wid & 1, fr = wid >> 1;
    u32x4 pv[16];
#pragma unroll
    for (int p = 0; p < 16; ++p) pv[p] = __builtin_amdgcn_raw_buffer_load_b128(rsKV, ((pn * 16 + p) * 2 + head) * 4096 + fr * 1024 + lane * 16, 0, 16);
    float zs = 0.f;
    if (wid < 2) {
#pragma unroll
      for (int p = 0; p < 16; ++p) zs += __uint_as_float(__builtin_amdgcn_raw_buffer_load_b32(rsZ, ((pn * 16 + p) * 2 + head) * 256 + lane * 4, 0, 16));
    }
    f32x4 s0 = {0.f, 0.f, 0.f, 0.f}, s1 = s0;
#pragma unroll
    for (int p = 0; p < 16; ++p) { s0 += (f32x4){bflo(pv[p][0]), bfhi(pv[p][0]), bflo(pv[p][1]), bfhi(pv[p][1])}; s1 += (f32x4){bflo(pv[p][2]), bfhi(pv[p][2]), bflo(pv[p][3]), bfhi(pv[p][3])}; }
    ldsst<u32x4>(lds0 + K1_KVT + (head * 4 + fr) * 1024 + lane * 16, pack8(s0, s1));
    if (wid < 2) ldsst<u16>(lds0 + K1_ZT + head * 128 + lane * 2, (u16)cvtpk(zs, 0.f));
  }
  lbar();
  ST0(1, 6);
  if (wid >= 4) {
    bf16x8 kv[2][2], zf[2];
#pragma unroll
    for (int ks = 0; ks < 2; ++ks) { zf[ks] = ldsld<bf16x8>(lds0 + K1_ZT + wc * 128 + ks * 64 + g * 16);
#pragma unroll
      for (int dt = 0; dt < 2; ++dt) kv[ks][dt] = ldsld<bf16x8>(lds0 + K1_KVT + (wc * 4 + ks * 2 + dt) * 1024 + lane * 16); }
#pragma unroll
    for (int m = 0; m < 4; ++m) {
      const f32x4 z4 = {0.f, 0.f, 0.f, 0.f};
      const bf16x8 b0 = asbf(phiq[m][0]), b1 = asbf(phiq[m][1]);
      const f32x4 den = mfma(zf[1], b1, mfma(zf[0], b0, z4));
      const f32x4 n0 = mfma(kv[1][0], b1, mfma(kv[0][0], b0, z4)), n1 = mfma(kv[1][1], b1, mfma(kv[0][1], b0, z4));
      const float r = 1.0f / (den[0] + 1e-6f);
      const unsigned oa = lds0 + K1_OST + (wr * 64 + m * 16 + i16) * OST_RS + wc * 64 + g * 8;
      ldsst<u32x2>(oa, (u32x2){cvtpk(n0[0] * r, n0[1] * r), cvtpk(n0[2] * r, n0[3] * r)});
      ldsst<u32x2>(oa + 32, (u32x2){cvtpk(n1[0] * r, n1[1] * r), cvtpk(n1[2] * r, n1[3] * r)});
    }
  }
  lbar();
#pragma unroll
  for (int i = 0; i < 2; ++i) { const int c = tid + i * 512, row = c >> 3, ch = c & 7;
    *(u32x4*)(ws + WS_O + (size_t)(pm * 128 + row) * (DM * 2) + pn * 128 + ch * 16) = ldsld<u32x4>(lds0 + K1_OST + row * OST_RS + ch * 16); }
  ST0(1, 7);
}

template<class C, bool STATS, int KID> DI void res_gemm(const void* A, unsigned a_bytes, const u16* W, unsigned w_bytes, const u16* res, u16* out, f32x2* stats, int rx, unsigned char* ws) {
  extern __shared__ __attribute__((aligned(16))) unsigned char shm[];
  const unsigned lds0 = (unsigned)(size_t)(LAS unsigned char*)shm;
  const int tid = threadIdx.x, lane = tid & 63, wid = __builtin_amdgcn_readfirstlane(tid >> 6);
  ST0(KID, 0);
  int pm, pn; tile_of_block(blockIdx.x, rx, pm, pn);
  if (wid >= 4) {
    producer_loop<C>(lds0, wid - 4, lane, make_srd(A, a_bytes), make_srd(W, w_bytes), (unsigned)pm * 128 * C::LDA, [&](int) { return (unsigned)pn * 128; });
    bar();
    return;
  }
  u32x4 rv[4];
#pragma unroll
  for (int i = 0; i < 4; ++i) { const int c = tid + i * 256; rv[i] = *(const u32x4*)(res + (size_t)(pm * 128 + (c >> 3)) * DM + pn * 64 + (c & 7) * 8); }
  f32x4 acc[C::NT][C::MT];
  consumer_loop<C, KID>(acc, lds0, wid, lane, ws);
  const int g = lane >> 4, i16 = lane & 15, wr = wid >> 1, wc = wid & 1;
#pragma unroll
  for (int n = 0; n < 2; ++n)
#pragma unroll
    for (int m = 0; m < 4; ++m)
      ldsst<u32x2>(lds0 + (wr * 64 + m * 16 + i16) * OST_RS + (wc * 32 + n * 16 + g * 4) * 2, (u32x2){cvtpk(acc[n][m][0], acc[n][m][1]), cvtpk(acc[n][m][2], acc[n][m][3])});
  lbar();
#pragma unroll
  for (int i = 0; i < 4; ++i) { const int c = tid + i * 256, row = c >> 3, ch = c & 7;
    const u32x4 a = ldsld<u32x4>(lds0 + row * OST_RS + ch * 16); u32x4 o; float s = 0.f, qq = 0.f;
#pragma unroll
    for (int j = 0; j < 4; ++j) { o[j] = cvtpk(bflo(rv[i][j]) + bflo(a[j]), bfhi(rv[i][j]) + bfhi(a[j]));
      if (STATS) { const float lo = bflo(o[j]), hi = bfhi(o[j]); s += lo + hi; qq += lo * lo + hi * hi; } }
    *(u32x4*)(out + (size_t)(pm * 128 + row) * DM + pn * 64 + ch * 8) = o;
    if (STATS) {
#pragma unroll
      for (int of = 1; of < 8; of <<= 1) { s += __shfl_xor(s, of); qq += __shfl_xor(qq, of); }
      if (ch == 0) stats[(pm * 16 + pn) * 128 + row] = (f32x2){s, qq};
    }
  }
  ST0(KID, 7);
}
using C2 = Cfg<128, 64, 1, 2, 2, 5, DM, DM * 2, DM * 2>;
using C5 = Cfg<128, 64, 1, 2, 2, 5, FH, FH * 2, DM * 2>;
__global__ __launch_bounds__(512, 2) void k2_wo(const u16* __restrict__ x, const u16* __restrict__ wo, unsigned char* __restrict__ ws) {
  res_gemm<C2, true, 2>(ws + WS_O, SEQ * DM * 2, wo, DM * DM * 2, x, (u16*)(ws + WS_H), (f32x2*)(ws + WS_HST), 4, ws);
}
__global__ __launch_bounds__(512, 2) void k5_out(const u16* __restrict__ w_out, unsigned char* __restrict__ ws, u16* __restrict__ out) {
  res_gemm<C5, false, 5>(ws + WS_G, SEQ * FH * 2, w_out, FH * DM * 2, (const u16*)(ws + WS_H), out, nullptr, 4, ws);
}

using C3 = Cfg<128, 112, 2, 2, 2, 3, DM, DM * 2, FF * 2>;
constexpr int K3_RST = C3::RING;
constexpr int K3_CS = K3_RST + 1024;
constexpr int K3_ZS = K3_CS + 1024;
constexpr int K3_LDS = K3_ZS + 1024;
constexpr int K3_XF = 0;
constexpr int K3_STG = 32768, STG_RS = 240;
DI float gelu_tanh(float x) {
  constexpr float C0 = -2.0f * 0.7978845608028654f * 1.4426950408889634f, C1_ = C0 * 0.044715f;
  const float e = __builtin_amdgcn_exp2f(x * __builtin_fmaf(x * x, C1_, C0));
  return x * __builtin_amdgcn_rcpf(1.0f + e);
}
__global__ __launch_bounds__(512, 2) void k3_in(const u16* __restrict__ w_in, unsigned char* __restrict__ ws) {
  extern __shared__ __attribute__((aligned(16))) unsigned char shm[];
  const unsigned lds0 = (unsigned)(size_t)(LAS unsigned char*)shm;
  const int tid = threadIdx.x, lane = tid & 63, wid = __builtin_amdgcn_readfirstlane(tid >> 6);
  const int g = lane >> 4, i16 = lane & 15;
  ST0(3, 0);
  int pm, pn; tile_of_block(blockIdx.x, 8, pm, pn);
  const int w4 = wid & 3, wr = w4 >> 1, wc = w4 & 1, mp = wid >> 2;
  GAS unsigned* cnt = (GAS unsigned*)(ws + WS_CTL) + (16 + pm) * 64;
  GAS unsigned* tmo = (GAS unsigned*)(ws + WS_CTL) + 32 * 64;
  u32x2 z[2][7];
  if (wid < 4) {
    if (tid < 128) { const f32x2* st = (const f32x2*)(ws + WS_HST) + (size_t)pm * 16 * 128 + tid; f32x2 s = {0.f, 0.f};
#pragma unroll
      for (int p = 0; p < 16; ++p) s += st[p * 128];
      const float mean = s[0] * (1.0f / DM), var = fmaxf(s[1] * (1.0f / DM) - mean * mean, 0.f);
      ldsst<f32x2>(lds0 + K3_RST + tid * 8, (f32x2){mean, 1.0f / sqrtf(var + 1e-5f)}); }
    if (tid < 224) { const float* cs = (const float*)(ws + WS_CSUM) + (tid < 112 ? pn * 112 + tid : FH + pn * 112 + tid - 112);
      ldsst<float>(lds0 + K3_CS + (tid < 112 ? tid : tid + 16) * 4, cs[0] + cs[FF] + cs[2 * FF] + cs[3 * FF]); }
    f32x4 acc[C3::NT][C3::MT];
    consumer_loop<C3, 3>(acc, lds0, wid, lane, ws);
    f32x2 rs[4];
#pragma unroll
    for (int m = 0; m < 4; ++m) rs[m] = ldsld<f32x2>(lds0 + K3_RST + (wr * 64 + m * 16 + i16) * 8);
#pragma unroll
    for (int n = 0; n < 7; ++n) { const f32x4 cs = ldsld<f32x4>(lds0 + K3_CS + wc * 512 + (n * 16 + g * 4) * 4);
#pragma unroll
      for (int m = 0; m < 4; ++m) { const f32x4 v = (acc[n][m] - cs * rs[m][0]) * rs[m][1]; const u32x2 w = {cvtpk(v[0], v[1]), cvtpk(v[2], v[3])};
        if (m < 2) z[m][n] = w; else ldsst<u32x2>(lds0 + K3_XF + ((w4 * 2 + m - 2) * 7 + n) * 512 + lane * 8, w); } }
    lbar();
  } else {
    producer_loop<C3>(lds0, w4, lane, make_srd(ws + WS_H, SEQ * DM * 2), make_srd(w_in, DM * FF * 2), (unsigned)pm * 128 * C3::LDA,
                      [&](int strip) { return (unsigned)(strip * FH + pn * 112) * 2; });
    bar();
#pragma unroll
    for (int m = 0; m < 2; ++m)
#pragma unroll
      for (int n = 0; n < 7; ++n) z[m][n] = ldsld<u32x2>(lds0 + K3_XF + ((w4 * 2 + m) * 7 + n) * 512 + lane * 8);
  }
  float s[2] = {0.f, 0.f}, qq[2] = {0.f, 0.f};
#pragma unroll
  for (int m = 0; m < 2; ++m)
#pragma unroll
    for (int n = 0; n < 7; ++n) {
      const u32x2 w = {cvtpk(gelu_tanh(bflo(z[m][n][0])), gelu_tanh(bfhi(z[m][n][0]))), cvtpk(gelu_tanh(bflo(z[m][n][1])), gelu_tanh(bfhi(z[m][n][1])))};
      z[m][n] = w;
      if (wc == 1) { const float a = bflo(w[0]), b = bfhi(w[0]), c = bflo(w[1]), d = bfhi(w[1]); s[m] += (a + b) + (c + d); qq[m] += (a * a + b * b) + (c * c + d * d); }
    }
  const int row0 = wr * 64 + mp * 32;
  if (wc == 0) {
    unsigned char* z1 = ws + WS_Z1 + ((size_t)((pm * 16 + pn) * 4 + wr * 2 + mp) * 7) * 1024 + lane * 16;
#pragma unroll
    for (int n = 0; n < 7; ++n) *(u32x4*)(z1 + n * 1024) = (u32x4){z[0][n][0], z[0][n][1], z[1][n][0], z[1][n][1]};
  } else {
#pragma unroll
    for (int m = 0; m < 2; ++m) { const float a = sum4(s[m]), b = sum4(qq[m]);
      if (g == 0) __hip_atomic_store((GAS unsigned long long*)(ws + WS_ZST) + (pm * 16 + pn) * 128 + row0 + m * 16 + i16,
                                     ((unsigned long long)__float_as_uint(b) << 32) | __float_as_uint(a), RLX_AGENT); }
    waitv<0>();
  }
  ST0(3, 4);
  bar();
  if (wid == 0) {
    if (lane == 0) __hip_atomic_fetch_add(cnt, 1u, RLX_AGENT);
    wait_count(cnt, 16u, tmo);
  }
  bar();
  ST0(3, 5);
  if (tid < 128) { f32x2 t = {0.f, 0.f};
#pragma unroll
    for (int p = 0; p < 16; ++p) { const unsigned long long w = __hip_atomic_load((GAS unsigned long long*)(ws + WS_ZST) + (pm * 16 + p) * 128 + tid, RLX_AGENT);
      t += (f32x2){__uint_as_float((unsigned)w), __uint_as_float((unsigned)(w >> 32))}; }
    const float mean = t[0] * (1.0f / FH), var = fmaxf(t[1] * (1.0f / FH) - mean * mean, 0.f);
    ldsst<f32x2>(lds0 + K3_ZS + tid * 8, (f32x2){mean, 1.0f / sqrtf(var + 1e-5f)}); }
  lbar();
  ST0(3, 6);
  if (wc == 1) {
    const unsigned stg = lds0 + K3_STG + (wr * 2 + mp) * 32 * STG_RS;
#pragma unroll
    for (int m = 0; m < 2; ++m) { const f32x2 ms = ldsld<f32x2>(lds0 + K3_ZS + (row0 + m * 16 + i16) * 8);
#pragma unroll
      for (int n = 0; n < 7; ++n)
        ldsst<u32x2>(stg + (m * 16 + i16) * STG_RS + n * 32 + g * 8,
                     (u32x2){cvtpk((bflo(z[m][n][0]) - ms[0]) * ms[1], (bfhi(z[m][n][0]) - ms[0]) * ms[1]), cvtpk((bflo(z[m][n][1]) - ms[0]) * ms[1], (bfhi(z[m][n][1]) - ms[0]) * ms[1])}); }
    waitl0();
    unsigned char* z2 = ws + WS_Z2 + ((size_t)pn * SEQ + pm * 128 + row0) * 224;
#pragma unroll
    for (int i = 0; i < 7; ++i) { const int c = i * 64 + lane, r = c / 14, ch = c % 14;
      *(u32x4*)(z2 + c * 16) = ldsld<u32x4>(stg + r * STG_RS + ch * 16); }
  }
  if (wid == 1) ST(3, 7);
}

using C4 = Cfg<128, 112, 1, 4, 1, 4, SEQ, SEQ * 2, 224>;
__global__ __launch_bounds__(512, 2) void k4_sp(const u16* __restrict__ w_sp, const u16* __restrict__ b_sp, unsigned char* __restrict__ ws) {
  extern __shared__ __attribute__((aligned(16))) unsigned char shm[];
  const unsigned lds0 = (unsigned)(size_t)(LAS unsigned char*)shm;
  const int tid = threadIdx.x, lane = tid & 63, wid = __builtin_amdgcn_readfirstlane(tid >> 6);
  ST0(4, 0);
  int pm, pn; tile_of_block(blockIdx.x, 4, pm, pn);
  if (wid >= 4) {
    producer_loop<C4>(lds0, wid - 4, lane, make_srd(w_sp, SEQ * SEQ * 2), make_srd(ws + WS_Z2, SEQ * FH * 2), (unsigned)pm * 128 * C4::LDA, [&](int) { return (unsigned)pn * (SEQ * 224); });
    return;
  }
  const int g = lane >> 4, i16 = lane & 15;
  u32x4 z1[7];
  const unsigned char* z1p = ws + WS_Z1 + ((size_t)((pm * 16 + pn) * 4 + wid) * 7) * 1024 + lane * 16;
#pragma unroll
  for (int n = 0; n < 7; ++n) z1[n] = *(const u32x4*)(z1p + n * 1024);
  float bs[2];
#pragma unroll
  for (int m = 0; m < 2; ++m) bs[m] = bflo(b_sp[pm * 128 + wid * 32 + m * 16 + i16]);
  f32x4 acc[C4::NT][C4::MT];
  consumer_loop<C4, 4>(acc, lds0, wid, lane, ws);
  bar();
  const unsigned stg = lds0 + wid * 32 * STG_RS;
#pragma unroll
  for (int m = 0; m < 2; ++m)
#pragma unroll
    for (int n = 0; n < 7; ++n) { const unsigned a = z1[n][2 * m], b = z1[n][2 * m + 1]; const f32x4 v = acc[n][m] + bs[m];
      ldsst<u32x2>(stg + (m * 16 + i16) * STG_RS + n * 32 + g * 8, (u32x2){cvtpk(bflo(a) * v[0], bfhi(a) * v[1]), cvtpk(bflo(b) * v[2], bfhi(b) * v[3])}); }
  waitl0();
  unsigned char* gp = ws + WS_G + (size_t)(pm * 128 + wid * 32) * (FH * 2) + pn * 224;
#pragma unroll
  for (int i = 0; i < 7; ++i) { const int c = i * 64 + lane, r = c / 14, ch = c % 14;
    *(u32x4*)(gp + (size_t)r * (FH * 2) + ch * 16) = ldsld<u32x4>(stg + r * STG_RS + ch * 16); }
  ST0(4, 7);
}
}

extern "C" void kernel_launch(void* const* d_in, const int* in_sizes, int n_in, void* d_out, int out_size, void* d_ws, size_t ws_size, hipStream_t stream) {
  const u16* x = (const u16*)d_in[0];
  const u16* wqkv = (const u16*)d_in[1];
  const u16* wphi = (const u16*)d_in[2];
  const u16* wo = (const u16*)d_in[3];
  const u16* w_in = (const u16*)d_in[5];
  const u16* w_sp = (const u16*)d_in[6];
  const u16* b_sp = (const u16*)d_in[7];
  const u16* w_out = (const u16*)d_in[8];
  unsigned char* ws = (unsigned char*)d_ws;
  u16* out = (u16*)d_out;
  (void)in_sizes; (void)n_in; (void)out_size; (void)ws_size;
  (void)hipFuncSetAttribute((const void*)k1_qkv, hipFuncAttributeMaxDynamicSharedMemorySize, K1_LDS);
  (void)hipFuncSetAttribute((const void*)k2_wo, hipFuncAttributeMaxDynamicSharedMemorySize, C2::RING);
  (void)hipFuncSetAttribute((const void*)k3_in, hipFuncAttributeMaxDynamicSharedMemorySize, K3_LDS);
  (void)hipFuncSetAttribute((const void*)k4_sp, hipFuncAttributeMaxDynamicSharedMemorySize, C4::RING);
  (void)hipFuncSetAttribute((const void*)k5_out, hipFuncAttributeMaxDynamicSharedMemorySize, C5::RING);
  k0_prep<<<256, 256, 0, stream>>>(x, w_in, ws, wqkv, wo, w_sp, w_out);
  k1_qkv<<<256, 512, K1_LDS, stream>>>(wqkv, wphi, ws);
  k2_wo<<<256, 512, C2::RING, stream>>>(x, wo, ws);
  k3_in<<<256, 512, K3_LDS, stream>>>(w_in, ws);
  k4_sp<<<256, 512, C4::RING, stream>>>(w_sp, b_sp, ws);
  k5_out<<<256, 512, C5::RING, stream>>>(w_out, ws, out);
}
```

```cpp
#include <hip/hip_runtime.h>
#include <hip/hip_bf16.h>
#include <utility>

namespace {
typedef short s16x4 __attribute__((ext_vector_type(4)));
typedef short s16x8 __attribute__((ext_vector_type(8)));
typedef __bf16 bf16x8 __attribute__((ext_vector_type(8)));
typedef __bf16 bf16x2 __attribute__((ext_vector_type(2)));
typedef float f32x2 __attribute__((ext_vector_type(2)));
typedef float f32x4 __attribute__((ext_vector_type(4)));
typedef int i32x4 __attribute__((ext_vector_type(4)));
typedef unsigned u32x4 __attribute__((ext_vector_type(4)));
typedef unsigned u32x2 __attribute__((ext_vector_type(2)));
typedef unsigned short u16;
#define LAS __attribute__((address_space(3)))
#define GAS __attribute__((address_space(1)))
#define DI __device__ __forceinline__
#define FENCE() __builtin_amdgcn_sched_barrier(0)
#define RLX_AGENT __ATOMIC_RELAXED, __HIP_MEMORY_SCOPE_AGENT

constexpr int SEQ = 2048, DM = 1024, FH = 1792, FF = 3584;
constexpr size_t WS_CTL = 0;
enum { CNT_KV = 0, CNT_O = 16, CNT_H = 32, CNT_ZS = 48, CNT_Z2 = 64, CNT_G = 80, CNT_TMO = 96, CNT_N = 128 };
constexpr size_t CTL_BYTES = CNT_N * 256;
constexpr size_t WS_XN = CTL_BYTES;
constexpr size_t WS_CSUM = WS_XN + (size_t)SEQ * DM * 2;
constexpr size_t WS_KVP = WS_CSUM + 4 * FF * 4;
constexpr size_t WS_ZP = WS_KVP + (size_t)16 * 16 * 8192;
constexpr size_t WS_O = WS_ZP + 16 * 16 * 512;
constexpr size_t WS_H = WS_O + (size_t)SEQ * DM * 2;
constexpr size_t WS_HST = WS_H + (size_t)SEQ * DM * 2;
constexpr size_t WS_Z2 = WS_HST + 16 * 16 * 128 * 8;
constexpr size_t WS_ZST = WS_Z2 + (size_t)SEQ * FH * 2;
constexpr size_t WS_G = WS_ZST + 16 * 16 * 128 * 8;
constexpr size_t WS_END = WS_G + (size_t)SEQ * FH * 2;

DI i32x4 make_srd(const void* p, unsigned bytes) {
  const unsigned long long a = (unsigned long long)p; i32x4 rs;
  rs[0] = __builtin_amdgcn_readfirstlane((int)(unsigned)a); rs[1] = __builtin_amdgcn_readfirstlane((int)(unsigned)(a >> 32)) & 0xffff;
  rs[2] = (int)bytes; rs[3] = 0x00020000; return rs;
}
DI __amdgpu_buffer_rsrc_t make_rsrc(const void* p, unsigned bytes) { return __builtin_amdgcn_make_buffer_rsrc(const_cast<void*>(p), 0, bytes, 0x00020000); }
template<bool SC1> DI void dma16(unsigned ldsaddr, unsigned voff, i32x4 rs, unsigned soff) {
  if constexpr (SC1) asm volatile("s_mov_b32 m0, %0\n\ts_nop 0\n\tbuffer_load_dwordx4 %1, %2, %3 offen sc1 lds" :: "s"(ldsaddr), "v"(voff), "s"(rs), "s"(soff));
  else asm volatile("s_mov_b32 m0, %0\n\ts_nop 0\n\tbuffer_load_dwordx4 %1, %2, %3 offen lds" :: "s"(ldsaddr), "v"(voff), "s"(rs), "s"(soff));
}
DI void touch64(unsigned ldsdummy, unsigned voff, i32x4 rs) {
  asm volatile("s_mov_b32 m0, %0\n\ts_nop 0\n\tbuffer_load_dword %1, %2, 0 offen lds" :: "s"(ldsdummy), "v"(voff), "s"(rs));
}
template<int N> DI void waitv() { asm volatile("s_waitcnt vmcnt(%0)" :: "i"(N) : "memory"); }
DI void waitl0() { asm volatile("" ::: "memory"); __builtin_amdgcn_s_waitcnt(0xC07F); asm volatile("" ::: "memory"); }
DI void bar() { asm volatile("s_barrier" ::: "memory"); }
DI void lbar() { waitl0(); bar(); }
DI unsigned cvtpk(float lo, float hi) { f32x2 v = {lo, hi}; bf16x2 b = __builtin_convertvector(v, bf16x2); return __builtin_bit_cast(unsigned, b); }
DI float bflo(unsigned u) { return __uint_as_float(u << 16); }
DI float bfhi(unsigned u) { return __uint_as_float(u & 0xffff0000u); }
DI bf16x8 asbf(u32x4 v) { return __builtin_bit_cast(bf16x8, v); }
DI f32x4 mfma(bf16x8 a, bf16x8 b, f32x4 c) { return __builtin_amdgcn_mfma_f32_16x16x32_bf16(a, b, c, 0, 0, 0); }
DI s16x4 trread(unsigned addr) { return __builtin_amdgcn_ds_read_tr16_b64_v4i16((LAS s16x4*)addr); }
DI bf16x8 join(s16x4 lo, s16x4 hi) { return __builtin_bit_cast(bf16x8, __builtin_shufflevector(lo, hi, 0, 1, 2, 3, 4, 5, 6, 7)); }
template<class T> DI T ldsld(unsigned addr) { return *(const LAS T*)addr; }
template<class T> DI void ldsst(unsigned addr, T v) { *(LAS T*)addr = v; }
template<class F> DI float red4(float x, F f) {
  const unsigned u = __float_as_uint(x);
  auto a = __builtin_amdgcn_permlane16_swap(u, u, false, false);
  const float y = f(__uint_as_float(a[0]), __uint_as_float(a[1]));
  const unsigned v = __float_as_uint(y);
  auto b = __builtin_amdgcn_permlane32_swap(v, v, false, false);
  return f(__uint_as_float(b[0]), __uint_as_float(b[1]));
}
DI float sum4(float x) { return red4(x, [](float a, float b) { return a + b; }); }
DI float max4(float x) { return red4(x, [](float a, float b) { return fmaxf(a, b); }); }
DI float min4(float x) { return red4(x, [](float a, float b) { return fminf(a, b); }); }

#define ST(k, i)
#define ST0(k, i)
#define ST4(k, i)
DI void wait_count(GAS unsigned* cnt, unsigned want, GAS unsigned* tmo) {
  for (unsigned spins = 0;; ++spins) {
    if ((unsigned)__builtin_amdgcn_readfirstlane(__hip_atomic_load(cnt, RLX_AGENT)) >= want) break;
    if (spins > (1u << 16)) { __hip_atomic_store(tmo, 1u, RLX_AGENT); break; }
    if ((spins & 255u) == 255u && __builtin_amdgcn_readfirstlane(__hip_atomic_load(tmo, RLX_AGENT))) break;
    __builtin_amdgcn_s_sleep(1);
  }
}

template<int SW> __host__ __device__ constexpr int bmask(int pos) { return SW == 64 ? ((pos >> 1) & 3) : 0; }
__host__ __device__ constexpr int kswap(int k) { return (k & ~12) | ((k & 4) << 1) | ((k & 8) >> 1); }

enum { A_IMG = 0, A_ROWS = 1 };
enum { B_W64 = 0, B_W112 = 1, B_IMG = 2 };
template<int SW_, int NS_, int WM_, int WN_, int ST_, int K_, int AMODE_, int LDA_, int BMODE_, int LDB_> struct Cfg {
  static constexpr int BM = 128, SW = SW_, NS = NS_, BN = SW_ * NS_, WM = WM_, WN = WN_, ST = ST_, K = K_, AMODE = AMODE_, LDA = LDA_, BMODE = BMODE_, LDB = LDB_;
  static constexpr int NKT = K / 64;
  static constexpr int MT = BM / WM / 16, NT = BN / WN / 16;
  static constexpr int A_BYTES = BM * 128, ROWB = SW * 2, STRIP_BYTES = 64 * ROWB, B_BYTES = NS * STRIP_BYTES, STAGE = A_BYTES + B_BYTES, RING = ST * STAGE;
  static constexpr int PA = 16, PB = B_BYTES / 1024, PT = PA + PB, NL = 4;
  static constexpr int IA = PA / NL, IBMAX = (PB + NL - 1) / NL, IBMIN = PB / NL, NBIG = PB - IBMIN * NL;
  static constexpr int PMAX = IA + IBMAX, PMIN = IA + IBMIN;
  static constexpr bool STRIP_PER_WAVE = (NS == WN) && (WN > 1);
  static constexpr int TPS = STRIP_PER_WAVE ? SW / 16 : SW / 16 / WN;
  static constexpr bool SWZ = (SW == 64);
  static constexpr int NV = SWZ ? TPS : 1;
  static constexpr int NM = MT * NT, NR = MT + 2 * NT;
  static_assert(WM * WN == 4 && STRIP_BYTES % 1024 == 0 && NT == (STRIP_PER_WAVE ? TPS : TPS * NS) && ST >= 3 && NKT >= ST + 1, "config");
  static_assert((ST - 1) * PMAX <= 63, "vmcnt range");
};
template<class C> struct Frags { bf16x8 a[C::MT]; s16x4 bl[C::NT], bh[C::NT]; };

template<class C, int I> DI void read_item(Frags<C>& f, unsigned aA, const unsigned (&aB)[C::NV], int ksimm) {
  if constexpr (I >= 2 && I < 2 + C::MT) {
    constexpr int m = I - 2;
    f.a[m] = ldsld<bf16x8>(aA + m * 2048);
  } else {
    constexpr int r = I < 2 ? I : I - C::MT, j = r >> 1, u = r & 1;
    constexpr int strip = C::STRIP_PER_WAVE ? 0 : j / C::TPS, tj = C::STRIP_PER_WAVE ? j : j % C::TPS;
    constexpr int imm = strip * C::STRIP_BYTES + u * 8 * C::ROWB + (C::SWZ ? 0 : tj * 32);
    const s16x4 v = trread(aB[C::SWZ ? tj : 0] + imm + ksimm);
    if constexpr (u == 0) f.bl[j] = v; else f.bh[j] = v;
  }
}
template<class C, int I> DI void mfma_item(f32x4 (&acc)[C::NT][C::MT], const Frags<C>& f) {
  constexpr int n = I / C::MT, m = I % C::MT;
  acc[n][m] = mfma(join(f.bl[n], f.bh[n]), f.a[m], acc[n][m]);
}
template<class C, int... Is>
DI void step_impl(f32x4 (&acc)[C::NT][C::MT], const Frags<C>& cur, Frags<C>& nxt, unsigned aA, const unsigned (&aB)[C::NV], int ksimm, std::integer_sequence<int, Is...>) {
  ((mfma_item<C, Is>(acc, cur), FENCE(),
    [&] { if constexpr (C::NR <= C::NM) { if constexpr (Is < C::NR) read_item<C, Is>(nxt, aA, aB, ksimm); }
          else { if constexpr (2 * Is < C::NR) read_item<C, 2 * Is>(nxt, aA, aB, ksimm); if constexpr (2 * Is + 1 < C::NR) read_item<C, 2 * Is + 1>(nxt, aA, aB, ksimm); } }(),
    FENCE()), ...);
}
template<class C> DI void step(f32x4 (&acc)[C::NT][C::MT], const Frags<C>& cur, Frags<C>& nxt, unsigned aA, const unsigned (&aB)[C::NV], int ksimm) {
  static_assert(2 * C::NM >= C::NR, "reads per mfma");
  step_impl<C>(acc, cur, nxt, aA, aB, ksimm, std::make_integer_sequence<int, C::NM>{});
}
template<class C, int... Is> DI void read_all_impl(Frags<C>& f, unsigned aA, const unsigned (&aB)[C::NV], std::integer_sequence<int, Is...>) { (read_item<C, Is>(f, aA, aB, 0), ...); }

template<class C, int KID, class Hook> DI void consumer_loop(f32x4 (&acc)[C::NT][C::MT], unsigned lds0, int wid, int lane, unsigned char* ws, Hook hook) {
  const int wc = wid % C::WN, wr = wid / C::WN;
  const int g = lane >> 4, i16 = lane & 15, q = i16 >> 2, p = lane & 3;
  unsigned aA0, aA1, aB[C::NV];
  { const int row = wr * (C::BM / C::WM) + i16, f = (row >> 1) & 7; aA0 = lds0 + row * 128 + (g ^ f) * 16; aA1 = aA0 ^ 64; }
  { const int pos = (g >> 1) * 16 + (g & 1) * 4 + q;
    const unsigned base = lds0 + C::A_BYTES + (C::STRIP_PER_WAVE ? wc * C::STRIP_BYTES : 0) + pos * C::ROWB + (p >> 1) * 16 + (p & 1) * 8;
    const int t0 = C::STRIP_PER_WAVE ? 0 : wc * C::TPS;
#pragma unroll
    for (int v = 0; v < C::NV; ++v) aB[v] = base + (C::SWZ ? (((t0 + v) ^ bmask<C::SW>(pos)) * 32) : t0 * 32); }
#pragma unroll
  for (int n = 0; n < C::NT; ++n)
#pragma unroll
    for (int m = 0; m < C::MT; ++m) acc[n][m] = (f32x4){0.f, 0.f, 0.f, 0.f};
  Frags<C> F0, F1;
  ST0(KID, 1);
  waitl0(); bar();
  ST0(KID, 2); waitl0();
  read_all_impl<C>(F0, aA0, aB, std::make_integer_sequence<int, C::NR>{});
  FENCE();
  hook();
  FENCE();
  int slot = 0;
#pragma unroll 1
  for (int t = 0; t < C::NKT; ++t) {
    unsigned a, b[C::NV];
    unsigned o = slot * C::STAGE;
    a = aA1 + o;
#pragma unroll
    for (int v = 0; v < C::NV; ++v) b[v] = aB[v] + o;
    step<C>(acc, F0, F1, a, b, 32 * C::ROWB);
    slot = slot + 1 == C::ST ? 0 : slot + 1;
    waitl0(); bar();
    o = slot * C::STAGE;
    a = aA0 + o;
#pragma unroll
    for (int v = 0; v < C::NV; ++v) b[v] = aB[v] + o;
    step<C>(acc, F1, F0, a, b, 0);
  }
  waitl0();
  ST0(KID, 3);
}

template<class C> struct Loader {
  static constexpr int NVB = C::BMODE == B_W112 ? 7 : 1;
  unsigned va, vb[NVB]; int lw; unsigned lds0, a0, b0; i32x4 rsA, rsB;
  DI void init_a(int lane) {
    if constexpr (C::AMODE == A_IMG) va = lane * 16;
    else va = (lane >> 3) * C::LDA + (((lane & 7) ^ (((lw & 1) << 2) | (lane >> 4))) * 16);
  }
  DI void init_b(int lane) {
    if constexpr (C::BMODE == B_IMG) vb[0] = lane * 16;
    else if constexpr (C::BMODE == B_W64) vb[0] = (8 * (lane >> 5) + ((lane >> 3) & 3)) * C::LDB + (((lane & 7) ^ (((lane >> 4) & 3) << 1)) * 16);
    else {
#pragma unroll
      for (int i = 0; i < 7; ++i) { const unsigned lin = i * 64 + lane, pos = lin / 14u, ch = lin - pos * 14u; vb[i] = kswap(pos) * C::LDB + ch * 16; }
    }
  }
  template<int I> DI void piece_a(unsigned sa, int t) const {
    const int p = lw + I * 4;
    if constexpr (C::AMODE == A_IMG) dma16<true>(sa + p * 1024, va, rsA, a0 + t * 16384 + p * 1024);
    else dma16<false>(sa + p * 1024, va, rsA, a0 + p * (8 * C::LDA) + t * 128);
  }
  template<int I> DI void piece_b(unsigned sa, int t, unsigned stripbytes) const {
    if constexpr (C::BMODE == B_W112) dma16<false>(sa + C::A_BYTES + lw * 7168 + I * 1024, vb[I], rsB, b0 + t * (64 * C::LDB));
    else {
      const int p = lw + I * 4;
      if (I < C::IBMIN || lw < C::NBIG) {
        if constexpr (C::BMODE == B_IMG) dma16<true>(sa + C::A_BYTES + p * 1024, vb[0], rsB, b0 + t * C::B_BYTES + p * 1024);
        else { constexpr int strip = I / 2; const int pp = lw + (I & 1) * 4;
          dma16<false>(sa + C::A_BYTES + strip * 8192 + pp * 1024, vb[0], rsB, b0 + strip * stripbytes + (t * 64 + 16 * (pp >> 1) + 4 * (pp & 1)) * C::LDB); }
      }
    }
  }
  template<int... Is> DI void tile_a_impl(int t, int slot, std::integer_sequence<int, Is...>) const { const unsigned sa = lds0 + slot * C::STAGE; (piece_a<Is>(sa, t), ...); }
  template<int... Is> DI void tile_b_impl(int t, int slot, unsigned sb, std::integer_sequence<int, Is...>) const { const unsigned sa = lds0 + slot * C::STAGE; (piece_b<Is>(sa, t, sb), ...); }
  DI void tile_a(int t, int slot) const { tile_a_impl(t, slot, std::make_integer_sequence<int, C::IA>{}); }
  DI void tile_b(int t, int slot, unsigned sb) const { tile_b_impl(t, slot, sb, std::make_integer_sequence<int, C::IBMAX>{}); }
  template<int TILES> DI void wait() const {
    if constexpr (C::NBIG == 0 || C::BMODE == B_W112) waitv<TILES * C::PMAX>();
    else { if (lw < C::NBIG) waitv<TILES * C::PMAX>(); else waitv<TILES * C::PMIN>(); }
  }
};
template<class C, int... Is> DI void producer_tail(const Loader<C>& L, std::integer_sequence<int, Is...>) {
  ((L.template wait<(C::ST - 2 - Is > 0 ? C::ST - 2 - Is : 0)>(), bar()), ...);
}
template<class C> DI void producer_loop(unsigned lds0, int lw, int lane, i32x4 rsA, i32x4 rsB, unsigned a0, unsigned b0, unsigned stripbytes) {
  Loader<C> L; L.rsA = rsA; L.rsB = rsB; L.lw = lw; L.lds0 = lds0;
  L.a0 = __builtin_amdgcn_readfirstlane(a0); L.b0 = __builtin_amdgcn_readfirstlane(b0);
  L.init_a(lane); L.tile_a(0, 0); FENCE();
  L.init_b(lane); FENCE();
  L.tile_b(0, 0, stripbytes); L.tile_a(1, 1); L.tile_b(1, 1, stripbytes);
  L.template wait<1>(); bar();
#pragma unroll
  for (int s = 2; s < C::ST; ++s) { L.tile_a(s, s); L.tile_b(s, s, stripbytes); }
  int slot = 0;
#pragma unroll 1
  for (int t = 1; t <= C::NKT - C::ST; ++t) {
    L.template wait<C::ST - 2>(); bar();
    L.tile_a(t - 1 + C::ST, slot); L.tile_b(t - 1 + C::ST, slot, stripbytes);
    slot = slot + 1 == C::ST ? 0 : slot + 1;
  }
  producer_tail<C>(L, std::make_integer_sequence<int, C::ST>{});
}
template<int NLINES, class F> DI void prewarm(unsigned ldsdummy, int lw, int lane, i32x4 rs, F off) {
#pragma unroll
  for (int i = 0; i < (NLINES + 255) / 256; ++i) {
    const unsigned idx = (i * 4 + lw) * 64 + lane;
    if ((i * 4 + 4) * 64 <= NLINES || (i * 4 + lw) * 64 < NLINES) touch64(ldsdummy, off(idx < NLINES ? idx : NLINES - 1), rs);
  }
}
DI unsigned aimg(int r, int c) { return r * 128 + ((c ^ ((r >> 1) & 7)) * 16); }
DI void tile_of_block(int b, int rx, int& pm, int& pn) {
  const int xcd = b & 7, j = b >> 3, px = 16 / rx, cx = 32 / rx;
  pm = (xcd % px) * rx + j % rx; pn = (xcd / px) * cx + j / rx;
}

__global__ __launch_bounds__(256) void k0_prep(const u16* __restrict__ x, const u16* __restrict__ w_in, const u16* __restrict__ wqkv, unsigned char* __restrict__ ws) {
  const int tid = threadIdx.x, lane = tid & 63, wid = tid >> 6, b = blockIdx.x;
  ST0(0, 0);
  if (b == 255 && tid < CNT_N) *(unsigned*)(ws + WS_CTL + tid * 256) = 0u;
  u32x4 v[2][2];
#pragma unroll
  for (int r = 0; r < 2; ++r)
#pragma unroll
    for (int c = 0; c < 2; ++c) v[r][c] = *(const u32x4*)(x + (size_t)(b * 8 + wid * 2 + r) * DM + c * 512 + lane * 8);
  f32x4 cs0 = {0, 0, 0, 0}, cs1 = {0, 0, 0, 0};
  const int cb = b % 56, kq = b / 56;
  u32x4 w[8];
  if (b < 224) {
#pragma unroll
    for (int i = 0; i < 8; ++i) w[i] = *(const u32x4*)(w_in + (size_t)(kq * 256 + i * 32 + (tid >> 3)) * FF + cb * 64 + (tid & 7) * 8);
  }
  unsigned pf = 0;
  if (tid < 192) pf = *(const unsigned*)((const char*)wqkv + (size_t)(b * 192 + tid) * 128);
#pragma unroll
  for (int r = 0; r < 2; ++r) {
    float f[16]; float s = 0;
#pragma unroll
    for (int c = 0; c < 2; ++c)
#pragma unroll
      for (int j = 0; j < 4; ++j) { f[c * 8 + 2 * j] = bflo(v[r][c][j]); f[c * 8 + 2 * j + 1] = bfhi(v[r][c][j]); }
#pragma unroll
    for (int j = 0; j < 16; ++j) s += f[j];
#pragma unroll
    for (int o = 1; o < 64; o <<= 1) s += __shfl_xor(s, o);
    const float mean = s * (1.0f / DM); float q = 0;
#pragma unroll
    for (int j = 0; j < 16; ++j) { f[j] -= mean; q += f[j] * f[j]; }
#pragma unroll
    for (int o = 1; o < 64; o <<= 1) q += __shfl_xor(q, o);
    const float rstd = 1.0f / sqrtf(q * (1.0f / DM) + 1e-5f);
    const int row = (b & 15) * 8 + wid * 2 + r;
#pragma unroll
    for (int c = 0; c < 2; ++c) { u32x4 o;
#pragma unroll
      for (int j = 0; j < 4; ++j) o[j] = cvtpk(f[c * 8 + 2 * j] * rstd, f[c * 8 + 2 * j + 1] * rstd);
      *(u32x4*)(ws + WS_XN + (size_t)((b >> 4) * 16 + c * 8 + (lane >> 3)) * 16384 + aimg(row, lane & 7)) = o; }
  }
  if (b < 224) {
    __shared__ f32x4 red[4][8][2];
#pragma unroll
    for (int i = 0; i < 8; ++i) { cs0 += (f32x4){bflo(w[i][0]), bfhi(w[i][0]), bflo(w[i][1]), bfhi(w[i][1])}; cs1 += (f32x4){bflo(w[i][2]), bfhi(w[i][2]), bflo(w[i][3]), bfhi(w[i][3])}; }
#pragma unroll
    for (int o = 8; o < 64; o <<= 1)
#pragma unroll
      for (int j = 0; j < 4; ++j) { cs0[j] += __shfl_xor(cs0[j], o); cs1[j] += __shfl_xor(cs1[j], o); }
    if (lane < 8) { red[wid][lane][0] = cs0; red[wid][lane][1] = cs1; }
    __syncthreads();
    if (tid < 16) { const f32x4 s = red[0][tid >> 1][tid & 1] + red[1][tid >> 1][tid & 1] + red[2][tid >> 1][tid & 1] + red[3][tid >> 1][tid & 1];
      *(f32x4*)(ws + WS_CSUM + ((size_t)kq * FF + cb * 64 + tid * 4) * 4) = s; }
  }
  asm volatile("" :: "v"(pf));
  ST0(0, 7);
}

struct Ctx {
  unsigned lds0; int tid, lane, wid, pm, pn;
  unsigned char* ws; __amdgpu_buffer_rsrc_t rs;
  DI GAS unsigned* cnt(int i) const { return (GAS unsigned*)(ws + WS_CTL) + i * 64; }
  DI i32x4 ws_srd() const { return make_srd(ws, (unsigned)WS_END); }
};
constexpr int AUX_SC1 = 16;
DI void st16(const Ctx& c, unsigned off, u32x4 v) { __builtin_amdgcn_raw_buffer_store_b128(v, c.rs, off, 0, AUX_SC1); }
DI u32x4 ld16(const Ctx& c, unsigned off) { return __builtin_amdgcn_raw_buffer_load_b128(c.rs, off, 0, AUX_SC1); }
DI f32x2 ld8(const Ctx& c, unsigned off) { return __builtin_bit_cast(f32x2, __builtin_amdgcn_raw_buffer_load_b64(c.rs, off, 0, AUX_SC1)); }
DI void st8(const Ctx& c, unsigned off, f32x2 v) { __builtin_amdgcn_raw_buffer_store_b64(__builtin_bit_cast(u32x2, v), c.rs, off, 0, AUX_SC1); }
DI void meet(const Ctx& c, int counter) {
  waitl0(); waitv<0>(); bar();
  if (c.wid == 4) {
    if (c.lane == 0) __hip_atomic_fetch_add(c.cnt(counter), 1u, RLX_AGENT);
    wait_count(c.cnt(counter), 16u, c.cnt(CNT_TMO));
  }
  bar();
}
constexpr int LDS_Z1 = 122880;
constexpr int LDS_TAB = LDS_Z1 + 28672;
constexpr int LDS_DUMMY = LDS_TAB + 3072, LDS_TOTAL = LDS_DUMMY + 256;

using C1 = Cfg<64, 3, 2, 2, 3, DM, A_IMG, 0, B_W64, 3 * DM * 2>;
constexpr int K1_WPHI = C1::RING;
constexpr int K1_QX = 0;
constexpr int K1_PHI = K1_QX + 16384, PHI_RS = 136, PHI_HEAD = 128 * PHI_RS;
constexpr int K1_V = K1_PHI + 2 * PHI_HEAD, V_RS = 72, V_HEAD = 128 * V_RS;
constexpr int K1_KVT = K1_V + 2 * V_HEAD;
constexpr int K1_ZT = K1_KVT + 8192;
constexpr int K1_OST = K1_ZT + 256;
static_assert(K1_OST + 16384 <= C1::RING && K1_OST % 16 == 0 && K1_V % 8 == 0 && K1_WPHI + 4096 <= LDS_DUMMY, "P1 epilogue map");

DI float max3(float a, float b, float c) { float r; asm("v_max3_f32 %0, %1, %2, %3" : "=v"(r) : "v"(a), "v"(b), "v"(c)); return r; }
DI float min3(float a, float b, float c) { float r; asm("v_min3_f32 %0, %1, %2, %3" : "=v"(r) : "v"(a), "v"(b), "v"(c)); return r; }
DI void feature_map(f32x4 p0, f32x4 p1, u32x4& pos, u32x4& neg) {
  constexpr float L2E = 1.4426950408889634f;
  asm volatile("s_nop 7" : "+v"(p0), "+v"(p1));
  const float mx = max4(max3(max3(p0[0], p0[1], p0[2]), max3(p0[3], p1[0], p1[1]), max3(p1[2], p1[3], p1[3])));
  const float mn = min4(min3(min3(p0[0], p0[1], p0[2]), min3(p0[3], p1[0], p1[1]), min3(p1[2], p1[3], p1[3])));
  const float cx = -mx * L2E, cn = mn * L2E;
  float ep[8], en[8], sp = 0.f, sn = 0.f;
#pragma unroll
  for (int j = 0; j < 8; ++j) { const float xv = j < 4 ? p0[j & 3] : p1[j & 3];
    ep[j] = __builtin_amdgcn_exp2f(__builtin_fmaf(xv, L2E, cx)); en[j] = __builtin_amdgcn_exp2f(__builtin_fmaf(xv, -L2E, cn)); sp += ep[j]; sn += en[j]; }
  const float rp = __builtin_amdgcn_rcpf(sum4(sp)), rn = __builtin_amdgcn_rcpf(sum4(sn));
#pragma unroll
  for (int j = 0; j < 4; ++j) { pos[j] = cvtpk(ep[2 * j] * rp, ep[2 * j + 1] * rp); neg[j] = cvtpk(en[2 * j] * rn, en[2 * j + 1] * rn); }
}
DI u32x4 pack8(const f32x4 a, const f32x4 b) { return (u32x4){cvtpk(a[0], a[1]), cvtpk(a[2], a[3]), cvtpk(b[0], b[1]), cvtpk(b[2], b[3])}; }

DI void phase1(const Ctx& c, const u16* wqkv, const u16* wphi, const u16* wo, const u16* w_out) {
  const unsigned lds0 = c.lds0; const int tid = c.tid, lane = c.lane, wid = c.wid, pm = c.pm, pn = c.pn; unsigned char* ws = c.ws;
  const int g = lane >> 4, i16 = lane & 15, q4 = i16 >> 2, p4 = lane & 3;
  const int w4 = wid & 3, wr = w4 >> 1, wc = w4 & 1;
  ST0(1, 0);
  u32x4 phiq[4][2];
  const unsigned wph = lds0 + K1_WPHI + wc * 2048 + (4 * g + q4) * 64 + p4 * 8;
  if (wid < 4) {
    const u32x4 wp = *(const u32x4*)(wphi + (size_t)pn * 2048 + tid * 8);
    f32x4 acc[C1::NT][C1::MT];
    consumer_loop<C1, 1>(acc, lds0, wid, lane, ws, [&] { ldsst<u32x4>(lds0 + K1_WPHI + tid * 16, wp); });
#pragma unroll
    for (int m = 0; m < 4; ++m) ldsst<u32x4>(lds0 + K1_QX + (w4 * 4 + m) * 1024 + lane * 16, pack8(acc[0][m], acc[1][m]));
    lbar();
    const bf16x8 wf0 = join(trread(wph), trread(wph + 1024)), wf1 = join(trread(wph + 32), trread(wph + 1024 + 32));
#pragma unroll
    for (int m = 0; m < 4; ++m) {
      const bf16x8 kf = asbf(pack8(acc[2][m], acc[3][m]));
      const f32x4 z4 = {0.f, 0.f, 0.f, 0.f};
      u32x4 pos, neg; feature_map(mfma(wf0, kf, z4), mfma(wf1, kf, z4), pos, neg);
      const int tok = wr * 64 + m * 16 + i16;
      const unsigned pa = lds0 + K1_PHI + wc * PHI_HEAD + tok * PHI_RS + g * 8;
      ldsst<u32x2>(pa, (u32x2){pos[0], pos[1]}); ldsst<u32x2>(pa + 32, (u32x2){pos[2], pos[3]});
      ldsst<u32x2>(pa + 64, (u32x2){neg[0], neg[1]}); ldsst<u32x2>(pa + 96, (u32x2){neg[2], neg[3]});
      const u32x4 vv = pack8(acc[4][m], acc[5][m]);
      const unsigned va = lds0 + K1_V + wc * V_HEAD + tok * V_RS + g * 8;
      ldsst<u32x2>(va, (u32x2){vv[0], vv[1]}); ldsst<u32x2>(va + 32, (u32x2){vv[2], vv[3]});
    }
    lbar();
    f32x4 d[2][2], dz[2];
#pragma unroll
    for (int a = 0; a < 2; ++a) { dz[a] = (f32x4){0.f, 0.f, 0.f, 0.f}; d[a][0] = dz[a]; d[a][1] = dz[a]; }
    const bf16x8 ones = asbf((u32x4){0x3f803f80u, 0x3f803f80u, 0x3f803f80u, 0x3f803f80u});
    const unsigned pr = lds0 + K1_PHI + wc * PHI_HEAD + (4 * g + q4) * PHI_RS + wr * 64 + p4 * 8;
    const unsigned vr = lds0 + K1_V + wc * V_HEAD + (4 * g + q4) * V_RS + p4 * 8;
    bf16x8 pf[4][2], vf[4][2];
#pragma unroll
    for (int s = 0; s < 4; ++s)
#pragma unroll
      for (int a = 0; a < 2; ++a) { pf[s][a] = join(trread(pr + s * 32 * PHI_RS + a * 32), trread(pr + (s * 32 + 16) * PHI_RS + a * 32));
                                    vf[s][a] = join(trread(vr + s * 32 * V_RS + a * 32), trread(vr + (s * 32 + 16) * V_RS + a * 32)); }
#pragma unroll
    for (int s = 0; s < 4; ++s)
#pragma unroll
      for (int a = 0; a < 2; ++a) { d[a][0] = mfma(pf[s][a], vf[s][0], d[a][0]); d[a][1] = mfma(pf[s][a], vf[s][1], d[a][1]); dz[a] = mfma(pf[s][a], ones, dz[a]); }
    const unsigned kvoff = WS_KVP + ((pn * 16 + pm) * 2 + wc) * 4096 + wr * 2048 + lane * 16;
#pragma unroll
    for (int dt = 0; dt < 2; ++dt) st16(c, kvoff + dt * 1024, pack8(d[0][dt], d[1][dt]));
    if (i16 < 2) st16(c, WS_ZP + ((pn * 16 + pm) * 2 + wc) * 256 + wr * 128 + g * 32 + i16 * 16, __builtin_bit_cast(u32x4, i16 == 0 ? dz[0] : dz[1]));
    ST0(1, 4);
  } else {
    producer_loop<C1>(lds0, w4, lane, c.ws_srd(), make_srd(wqkv, DM * 3 * DM * 2), (unsigned)WS_XN + pm * (16 * 16384), (unsigned)pn * 128, DM * 2);
    { const unsigned b = blockIdx.x;
      prewarm<64>(lds0 + LDS_DUMMY, w4, lane, make_srd(wo, DM * DM * 2), [&](unsigned i) { return (b * 64 + i) * 128; });
      prewarm<112>(lds0 + LDS_DUMMY, 3 - w4, lane, make_srd(w_out, FH * DM * 2), [&](unsigned i) { return (b * 112 + i) * 128; }); }
    bar();
    const bf16x8 wf0 = join(trread(wph), trread(wph + 1024)), wf1 = join(trread(wph + 32), trread(wph + 1024 + 32));
#pragma unroll
    for (int m = 0; m < 4; ++m) {
      const bf16x8 qf = ldsld<bf16x8>(lds0 + K1_QX + (w4 * 4 + m) * 1024 + lane * 16);
      const f32x4 z4 = {0.f, 0.f, 0.f, 0.f};
      feature_map(mfma(wf0, qf, z4), mfma(wf1, qf, z4), phiq[m][0], phiq[m][1]);
    }
    bar();
  }
  meet(c, CNT_KV + pn);
  ST0(1, 5);
  { const int head = wid & 1, fr = wid >> 1;
    u32x4 pv[16];
#pragma unroll
    for (int p = 0; p < 16; ++p) pv[p] = ld16(c, WS_KVP + ((pn * 16 + p) * 2 + head) * 4096 + fr * 1024 + lane * 16);
    float zs = 0.f;
    if (wid < 2) {
      float zv[16];
#pragma unroll
      for (int p = 0; p < 16; ++p) zv[p] = __uint_as_float(__builtin_amdgcn_raw_buffer_load_b32(c.rs, WS_ZP + ((pn * 16 + p) * 2 + head) * 256 + lane * 4, 0, AUX_SC1));
#pragma unroll
      for (int p = 0; p < 16; ++p) zs += zv[p];
    }
    f32x4 s0 = {0.f, 0.f, 0.f, 0.f}, s1 = s0;
#pragma unroll
    for (int p = 0; p < 16; ++p) { s0 += (f32x4){bflo(pv[p][0]), bfhi(pv[p][0]), bflo(pv[p][1]), bfhi(pv[p][1])}; s1 += (f32x4){bflo(pv[p][2]), bfhi(pv[p][2]), bflo(pv[p][3]), bfhi(pv[p][3])}; }
    ldsst<u32x4>(lds0 + K1_KVT + (head * 4 + fr) * 1024 + lane * 16, pack8(s0, s1));
    if (wid < 2) ldsst<u16>(lds0 + K1_ZT + head * 128 + lane * 2, (u16)cvtpk(zs, 0.f));
  }
  lbar();
  ST0(1, 6);
  if (wid >= 4) {
    bf16x8 kv[2][2], zf[2];
#pragma unroll
    for (int ks = 0; ks < 2; ++ks) { zf[ks] = ldsld<bf16x8>(lds0 + K1_ZT + wc * 128 + ks * 64 + g * 16);
#pragma unroll
      for (int dt = 0; dt < 2; ++dt) kv[ks][dt] = ldsld<bf16x8>(lds0 + K1_KVT + (wc * 4 + ks * 2 + dt) * 1024 + lane * 16); }
#pragma unroll
    for (int m = 0; m < 4; ++m) {
      const f32x4 z4 = {0.f, 0.f, 0.f, 0.f};
      const bf16x8 b0 = asbf(phiq[m][0]), b1 = asbf(phiq[m][1]);
      const f32x4 den = mfma(zf[1], b1, mfma(zf[0], b0, z4));
      const f32x4 n0 = mfma(kv[1][0], b1, mfma(kv[0][0], b0, z4)), n1 = mfma(kv[1][1], b1, mfma(kv[0][1], b0, z4));
      const float r = 1.0f / (den[0] + 1e-6f);
      const int tok = wr * 64 + m * 16 + i16;
      const unsigned oa = lds0 + K1_OST + (g & 1) * 8;
      ldsst<u32x2>(oa + aimg(tok, wc * 4 + (g >> 1)), (u32x2){cvtpk(n0[0] * r, n0[1] * r), cvtpk(n0[2] * r, n0[3] * r)});
      ldsst<u32x2>(oa + aimg(tok, wc * 4 + 2 + (g >> 1)), (u32x2){cvtpk(n1[0] * r, n1[1] * r), cvtpk(n1[2] * r, n1[3] * r)});
    }
  }
  lbar();
#pragma unroll
  for (int i = 0; i < 2; ++i) { const int p = tid + i * 512; st16(c, WS_O + (pm * 16 + pn) * 16384 + p * 16, ldsld<u32x4>(lds0 + K1_OST + p * 16)); }
  ST0(1, 7);
}

using C2 = Cfg<64, 1, 2, 2, 4, DM, A_IMG, 0, B_W64, DM * 2>;
using C5 = Cfg<64, 1, 2, 2, 4, FH, A_IMG, 0, B_W64, DM * 2>;
template<class C, bool FIRST, int KID> DI void phase_res(const Ctx& c, unsigned a_off, const u16* W, unsigned w_bytes, const u16* x, u16* out) {
  const unsigned lds0 = c.lds0; const int tid = c.tid, lane = c.lane, wid = c.wid, pm = c.pm, pn = c.pn; unsigned char* ws = c.ws;
  ST0(KID, 0);
  if (wid >= 4) {
    producer_loop<C>(lds0, wid - 4, lane, c.ws_srd(), make_srd(W, w_bytes), a_off + (unsigned)pm * (C::NKT * 16384), (unsigned)pn * 128, 0);
    bar();
  } else {
    u32x4 rv[4];
    f32x4 acc[C::NT][C::MT];
    consumer_loop<C, KID>(acc, lds0, wid, lane, ws, [&] {
#pragma unroll
      for (int i = 0; i < 4; ++i) { const int p = tid + i * 256, row = p >> 3, cc = (p & 7) ^ ((row >> 1) & 7);
        rv[i] = FIRST ? *(const u32x4*)((const unsigned char*)x + (size_t)(pm * 128 + row) * (DM * 2) + pn * 128 + cc * 16) : ld16(c, WS_H + (pm * 16 + pn) * 16384 + p * 16); } });
    const int g = lane >> 4, i16 = lane & 15, wr = wid >> 1, wc = wid & 1;
#pragma unroll
    for (int n = 0; n < 2; ++n)
#pragma unroll
      for (int m = 0; m < 4; ++m)
        ldsst<u32x2>(lds0 + aimg(wr * 64 + m * 16 + i16, wc * 4 + n * 2 + (g >> 1)) + (g & 1) * 8, (u32x2){cvtpk(acc[n][m][0], acc[n][m][1]), cvtpk(acc[n][m][2], acc[n][m][3])});
    lbar();
#pragma unroll
    for (int i = 0; i < 4; ++i) { const int p = tid + i * 256, row = p >> 3, cc = (p & 7) ^ ((row >> 1) & 7);
      const u32x4 a = ldsld<u32x4>(lds0 + p * 16); u32x4 o; float s = 0.f, qq = 0.f;
#pragma unroll
      for (int j = 0; j < 4; ++j) { o[j] = cvtpk(bflo(rv[i][j]) + bflo(a[j]), bfhi(rv[i][j]) + bfhi(a[j]));
        if (FIRST) { const float lo = bflo(o[j]), hi = bfhi(o[j]); s += lo + hi; qq += lo * lo + hi * hi; } }
      if (FIRST) {
        st16(c, WS_H + (pm * 16 + pn) * 16384 + p * 16, o);
#pragma unroll
        for (int of = 1; of < 8; of <<= 1) { s += __shfl_xor(s, of); qq += __shfl_xor(qq, of); }
        if ((p & 7) == 0) st8(c, WS_HST + ((pm * 16 + pn) * 128 + row) * 8, (f32x2){s, qq});
      } else *(u32x4*)((unsigned char*)out + (size_t)(pm * 128 + row) * (DM * 2) + pn * 128 + cc * 16) = o;
    }
  }
  ST0(KID, 7);
}

using C3 = Cfg<112, 2, 2, 2, 3, DM, A_IMG, 0, B_W112, FF * 2>;
constexpr int K3_RST = LDS_TAB;
constexpr int K3_CS = K3_RST + 1024;
constexpr int K3_ZS = K3_CS + 1024;
constexpr int K3_XF = 0;
constexpr int K3_STG = 32768, STG_RS = 240;
static_assert(C3::RING <= LDS_TAB && K3_STG + 4 * 32 * STG_RS <= LDS_Z1, "P3 map");
DI float gelu_tanh(float x) {
  constexpr float C0 = -2.0f * 0.7978845608028654f * 1.4426950408889634f, C1_ = C0 * 0.044715f;
  const float e = __builtin_amdgcn_exp2f(x * __builtin_fmaf(x * x, C1_, C0));
  return x * __builtin_amdgcn_rcpf(1.0f + e);
}
DI void phase3(const Ctx& c, const u16* w_in, const u16* w_sp) {
  const unsigned lds0 = c.lds0; const int tid = c.tid, lane = c.lane, wid = c.wid, pm = c.pm, pn = c.pn; unsigned char* ws = c.ws;
  const int g = lane >> 4, i16 = lane & 15;
  const int w4 = wid & 3, wr = w4 >> 1, wc = w4 & 1, mp = wid >> 2;
  ST0(3, 0);
  u32x2 z[2][7];
  float s[2] = {0.f, 0.f}, qq[2] = {0.f, 0.f};
  auto act = [&](int m, int n, const f32x4 v) {
    const u32x2 w = {cvtpk(gelu_tanh(v[0]), gelu_tanh(v[1])), cvtpk(gelu_tanh(v[2]), gelu_tanh(v[3]))};
    z[m][n] = w;
    if (wc == 1) { const float a = bflo(w[0]), b = bfhi(w[0]), cc = bflo(w[1]), d = bfhi(w[1]); s[m] += (a + b) + (cc + d); qq[m] += (a * a + b * b) + (cc * cc + d * d); }
  };
  if (wid < 4) {
    f32x2 st[8]; float cs4[4];
#pragma unroll
    for (int p = 0; p < 8; ++p) st[p] = ld8(c, WS_HST + (((pm * 16 + (tid & 1) * 8) + p) * 128 + (tid >> 1)) * 8);
    { const float* cs = (const float*)(ws + WS_CSUM) + (tid < 112 ? pn * 112 + tid : tid < 224 ? FH + pn * 112 + tid - 112 : 0);
#pragma unroll
      for (int p = 0; p < 4; ++p) cs4[p] = cs[p * FF]; }
    f32x4 acc[C3::NT][C3::MT];
    consumer_loop<C3, 3>(acc, lds0, wid, lane, ws, [&] {
      f32x2 t = (st[0] + st[1]) + (st[2] + st[3]) + ((st[4] + st[5]) + (st[6] + st[7]));
      t[0] += __shfl_xor(t[0], 1); t[1] += __shfl_xor(t[1], 1);
      const float mean = t[0] * (1.0f / DM), var = fmaxf(t[1] * (1.0f / DM) - mean * mean, 0.f);
      if ((tid & 1) == 0) ldsst<f32x2>(lds0 + K3_RST + (tid >> 1) * 8, (f32x2){mean, 1.0f / sqrtf(var + 1e-5f)});
      if (tid < 224) ldsst<float>(lds0 + K3_CS + (tid < 112 ? tid : tid + 16) * 4, (cs4[0] + cs4[1]) + (cs4[2] + cs4[3])); });
    f32x2 rs[4];
#pragma unroll
    for (int m = 0; m < 4; ++m) rs[m] = ldsld<f32x2>(lds0 + K3_RST + (wr * 64 + m * 16 + i16) * 8);
    f32x4 csv[7];
#pragma unroll
    for (int n = 0; n < 7; ++n) csv[n] = ldsld<f32x4>(lds0 + K3_CS + wc * 512 + (n * 16 + g * 4) * 4);
#pragma unroll
    for (int n = 0; n < 7; ++n)
#pragma unroll
      for (int m = 2; m < 4; ++m) { const f32x4 v = (acc[n][m] - csv[n] * rs[m][0]) * rs[m][1];
        ldsst<u32x2>(lds0 + K3_XF + ((w4 * 2 + m - 2) * 7 + n) * 512 + lane * 8, (u32x2){cvtpk(v[0], v[1]), cvtpk(v[2], v[3])}); }
    lbar();
#pragma unroll
    for (int m = 0; m < 2; ++m)
#pragma unroll
      for (int n = 0; n < 7; ++n) act(m, n, (acc[n][m] - csv[n] * rs[m][0]) * rs[m][1]);
  } else {
    producer_loop<C3>(lds0, w4, lane, c.ws_srd(), make_srd(w_in, DM * FF * 2), (unsigned)WS_H + pm * (16 * 16384),
                      (unsigned)((w4 & 1) * 32 * C3::LDB + ((w4 >> 1) * FH + pn * 112) * 2), 0);
    prewarm<256>(lds0 + LDS_DUMMY, w4, lane, make_srd(w_sp, SEQ * SEQ * 2), [&](unsigned i) { return (blockIdx.x * 256 + i) * 128; });
    bar();
#pragma unroll
    for (int m = 0; m < 2; ++m)
#pragma unroll
      for (int n = 0; n < 7; ++n) { const u32x2 w = ldsld<u32x2>(lds0 + K3_XF + ((w4 * 2 + m) * 7 + n) * 512 + lane * 8);
        act(m, n, (f32x4){bflo(w[0]), bfhi(w[0]), bflo(w[1]), bfhi(w[1])}); }
  }
  const int row0 = wr * 64 + mp * 32;
  if (wc == 0) {
#pragma unroll
    for (int n = 0; n < 7; ++n) ldsst<u32x4>(lds0 + LDS_Z1 + ((wr * 2 + mp) * 7 + n) * 1024 + lane * 16, (u32x4){z[0][n][0], z[0][n][1], z[1][n][0], z[1][n][1]});
  } else {
#pragma unroll
    for (int m = 0; m < 2; ++m) { const float a = sum4(s[m]), b = sum4(qq[m]);
      if (g == 0) st8(c, WS_ZST + ((pm * 16 + pn) * 128 + row0 + m * 16 + i16) * 8, (f32x2){a, b}); }
  }
  ST0(3, 4);
  meet(c, CNT_ZS + pm);
  ST0(3, 5);
  if (tid < 256) { f32x2 w[8];
#pragma unroll
    for (int p = 0; p < 8; ++p) w[p] = ld8(c, WS_ZST + ((pm * 16 + (tid & 1) * 8 + p) * 128 + (tid >> 1)) * 8);
    f32x2 t = (w[0] + w[1]) + (w[2] + w[3]) + ((w[4] + w[5]) + (w[6] + w[7]));
    t[0] += __shfl_xor(t[0], 1); t[1] += __shfl_xor(t[1], 1);
    const float mean = t[0] * (1.0f / FH), var = fmaxf(t[1] * (1.0f / FH) - mean * mean, 0.f);
    if ((tid & 1) == 0) ldsst<f32x2>(lds0 + K3_ZS + (tid >> 1) * 8, (f32x2){mean, 1.0f / sqrtf(var + 1e-5f)}); }
  lbar();
  ST0(3, 6);
  if (wc == 1) {
    const unsigned stg = lds0 + K3_STG + (wr * 2 + mp) * 32 * STG_RS;
#pragma unroll
    for (int m = 0; m < 2; ++m) { const f32x2 ms = ldsld<f32x2>(lds0 + K3_ZS + (row0 + m * 16 + i16) * 8);
#pragma unroll
      for (int n = 0; n < 7; ++n)
        ldsst<u32x2>(stg + (m * 16 + kswap(i16)) * STG_RS + n * 32 + g * 8,
                     (u32x2){cvtpk((bflo(z[m][n][0]) - ms[0]) * ms[1], (bfhi(z[m][n][0]) - ms[0]) * ms[1]), cvtpk((bflo(z[m][n][1]) - ms[0]) * ms[1], (bfhi(z[m][n][1]) - ms[0]) * ms[1])}); }
    waitl0();
    const unsigned z2 = WS_Z2 + (pn * SEQ + pm * 128 + row0) * 224;
#pragma unroll
    for (int i = 0; i < 7; ++i) { const unsigned ci = i * 64 + lane, r = ci / 14u, ch = ci - r * 14u;
      st16(c, z2 + ci * 16, ldsld<u32x4>(stg + r * STG_RS + ch * 16)); }
  }
  if (wid == 1) ST(3, 7);
}

using C4 = Cfg<112, 1, 4, 1, 4, SEQ, A_ROWS, SEQ * 2, B_IMG, 0>;
static_assert(C4::RING <= LDS_Z1, "P4 map");
DI void phase4(const Ctx& c, const u16* w_sp, const u16* b_sp) {
  const unsigned lds0 = c.lds0; const int lane = c.lane, wid = c.wid, pm = c.pm, pn = c.pn; unsigned char* ws = c.ws;
  ST0(4, 0);
  if (wid >= 4) {
    producer_loop<C4>(lds0, wid - 4, lane, make_srd(w_sp, SEQ * SEQ * 2), c.ws_srd(), (unsigned)pm * 128 * C4::LDA, (unsigned)WS_Z2 + pn * (SEQ * 224), 0);
  } else {
    const int g = lane >> 4, i16 = lane & 15;
    unsigned bs[2];
    f32x4 acc[C4::NT][C4::MT];
    consumer_loop<C4, 4>(acc, lds0, wid, lane, ws, [&] {
#pragma unroll
      for (int m = 0; m < 2; ++m) bs[m] = b_sp[pm * 128 + wid * 32 + m * 16 + i16]; });
    const unsigned stg = lds0 + wid * 32 * STG_RS;
#pragma unroll
    for (int n = 0; n < 7; ++n) { const u32x4 z1 = ldsld<u32x4>(lds0 + LDS_Z1 + (wid * 7 + n) * 1024 + lane * 16);
#pragma unroll
      for (int m = 0; m < 2; ++m) { const unsigned a = z1[2 * m], b = z1[2 * m + 1]; const f32x4 v = acc[n][m] + bflo(bs[m]);
        ldsst<u32x2>(stg + (m * 16 + i16) * STG_RS + n * 32 + g * 8, (u32x2){cvtpk(bflo(a) * v[0], bfhi(a) * v[1]), cvtpk(bflo(b) * v[2], bfhi(b) * v[3])}); } }
    waitl0();
    const unsigned gp = WS_G + pm * (28 * 16384);
#pragma unroll
    for (int i = 0; i < 7; ++i) { const unsigned ci = i * 64 + lane, r = ci / 14u, ch = ci - r * 14u, cc = pn * 14 + ch, row = wid * 32 + r;
      st16(c, gp + (cc >> 3) * 16384 + aimg(row, cc & 7), ldsld<u32x4>(stg + r * STG_RS + ch * 16)); }
  }
  ST0(4, 7);
}

struct Args { const u16 *x, *wqkv, *wphi, *wo, *w_in, *w_sp, *b_sp, *w_out; unsigned char* ws; u16* out; };
template<int P0, int P1> __global__ __launch_bounds__(512, 2) void k_main(Args a) {
  extern __shared__ __attribute__((aligned(16))) unsigned char shm[];
  Ctx c;
  c.lds0 = (unsigned)(size_t)(LAS unsigned char*)shm;
  c.tid = threadIdx.x; c.lane = c.tid & 63; c.wid = __builtin_amdgcn_readfirstlane(c.tid >> 6);
  { const int b = blockIdx.x, xl = b & 7, j = b >> 3;
    c.pm = (xl & 3) * 4 + (j & 3); c.pn = (xl >> 2) * 8 + (j >> 2); }
  c.ws = a.ws; c.rs = make_rsrc(a.ws, (unsigned)WS_END);
  if constexpr (P0 <= 1 && 1 <= P1) phase1(c, a.wqkv, a.wphi, a.wo, a.w_out);
  if constexpr (P0 <= 1 && 2 <= P1) meet(c, CNT_O + c.pm);
  if constexpr (P0 <= 2 && 2 <= P1) phase_res<C2, true, 2>(c, (unsigned)WS_O, a.wo, DM * DM * 2, a.x, nullptr);
  if constexpr (P0 <= 2 && 3 <= P1) meet(c, CNT_H + c.pm);
  if constexpr (P0 <= 3 && 3 <= P1) phase3(c, a.w_in, a.w_sp);
  if constexpr (P0 <= 3 && 4 <= P1) meet(c, CNT_Z2 + c.pn);
  if constexpr (P0 <= 4 && 4 <= P1) phase4(c, a.w_sp, a.b_sp);
  if constexpr (P0 <= 4 && 5 <= P1) meet(c, CNT_G + c.pm);
  if constexpr (P0 <= 5 && 5 <= P1) phase_res<C5, false, 5>(c, (unsigned)WS_G, a.w_out, FH * DM * 2, nullptr, a.out);
}
template<int P0, int P1> void launch_main(const Args& a, hipStream_t stream) {
  (void)hipFuncSetAttribute((const void*)k_main<P0, P1>, hipFuncAttributeMaxDynamicSharedMemorySize, LDS_TOTAL);
  k_main<P0, P1><<<256, 512, LDS_TOTAL, stream>>>(a);
}
}

extern "C" void kernel_launch(void* const* d_in, const int* in_sizes, int n_in, void* d_out, int out_size, void* d_ws, size_t ws_size, hipStream_t stream) {
  Args a;
  a.x = (const u16*)d_in[0]; a.wqkv = (const u16*)d_in[1]; a.wphi = (const u16*)d_in[2]; a.wo = (const u16*)d_in[3];
  a.w_in = (const u16*)d_in[5]; a.w_sp = (const u16*)d_in[6]; a.b_sp = (const u16*)d_in[7]; a.w_out = (const u16*)d_in[8];
  a.ws = (unsigned char*)d_ws; a.out = (u16*)d_out;
  (void)in_sizes; (void)n_in; (void)out_size; (void)ws_size;
  k0_prep<<<256, 256, 0, stream>>>(a.x, a.w_in, a.wqkv, a.ws);
  launch_main<1, 5>(a, stream);
}
```

```cpp
#include <hip/hip_runtime.h>
#include <hip/hip_bf16.h>
#include <utility>

namespace {
typedef short s16x4 __attribute__((ext_vector_type(4)));
typedef short s16x8 __attribute__((ext_vector_type(8)));
typedef __bf16 bf16x8 __attribute__((ext_vector_type(8)));
typedef __bf16 bf16x2 __attribute__((ext_vector_type(2)));
typedef float f32x2 __attribute__((ext_vector_type(2)));
typedef float f32x4 __attribute__((ext_vector_type(4)));
typedef int i32x4 __attribute__((ext_vector_type(4)));
typedef unsigned u32x4 __attribute__((ext_vector_type(4)));
typedef unsigned u32x2 __attribute__((ext_vector_type(2)));
typedef unsigned short u16;
#define LAS __attribute__((address_space(3)))
#define GAS __attribute__((address_space(1)))
#define DI __device__ __forceinline__
#define FENCE() __builtin_amdgcn_sched_barrier(0)
#define RLX_AGENT __ATOMIC_RELAXED, __HIP_MEMORY_SCOPE_AGENT

constexpr int SEQ = 2048, DM = 1024, FH = 1792, FF = 3584;
constexpr size_t WS_CTL = 0;
enum { CNT_KV = 0, FLG_O = 16, FLG_H = 32, CNT_ZS = 48, FLG_Z2 = 64, FLG_G = 80, CNT_TMO = 96, CNT_N = 128 };
constexpr size_t CTL_BYTES = CNT_N * 256;
constexpr size_t WS_XN = CTL_BYTES;
constexpr size_t WS_CSUM = WS_XN + (size_t)SEQ * DM * 2;
constexpr size_t WS_KVP = WS_CSUM + 4 * FF * 4;
constexpr size_t WS_ZP = WS_KVP + (size_t)16 * 16 * 8192;
constexpr size_t WS_O = WS_ZP + 16 * 16 * 512;
constexpr size_t WS_H = WS_O + (size_t)SEQ * DM * 2;
constexpr size_t WS_HST = WS_H + (size_t)SEQ * DM * 2;
constexpr size_t WS_Z2 = WS_HST + 16 * 16 * 128 * 8;
constexpr size_t WS_ZST = WS_Z2 + (size_t)SEQ * FH * 2;
constexpr size_t WS_G = WS_ZST + 16 * 16 * 128 * 8;
constexpr size_t WS_END = WS_G + (size_t)SEQ * FH * 2;

DI i32x4 make_srd(const void* p, unsigned bytes) {
  const unsigned long long a = (unsigned long long)p; i32x4 rs;
  rs[0] = __builtin_amdgcn_readfirstlane((int)(unsigned)a); rs[1] = __builtin_amdgcn_readfirstlane((int)(unsigned)(a >> 32)) & 0xffff;
  rs[2] = (int)bytes; rs[3] = 0x00020000; return rs;
}
DI __amdgpu_buffer_rsrc_t make_rsrc(const void* p, unsigned bytes) { return __builtin_amdgcn_make_buffer_rsrc(const_cast<void*>(p), 0, bytes, 0x00020000); }
template<bool SC1> DI void dma16(unsigned ldsaddr, unsigned voff, i32x4 rs, unsigned soff) {
  if constexpr (SC1) asm volatile("s_mov_b32 m0, %0\n\ts_nop 0\n\tbuffer_load_dwordx4 %1, %2, %3 offen sc1 lds" :: "s"(ldsaddr), "v"(voff), "s"(rs), "s"(soff));
  else asm volatile("s_mov_b32 m0, %0\n\ts_nop 0\n\tbuffer_load_dwordx4 %1, %2, %3 offen lds" :: "s"(ldsaddr), "v"(voff), "s"(rs), "s"(soff));
}
DI void touch64(unsigned ldsdummy, unsigned voff, i32x4 rs) {
  asm volatile("s_mov_b32 m0, %0\n\ts_nop 0\n\tbuffer_load_dword %1, %2, 0 offen lds" :: "s"(ldsdummy), "v"(voff), "s"(rs));
}
template<int N> DI void waitv() { asm volatile("s_waitcnt vmcnt(%0)" :: "i"(N) : "memory"); }
DI void waitl0() { asm volatile("" ::: "memory"); __builtin_amdgcn_s_waitcnt(0xC07F); asm volatile("" ::: "memory"); }
DI void bar() { asm volatile("s_barrier" ::: "memory"); }
DI void lbar() { waitl0(); bar(); }
DI unsigned cvtpk(float lo, float hi) { f32x2 v = {lo, hi}; bf16x2 b = __builtin_convertvector(v, bf16x2); return __builtin_bit_cast(unsigned, b); }
DI float bflo(unsigned u) { return __uint_as_float(u << 16); }
DI float bfhi(unsigned u) { return __uint_as_float(u & 0xffff0000u); }
DI bf16x8 asbf(u32x4 v) { return __builtin_bit_cast(bf16x8, v); }
DI f32x4 mfma(bf16x8 a, bf16x8 b, f32x4 c) { return __builtin_amdgcn_mfma_f32_16x16x32_bf16(a, b, c, 0, 0, 0); }
DI s16x4 trread(unsigned addr) { return __builtin_amdgcn_ds_read_tr16_b64_v4i16((LAS s16x4*)addr); }
DI bf16x8 join(s16x4 lo, s16x4 hi) { return __builtin_bit_cast(bf16x8, __builtin_shufflevector(lo, hi, 0, 1, 2, 3, 4, 5, 6, 7)); }
template<class T> DI T ldsld(unsigned addr) { return *(const LAS T*)addr; }
template<class T> DI void ldsst(unsigned addr, T v) { *(LAS T*)addr = v; }
template<class F> DI float red4(float x, F f) {
  const unsigned u = __float_as_uint(x);
  auto a = __builtin_amdgcn_permlane16_swap(u, u, false, false);
  const float y = f(__uint_as_float(a[0]), __uint_as_float(a[1]));
  const unsigned v = __float_as_uint(y);
  auto b = __builtin_amdgcn_permlane32_swap(v, v, false, false);
  return f(__uint_as_float(b[0]), __uint_as_float(b[1]));
}
DI float sum4(float x) { return red4(x, [](float a, float b) { return a + b; }); }
DI float max4(float x) { return red4(x, [](float a, float b) { return fmaxf(a, b); }); }
DI float min4(float x) { return red4(x, [](float a, float b) { return fminf(a, b); }); }

#define ST(k, i)
#define ST0(k, i)
#define ST4(k, i)
DI void wait_count(GAS unsigned* cnt, unsigned want, GAS unsigned* tmo) {
  for (unsigned spins = 0;; ++spins) {
    if ((unsigned)__builtin_amdgcn_readfirstlane(__hip_atomic_load(cnt, RLX_AGENT)) >= want) break;
    if (spins > (1u << 16)) { __hip_atomic_store(tmo, 1u, RLX_AGENT); break; }
    if ((spins & 255u) == 255u && __builtin_amdgcn_readfirstlane(__hip_atomic_load(tmo, RLX_AGENT))) break;
    __builtin_amdgcn_s_sleep(1);
  }
}

template<int SW> __host__ __device__ constexpr int bmask(int pos) { return SW == 64 ? ((pos >> 1) & 3) : 0; }
__host__ __device__ constexpr int kswap(int k) { return (k & ~12) | ((k & 4) << 1) | ((k & 8) >> 1); }

enum { A_IMG = 0, A_ROWS = 1 };
enum { B_W64 = 0, B_W112 = 1, B_IMG = 2 };
template<int SW_, int NS_, int WM_, int WN_, int ST_, int K_, int AMODE_, int LDA_, int BMODE_, int LDB_> struct Cfg {
  static constexpr int BM = 128, SW = SW_, NS = NS_, BN = SW_ * NS_, WM = WM_, WN = WN_, ST = ST_, K = K_, AMODE = AMODE_, LDA = LDA_, BMODE = BMODE_, LDB = LDB_;
  static constexpr int NKT = K / 64;
  static constexpr int MT = BM / WM / 16, NT = BN / WN / 16;
  static constexpr int A_BYTES = BM * 128, ROWB = SW * 2, STRIP_BYTES = 64 * ROWB, B_BYTES = NS * STRIP_BYTES, STAGE = A_BYTES + B_BYTES, RING = ST * STAGE;
  static constexpr int PA = 16, PB = B_BYTES / 1024, PT = PA + PB, NL = 4;
  static constexpr int IA = PA / NL, IBMAX = (PB + NL - 1) / NL, IBMIN = PB / NL, NBIG = PB - IBMIN * NL;
  static constexpr int PMAX = IA + IBMAX, PMIN = IA + IBMIN;
  static constexpr bool STRIP_PER_WAVE = (NS == WN) && (WN > 1);
  static constexpr int TPS = STRIP_PER_WAVE ? SW / 16 : SW / 16 / WN;
  static constexpr bool SWZ = (SW == 64);
  static constexpr int NV = SWZ ? TPS : 1;
  static constexpr int NM = MT * NT, NR = MT + 2 * NT;
  static_assert(WM * WN == 4 && STRIP_BYTES % 1024 == 0 && NT == (STRIP_PER_WAVE ? TPS : TPS * NS) && ST >= 3 && NKT >= ST + 1, "config");
  static_assert((ST - 1) * PMAX <= 63, "vmcnt range");
};
template<class C> struct Frags { bf16x8 a[C::MT]; s16x4 bl[C::NT], bh[C::NT]; };

template<class C, int I> DI void read_item(Frags<C>& f, unsigned aA, const unsigned (&aB)[C::NV], int ksimm) {
  if constexpr (I >= 2 && I < 2 + C::MT) {
    constexpr int m = I - 2;
    f.a[m] = ldsld<bf16x8>(aA + m * 2048);
  } else {
    constexpr int r = I < 2 ? I : I - C::MT, j = r >> 1, u = r & 1;
    constexpr int strip = C::STRIP_PER_WAVE ? 0 : j / C::TPS, tj = C::STRIP_PER_WAVE ? j : j % C::TPS;
    constexpr int imm = strip * C::STRIP_BYTES + u * 8 * C::ROWB + (C::SWZ ? 0 : tj * 32);
    const s16x4 v = trread(aB[C::SWZ ? tj : 0] + imm + ksimm);
    if constexpr (u == 0) f.bl[j] = v; else f.bh[j] = v;
  }
}
template<class C, int I> DI void mfma_item(f32x4 (&acc)[C::NT][C::MT], const Frags<C>& f) {
  constexpr int n = I / C::MT, m = I % C::MT;
  acc[n][m] = mfma(join(f.bl[n], f.bh[n]), f.a[m], acc[n][m]);
}
template<class C, int... Is>
DI void step_impl(f32x4 (&acc)[C::NT][C::MT], const Frags<C>& cur, Frags<C>& nxt, unsigned aA, const unsigned (&aB)[C::NV], int ksimm, std::integer_sequence<int, Is...>) {
  ((mfma_item<C, Is>(acc, cur), FENCE(),
    [&] { if constexpr (C::NR <= C::NM) { if constexpr (Is < C::NR) read_item<C, Is>(nxt, aA, aB, ksimm); }
          else { if constexpr (2 * Is < C::NR) read_item<C, 2 * Is>(nxt, aA, aB, ksimm); if constexpr (2 * Is + 1 < C::NR) read_item<C, 2 * Is + 1>(nxt, aA, aB, ksimm); } }(),
    FENCE()), ...);
}
template<class C> DI void step(f32x4 (&acc)[C::NT][C::MT], const Frags<C>& cur, Frags<C>& nxt, unsigned aA, const unsigned (&aB)[C::NV], int ksimm) {
  static_assert(2 * C::NM >= C::NR, "reads per mfma");
  step_impl<C>(acc, cur, nxt, aA, aB, ksimm, std::make_integer_sequence<int, C::NM>{});
}
template<class C, int... Is> DI void read_all_impl(Frags<C>& f, unsigned aA, const unsigned (&aB)[C::NV], std::integer_sequence<int, Is...>) { (read_item<C, Is>(f, aA, aB, 0), ...); }

struct NoMid { DI void operator()(int) const {} };
template<class C, int KID, class Hook, class Mid = NoMid>
DI void consumer_loop(f32x4 (&acc)[C::NT][C::MT], unsigned lds0, int wid, int lane, unsigned char* ws, Hook hook, Mid mid = Mid{}) {
  const int wc = wid % C::WN, wr = wid / C::WN;
  const int g = lane >> 4, i16 = lane & 15, q = i16 >> 2, p = lane & 3;
  unsigned aA0, aA1, aB[C::NV];
  { const int row = wr * (C::BM / C::WM) + i16, f = (row >> 1) & 7; aA0 = lds0 + row * 128 + (g ^ f) * 16; aA1 = aA0 ^ 64; }
  { const int pos = (g >> 1) * 16 + (g & 1) * 4 + q;
    const unsigned base = lds0 + C::A_BYTES + (C::STRIP_PER_WAVE ? wc * C::STRIP_BYTES : 0) + pos * C::ROWB + (p >> 1) * 16 + (p & 1) * 8;
    const int t0 = C::STRIP_PER_WAVE ? 0 : wc * C::TPS;
#pragma unroll
    for (int v = 0; v < C::NV; ++v) aB[v] = base + (C::SWZ ? (((t0 + v) ^ bmask<C::SW>(pos)) * 32) : t0 * 32); }
#pragma unroll
  for (int n = 0; n < C::NT; ++n)
#pragma unroll
    for (int m = 0; m < C::MT; ++m) acc[n][m] = (f32x4){0.f, 0.f, 0.f, 0.f};
  Frags<C> F0, F1;
  ST0(KID, 1);
  waitl0(); bar();
  ST0(KID, 2); waitl0();
  read_all_impl<C>(F0, aA0, aB, std::make_integer_sequence<int, C::NR>{});
  FENCE();
  hook();
  FENCE();
  int slot = 0;
#pragma unroll 1
  for (int t = 0; t < C::NKT; ++t) {
    unsigned a, b[C::NV];
    unsigned o = slot * C::STAGE;
    a = aA1 + o;
#pragma unroll
    for (int v = 0; v < C::NV; ++v) b[v] = aB[v] + o;
    step<C>(acc, F0, F1, a, b, 32 * C::ROWB);
    mid(t);
    slot = slot + 1 == C::ST ? 0 : slot + 1;
    waitl0(); bar();
    o = slot * C::STAGE;
    a = aA0 + o;
#pragma unroll
    for (int v = 0; v < C::NV; ++v) b[v] = aB[v] + o;
    step<C>(acc, F1, F0, a, b, 0);
  }
  waitl0();
  ST0(KID, 3);
}

template<class C> struct Loader {
  static constexpr int NVB = C::BMODE == B_W112 ? 7 : 1;
  unsigned va, vb[NVB]; int lw; unsigned lds0, a0, b0; i32x4 rsA, rsB;
  DI void init_a(int lane) {
    if constexpr (C::AMODE == A_IMG) va = lane * 16;
    else va = (lane >> 3) * C::LDA + (((lane & 7) ^ (((lw & 1) << 2) | (lane >> 4))) * 16);
  }
  DI void init_b(int lane) {
    if constexpr (C::BMODE == B_IMG) vb[0] = lane * 16;
    else if constexpr (C::BMODE == B_W64) vb[0] = (8 * (lane >> 5) + ((lane >> 3) & 3)) * C::LDB + (((lane & 7) ^ (((lane >> 4) & 3) << 1)) * 16);
    else {
#pragma unroll
      for (int i = 0; i < 7; ++i) { const unsigned lin = i * 64 + lane, pos = lin / 14u, ch = lin - pos * 14u; vb[i] = kswap(pos) * C::LDB + ch * 16; }
    }
  }
  template<int I> DI void piece_a(unsigned sa, int t) const {
    const int p = lw + I * 4;
    if constexpr (C::AMODE == A_IMG) dma16<true>(sa + p * 1024, va, rsA, a0 + t * 16384 + p * 1024);
    else dma16<false>(sa + p * 1024, va, rsA, a0 + p * (8 * C::LDA) + t * 128);
  }
  template<int I> DI void piece_b(unsigned sa, int t, unsigned stripbytes) const {
    if constexpr (C::BMODE == B_W112) dma16<false>(sa + C::A_BYTES + lw * 7168 + I * 1024, vb[I], rsB, b0 + t * (64 * C::LDB));
    else {
      const int p = lw + I * 4;
      if (I < C::IBMIN || lw < C::NBIG) {
        if constexpr (C::BMODE == B_IMG) dma16<true>(sa + C::A_BYTES + p * 1024, vb[0], rsB, b0 + t * C::B_BYTES + p * 1024);
        else { constexpr int strip = I / 2; const int pp = lw + (I & 1) * 4;
          dma16<false>(sa + C::A_BYTES + strip * 8192 + pp * 1024, vb[0], rsB, b0 + strip * stripbytes + (t * 64 + 16 * (pp >> 1) + 4 * (pp & 1)) * C::LDB); }
      }
    }
  }
  template<int... Is> DI void tile_a_impl(int t, int slot, std::integer_sequence<int, Is...>) const { const unsigned sa = lds0 + slot * C::STAGE; (piece_a<Is>(sa, t), ...); }
  template<int... Is> DI void tile_b_impl(int t, int slot, unsigned sb, std::integer_sequence<int, Is...>) const { const unsigned sa = lds0 + slot * C::STAGE; (piece_b<Is>(sa, t, sb), ...); }
  DI void tile_a(int t, int slot) const { tile_a_impl(t, slot, std::make_integer_sequence<int, C::IA>{}); }
  DI void tile_b(int t, int slot, unsigned sb) const { tile_b_impl(t, slot, sb, std::make_integer_sequence<int, C::IBMAX>{}); }
  template<int NA, int NB, int X = 0> DI void wait() const {
    if constexpr (C::NBIG == 0) waitv<NA * C::IA + NB * C::IBMAX + X>();
    else { if (lw < C::NBIG) waitv<NA * C::IA + NB * C::IBMAX + X>(); else waitv<NA * C::IA + NB * C::IBMIN + X>(); }
  }
};
struct NoDep { static constexpr bool ON = false; int kt0 = 0; DI void ensure(int, int) {} };
template<class NeedFn> struct Dep {
  static constexpr bool ON = true;
  int kt0; __amdgpu_buffer_rsrc_t rs; unsigned flagoff; GAS unsigned* tmo; NeedFn need; unsigned seen;
  DI void ensure(int kt, int lane) {
    const unsigned n = need(kt);
    for (unsigned spins = 0; (seen & n) != n; ++spins) {
      const unsigned v = __builtin_amdgcn_raw_buffer_load_b32(rs, flagoff + (lane & 15) * 4, 0, 16);
      seen = (unsigned)__ballot(v != 0u) & 0xffffu;
      if ((seen & n) == n) break;
      if (spins > (1u << 16)) { __hip_atomic_store(tmo, 1u, RLX_AGENT); seen = 0xffffu; }
      else if ((spins & 255u) == 255u && __builtin_amdgcn_readfirstlane(__hip_atomic_load(tmo, RLX_AGENT))) seen = 0xffffu;
      else __builtin_amdgcn_s_sleep(1);
    }
  }
};
struct NoSide { static constexpr int EXTRA = 0; DI void tile(int, unsigned) {} DI void late() {} DI void table() {} };
template<class C, bool DEPB, class D, class Side>
DI void producer_loop(unsigned lds0, int lw, int lane, i32x4 rsA, i32x4 rsB, unsigned a0, unsigned b0, unsigned stripbytes, D dep, Side& side) {
  constexpr int ST = C::ST, NKT = C::NKT;
  Loader<C> L; L.rsA = rsA; L.rsB = rsB; L.lw = lw; L.lds0 = lds0;
  L.a0 = __builtin_amdgcn_readfirstlane(a0); L.b0 = __builtin_amdgcn_readfirstlane(b0);
  const int kt0 = __builtin_amdgcn_readfirstlane(dep.kt0);
  auto ktof = [&](int i) { const int k = kt0 + i; return k >= NKT ? k - NKT : k; };
  auto dtile = [&](int i, int slot) { const int k = ktof(i); dep.ensure(k, lane); if (DEPB) L.tile_b(k, slot, stripbytes); else L.tile_a(k, slot); };
  auto wtile = [&](int i, int slot) { const int k = ktof(i); if (DEPB) L.tile_a(k, slot); else L.tile_b(k, slot, stripbytes); };
  if constexpr (!D::ON) {
    L.init_a(lane); L.tile_a(0, 0); FENCE();
    L.init_b(lane); FENCE();
    L.tile_b(0, 0, stripbytes); L.tile_a(1, 1); L.tile_b(1, 1, stripbytes);
    L.template wait<1, 1>(); bar();
#pragma unroll
    for (int s = 2; s < ST; ++s) { L.tile_a(s, s); L.tile_b(s, s, stripbytes); }
  } else {
    if (DEPB) { L.init_b(lane); L.tile_b(kt0, 0, stripbytes); FENCE(); L.init_a(lane); } else { L.init_a(lane); L.tile_a(kt0, 0); FENCE(); L.init_b(lane); }
    FENCE();
#pragma unroll
    for (int s = 0; s < ST; ++s) wtile(s, s);
    L.template wait<DEPB ? ST - 1 : 0, DEPB ? 0 : ST - 1>(); bar();
#pragma unroll
    for (int s = 1; s < ST; ++s) dtile(s, s);
  }
  side.tile(0, lds0);
  int slot = 0;
  auto advance = [&](int t) {
    dtile(t - 1 + ST, slot); wtile(t - 1 + ST, slot);
    slot = slot + 1 == ST ? 0 : slot + 1;
    side.tile(t, lds0 + slot * C::STAGE);
  };
  [&]<int... Is>(std::integer_sequence<int, Is...>) {
    (([&] { constexpr int t = Is + 1;
            if constexpr (!D::ON) L.template wait<ST - 2, ST - 2>();
            else if constexpr (DEPB) L.template wait<t - 1, ST - 2>(); else L.template wait<ST - 2, t - 1>();
            waitl0(); bar(); advance(t); }()), ...);
  }(std::make_integer_sequence<int, ST - 1>{});
#pragma unroll 1
  for (int t = ST; t <= NKT - ST; ++t) {
    L.template wait<ST - 2, ST - 2>(); waitl0(); bar();
    advance(t);
  }
  side.late();
  [&]<int... Is>(std::integer_sequence<int, Is...>) {
    (([&] { constexpr int left = ST - 2 - Is > 0 ? ST - 2 - Is : 0, t = NKT - ST + 1 + Is;
            L.template wait<left, left, Side::EXTRA>();
            if constexpr (t == NKT) side.table();
            waitl0(); bar();
            if constexpr (t < NKT) { slot = slot + 1 == ST ? 0 : slot + 1; side.tile(t, lds0 + slot * C::STAGE); } }()), ...);
  }(std::make_integer_sequence<int, ST>{});
}
template<int NLINES, class F> DI void prewarm(unsigned ldsdummy, int lw, int lane, i32x4 rs, F off) {
#pragma unroll
  for (int i = 0; i < (NLINES + 255) / 256; ++i) {
    const unsigned idx = (i * 4 + lw) * 64 + lane;
    if ((i * 4 + 4) * 64 <= NLINES || (i * 4 + lw) * 64 < NLINES) touch64(ldsdummy, off(idx < NLINES ? idx : NLINES - 1), rs);
  }
}
DI unsigned aimg(int r, int c) { return r * 128 + ((c ^ ((r >> 1) & 7)) * 16); }
DI void tile_of_block(int b, int rx, int& pm, int& pn) {
  const int xcd = b & 7, j = b >> 3, px = 16 / rx, cx = 32 / rx;
  pm = (xcd % px) * rx + j % rx; pn = (xcd / px) * cx + j / rx;
}

__global__ __launch_bounds__(256) void k0_prep(const u16* __restrict__ x, const u16* __restrict__ wqkv, const u16* __restrict__ w_in, unsigned char* __restrict__ ws) {
  const int tid = threadIdx.x, lane = tid & 63, wid = tid >> 6, b = blockIdx.x;
  ST0(0, 0);
  if (tid < 8) *(u32x4*)(ws + WS_CTL + b * 128 + tid * 16) = (u32x4){0u, 0u, 0u, 0u};
  static_assert(CTL_BYTES == 256 * 128, "control block");
  u32x4 v[2][2];
#pragma unroll
  for (int r = 0; r < 2; ++r)
#pragma unroll
    for (int c = 0; c < 2; ++c) v[r][c] = *(const u32x4*)(x + (size_t)(b * 8 + wid * 2 + r) * DM + c * 512 + lane * 8);
  f32x4 cs0 = {0, 0, 0, 0}, cs1 = {0, 0, 0, 0};
  const int cb = b % 56, kq = b / 56;
  u32x4 w[8];
  if (b < 224) {
#pragma unroll
    for (int i = 0; i < 8; ++i) w[i] = *(const u32x4*)(w_in + (size_t)(kq * 256 + i * 32 + (tid >> 3)) * FF + cb * 64 + (tid & 7) * 8);
  }
  unsigned pf = 0;
  if (tid < 192) pf = *(const unsigned*)((const char*)wqkv + (size_t)(b * 192 + tid) * 128);
#pragma unroll
  for (int r = 0; r < 2; ++r) {
    float f[16]; float s = 0;
#pragma unroll
    for (int c = 0; c < 2; ++c)
#pragma unroll
      for (int j = 0; j < 4; ++j) { f[c * 8 + 2 * j] = bflo(v[r][c][j]); f[c * 8 + 2 * j + 1] = bfhi(v[r][c][j]); }
#pragma unroll
    for (int j = 0; j < 16; ++j) s += f[j];
#pragma unroll
    for (int o = 1; o < 64; o <<= 1) s += __shfl_xor(s, o);
    const float mean = s * (1.0f / DM); float q = 0;
#pragma unroll
    for (int j = 0; j < 16; ++j) { f[j] -= mean; q += f[j] * f[j]; }
#pragma unroll
    for (int o = 1; o < 64; o <<= 1) q += __shfl_xor(q, o);
    const float rstd = 1.0f / sqrtf(q * (1.0f / DM) + 1e-5f);
    const int row = (b & 15) * 8 + wid * 2 + r;
#pragma unroll
    for (int c = 0; c < 2; ++c) { u32x4 o;
#pragma unroll
      for (int j = 0; j < 4; ++j) o[j] = cvtpk(f[c * 8 + 2 * j] * rstd, f[c * 8 + 2 * j + 1] * rstd);
      *(u32x4*)(ws + WS_XN + (size_t)((b >> 4) * 16 + c * 8 + (lane >> 3)) * 16384 + aimg(row, lane & 7)) = o; }
  }
  if (b < 224) {
    __shared__ f32x4 red[4][8][2];
#pragma unroll
    for (int i = 0; i < 8; ++i) { cs0 += (f32x4){bflo(w[i][0]), bfhi(w[i][0]), bflo(w[i][1]), bfhi(w[i][1])}; cs1 += (f32x4){bflo(w[i][2]), bfhi(w[i][2]), bflo(w[i][3]), bfhi(w[i][3])}; }
#pragma unroll
    for (int o = 8; o < 64; o <<= 1)
#pragma unroll
      for (int j = 0; j < 4; ++j) { cs0[j] += __shfl_xor(cs0[j], o); cs1[j] += __shfl_xor(cs1[j], o); }
    if (lane < 8) { red[wid][lane][0] = cs0; red[wid][lane][1] = cs1; }
    __syncthreads();
    if (tid < 16) { const f32x4 s = red[0][tid >> 1][tid & 1] + red[1][tid >> 1][tid & 1] + red[2][tid >> 1][tid & 1] + red[3][tid >> 1][tid & 1];
      *(f32x4*)(ws + WS_CSUM + ((size_t)kq * FF + cb * 64 + tid * 4) * 4) = s; }
  }
  asm volatile("" :: "v"(pf));
  ST0(0, 7);
}

struct Ctx {
  unsigned lds0; int tid, lane, wid, pm, pn;
  unsigned char* ws; __amdgpu_buffer_rsrc_t rs;
  DI GAS unsigned* cnt(int i) const { return (GAS unsigned*)(ws + WS_CTL) + i * 64; }
  DI i32x4 ws_srd() const { return make_srd(ws, (unsigned)WS_END); }
};
constexpr int AUX_SC1 = 16;
constexpr unsigned ZST_EMPTY = 0xFFFFFFFFu;
DI void st16(const Ctx& c, unsigned off, u32x4 v) { __builtin_amdgcn_raw_buffer_store_b128(v, c.rs, off, 0, AUX_SC1); }
DI u32x4 ld16(const Ctx& c, unsigned off) { return __builtin_amdgcn_raw_buffer_load_b128(c.rs, off, 0, AUX_SC1); }
DI f32x2 ld8(const Ctx& c, unsigned off) { return __builtin_bit_cast(f32x2, __builtin_amdgcn_raw_buffer_load_b64(c.rs, off, 0, AUX_SC1)); }
DI void st8(const Ctx& c, unsigned off, f32x2 v) { __builtin_amdgcn_raw_buffer_store_b64(__builtin_bit_cast(u32x2, v), c.rs, off, 0, AUX_SC1); }
DI void publish(const Ctx& c, int line, int word) {
  waitl0(); waitv<0>(); bar();
  if (c.wid == 4 && c.lane == 0) __builtin_amdgcn_raw_buffer_store_b32(1u, c.rs, WS_CTL + line * 256 + word * 4, 0, AUX_SC1);
}
template<class F> DI Dep<F> make_dep(const Ctx& c, bool on, int line, int own, int kt0, F need) {
  Dep<F> d{kt0, c.rs, (unsigned)WS_CTL + line * 256, c.cnt(CNT_TMO), need, on ? 1u << own : 0xffffu}; return d;
}
constexpr int LDS_Z1 = 122880;
constexpr int LDS_TAB = LDS_Z1 + 28672;
constexpr int LDS_DUMMY = LDS_TAB + 3072, LDS_TOTAL = LDS_DUMMY + 256;

using C1 = Cfg<64, 3, 2, 2, 3, DM, A_IMG, 0, B_W64, 3 * DM * 2>;
constexpr int K1_WPHI = C1::RING;
constexpr int K1_QX = 0, K1_KX = 16384;
constexpr int K1_PHI = 32768, PHI_RS = 136, PHI_HEAD = 128 * PHI_RS;
constexpr int K1_V = K1_PHI + 2 * PHI_HEAD, V_RS = 72, V_HEAD = 128 * V_RS;
constexpr int K1_KVT = K1_V + 2 * V_HEAD;
constexpr int K1_ZT = K1_KVT + 8192;
constexpr int K1_OST = K1_ZT + 256;
static_assert(K1_OST + 16384 <= C1::RING && K1_OST % 16 == 0 && K1_V % 8 == 0 && K1_WPHI + 4096 <= LDS_DUMMY, "P1 epilogue map");

DI float max3(float a, float b, float c) { float r; asm("v_max3_f32 %0, %1, %2, %3" : "=v"(r) : "v"(a), "v"(b), "v"(c)); return r; }
DI float min3(float a, float b, float c) { float r; asm("v_min3_f32 %0, %1, %2, %3" : "=v"(r) : "v"(a), "v"(b), "v"(c)); return r; }
DI void feature_maps(const bf16x8 wf0, const bf16x8 wf1, const bf16x8 (&fr)[2], u32x4 (&pos)[2], u32x4 (&neg)[2]) {
  constexpr float L2E = 1.4426950408889634f;
  const f32x4 z4 = {0.f, 0.f, 0.f, 0.f};
  f32x4 pa0 = mfma(wf0, fr[0], z4), pa1 = mfma(wf1, fr[0], z4), pb0 = mfma(wf0, fr[1], z4), pb1 = mfma(wf1, fr[1], z4);
  asm volatile("s_nop 7" : "+v"(pa0), "+v"(pa1), "+v"(pb0), "+v"(pb1));
  const f32x4 p[2][2] = {{pa0, pa1}, {pb0, pb1}};
  float mx[2], mn[2];
#pragma unroll
  for (int i = 0; i < 2; ++i) {
    mx[i] = max4(max3(max3(p[i][0][0], p[i][0][1], p[i][0][2]), max3(p[i][0][3], p[i][1][0], p[i][1][1]), max3(p[i][1][2], p[i][1][3], p[i][1][3])));
    mn[i] = min4(min3(min3(p[i][0][0], p[i][0][1], p[i][0][2]), min3(p[i][0][3], p[i][1][0], p[i][1][1]), min3(p[i][1][2], p[i][1][3], p[i][1][3])));
  }
#pragma unroll
  for (int i = 0; i < 2; ++i) {
    const float cx = -mx[i] * L2E, cn = mn[i] * L2E;
    float ep[8], en[8], sp = 0.f, sn = 0.f;
#pragma unroll
    for (int j = 0; j < 8; ++j) { const float xv = p[i][j >> 2][j & 3];
      ep[j] = __builtin_amdgcn_exp2f(__builtin_fmaf(xv, L2E, cx)); en[j] = __builtin_amdgcn_exp2f(__builtin_fmaf(xv, -L2E, cn)); sp += ep[j]; sn += en[j]; }
    const float rp = __builtin_amdgcn_rcpf(sum4(sp)), rn = __builtin_amdgcn_rcpf(sum4(sn));
#pragma unroll
    for (int j = 0; j < 4; ++j) { pos[i][j] = cvtpk(ep[2 * j] * rp, ep[2 * j + 1] * rp); neg[i][j] = cvtpk(en[2 * j] * rn, en[2 * j + 1] * rn); }
  }
}
DI u32x4 pack8(const f32x4 a, const f32x4 b) { return (u32x4){cvtpk(a[0], a[1]), cvtpk(a[2], a[3]), cvtpk(b[0], b[1]), cvtpk(b[2], b[3])}; }

DI void phase1(const Ctx& c, const u16* wqkv, const u16* wphi, const u16* wo, const u16* w_out) {
  const unsigned lds0 = c.lds0; const int tid = c.tid, lane = c.lane, wid = c.wid, pm = c.pm, pn = c.pn; unsigned char* ws = c.ws;
  const int g = lane >> 4, i16 = lane & 15, q4 = i16 >> 2, p4 = lane & 3;
  const int w4 = wid & 3, wr = w4 >> 1, wc = w4 & 1, mp = wid >> 2;
  ST0(1, 0);
  if (wid == 3) st16(c, WS_ZST + (pm * 16 + pn) * 1024 + lane * 16, (u32x4){ZST_EMPTY, ZST_EMPTY, ZST_EMPTY, ZST_EMPTY});
  if (wid < 4) {
    const u32x4 wp = *(const u32x4*)(wphi + (size_t)pn * 2048 + tid * 8);
    f32x4 acc[C1::NT][C1::MT];
    consumer_loop<C1, 1>(acc, lds0, wid, lane, ws, [&] { ldsst<u32x4>(lds0 + K1_WPHI + tid * 16, wp); });
#pragma unroll
    for (int m = 0; m < 4; ++m) {
      ldsst<u32x4>(lds0 + K1_KX + (w4 * 4 + m) * 1024 + lane * 16, pack8(acc[2][m], acc[3][m]));
      const u32x4 vv = pack8(acc[4][m], acc[5][m]);
      const unsigned va = lds0 + K1_V + wc * V_HEAD + (wr * 64 + m * 16 + i16) * V_RS + g * 8;
      ldsst<u32x2>(va, (u32x2){vv[0], vv[1]}); ldsst<u32x2>(va + 32, (u32x2){vv[2], vv[3]});
      ldsst<u32x4>(lds0 + K1_QX + (w4 * 4 + m) * 1024 + lane * 16, pack8(acc[0][m], acc[1][m]));
    }
    waitl0();
  } else {
    NoSide side;
    producer_loop<C1, false>(lds0, w4, lane, c.ws_srd(), make_srd(wqkv, DM * 3 * DM * 2), (unsigned)WS_XN + pm * (16 * 16384), (unsigned)pn * 128, DM * 2, NoDep{}, side);
    const unsigned b = blockIdx.x;
    prewarm<64>(lds0 + LDS_DUMMY, w4, lane, make_srd(wo, DM * DM * 2), [&](unsigned i) { return (b * 64 + i) * 128; });
    prewarm<112>(lds0 + LDS_DUMMY, 3 - w4, lane, make_srd(w_out, FH * DM * 2), [&](unsigned i) { return (b * 112 + i) * 128; });
  }
  bar();
  const unsigned wph = lds0 + K1_WPHI + wc * 2048 + (4 * g + q4) * 64 + p4 * 8;
  const bf16x8 wf0 = join(trread(wph), trread(wph + 1024)), wf1 = join(trread(wph + 32), trread(wph + 1024 + 32));
  { bf16x8 kf[2]; u32x4 pos[2], neg[2];
#pragma unroll
    for (int i = 0; i < 2; ++i) kf[i] = ldsld<bf16x8>(lds0 + K1_KX + (w4 * 4 + 2 * mp + i) * 1024 + lane * 16);
    feature_maps(wf0, wf1, kf, pos, neg);
#pragma unroll
    for (int i = 0; i < 2; ++i) {
      const unsigned pa = lds0 + K1_PHI + wc * PHI_HEAD + (wr * 64 + (2 * mp + i) * 16 + i16) * PHI_RS + g * 8;
      ldsst<u32x2>(pa, (u32x2){pos[i][0], pos[i][1]}); ldsst<u32x2>(pa + 32, (u32x2){pos[i][2], pos[i][3]});
      ldsst<u32x2>(pa + 64, (u32x2){neg[i][0], neg[i][1]}); ldsst<u32x2>(pa + 96, (u32x2){neg[i][2], neg[i][3]});
    } }
  lbar();
  ST0(1, 4);
  { f32x4 d[2] = {{0.f, 0.f, 0.f, 0.f}, {0.f, 0.f, 0.f, 0.f}}, dz = {0.f, 0.f, 0.f, 0.f};
    const bf16x8 ones = asbf((u32x4){0x3f803f80u, 0x3f803f80u, 0x3f803f80u, 0x3f803f80u});
    const unsigned pr = lds0 + K1_PHI + wc * PHI_HEAD + (4 * g + q4) * PHI_RS + wr * 64 + p4 * 8;
    const unsigned vr = lds0 + K1_V + wc * V_HEAD + (4 * g + q4) * V_RS + mp * 32 + p4 * 8;
    bf16x8 pf[4][2], vf[4];
#pragma unroll
    for (int s = 0; s < 4; ++s) {
#pragma unroll
      for (int a = 0; a < 2; ++a) pf[s][a] = join(trread(pr + s * 32 * PHI_RS + a * 32), trread(pr + (s * 32 + 16) * PHI_RS + a * 32));
      vf[s] = join(trread(vr + s * 32 * V_RS), trread(vr + (s * 32 + 16) * V_RS)); }
#pragma unroll
    for (int s = 0; s < 4; ++s) { d[0] = mfma(pf[s][0], vf[s], d[0]); d[1] = mfma(pf[s][1], vf[s], d[1]); dz = mfma(mp ? pf[s][1] : pf[s][0], ones, dz); }
    st16(c, WS_KVP + ((pn * 16 + pm) * 2 + wc) * 4096 + (wr * 2 + mp) * 1024 + lane * 16, pack8(d[0], d[1]));
    if (i16 == 0) st16(c, WS_ZP + ((pn * 16 + pm) * 2 + wc) * 256 + wr * 128 + g * 32 + mp * 16, __builtin_bit_cast(u32x4, dz)); }
  waitv<0>(); bar();
  if (wid == 4 && lane == 0) __hip_atomic_fetch_add(c.cnt(CNT_KV + pn), 1u, RLX_AGENT);
  u32x4 phiq[2][2];
  { bf16x8 qf[2]; u32x4 pos[2], neg[2];
#pragma unroll
    for (int i = 0; i < 2; ++i) qf[i] = ldsld<bf16x8>(lds0 + K1_QX + (w4 * 4 + 2 * mp + i) * 1024 + lane * 16);
    feature_maps(wf0, wf1, qf, pos, neg);
#pragma unroll
    for (int i = 0; i < 2; ++i) { phiq[i][0] = pos[i]; phiq[i][1] = neg[i]; } }
  if (wid == 4) wait_count(c.cnt(CNT_KV + pn), 16u, c.cnt(CNT_TMO));
  bar();
  ST0(1, 5);
  { const int head = wid & 1, fr = wid >> 1;
    u32x4 pv[16];
#pragma unroll
    for (int p = 0; p < 16; ++p) pv[p] = ld16(c, WS_KVP + ((pn * 16 + p) * 2 + head) * 4096 + fr * 1024 + lane * 16);
    float zs = 0.f;
    if (wid < 2) {
      float zv[16];
#pragma unroll
      for (int p = 0; p < 16; ++p) zv[p] = __uint_as_float(__builtin_amdgcn_raw_buffer_load_b32(c.rs, WS_ZP + ((pn * 16 + p) * 2 + head) * 256 + lane * 4, 0, AUX_SC1));
#pragma unroll
      for (int p = 0; p < 16; ++p) zs += zv[p];
    }
    f32x4 s0 = {0.f, 0.f, 0.f, 0.f}, s1 = s0;
#pragma unroll
    for (int p = 0; p < 16; ++p) { s0 += (f32x4){bflo(pv[p][0]), bfhi(pv[p][0]), bflo(pv[p][1]), bfhi(pv[p][1])}; s1 += (f32x4){bflo(pv[p][2]), bfhi(pv[p][2]), bflo(pv[p][3]), bfhi(pv[p][3])}; }
    ldsst<u32x4>(lds0 + K1_KVT + (head * 4 + fr) * 1024 + lane * 16, pack8(s0, s1));
    if (wid < 2) ldsst<u16>(lds0 + K1_ZT + head * 128 + lane * 2, (u16)cvtpk(zs, 0.f));
  }
  lbar();
  ST0(1, 6);
  { bf16x8 kv[2][2], zf[2];
#pragma unroll
    for (int ks = 0; ks < 2; ++ks) { zf[ks] = ldsld<bf16x8>(lds0 + K1_ZT + wc * 128 + ks * 64 + g * 16);
#pragma unroll
      for (int dt = 0; dt < 2; ++dt) kv[ks][dt] = ldsld<bf16x8>(lds0 + K1_KVT + (wc * 4 + ks * 2 + dt) * 1024 + lane * 16); }
#pragma unroll
    for (int i = 0; i < 2; ++i) {
      const f32x4 z4 = {0.f, 0.f, 0.f, 0.f};
      const bf16x8 b0 = asbf(phiq[i][0]), b1 = asbf(phiq[i][1]);
      const f32x4 den = mfma(zf[1], b1, mfma(zf[0], b0, z4));
      const f32x4 n0 = mfma(kv[1][0], b1, mfma(kv[0][0], b0, z4)), n1 = mfma(kv[1][1], b1, mfma(kv[0][1], b0, z4));
      const float r = 1.0f / (den[0] + 1e-6f);
      const int tok = wr * 64 + (2 * mp + i) * 16 + i16;
      const unsigned oa = lds0 + K1_OST + (g & 1) * 8;
      ldsst<u32x2>(oa + aimg(tok, wc * 4 + (g >> 1)), (u32x2){cvtpk(n0[0] * r, n0[1] * r), cvtpk(n0[2] * r, n0[3] * r)});
      ldsst<u32x2>(oa + aimg(tok, wc * 4 + 2 + (g >> 1)), (u32x2){cvtpk(n1[0] * r, n1[1] * r), cvtpk(n1[2] * r, n1[3] * r)});
    } }
  lbar();
#pragma unroll
  for (int i = 0; i < 2; ++i) { const int p = tid + i * 512; st16(c, WS_O + (pm * 16 + pn) * 16384 + p * 16, ldsld<u32x4>(lds0 + K1_OST + p * 16)); }
  ST0(1, 7);
}

using C2 = Cfg<64, 1, 2, 2, 4, DM, A_IMG, 0, B_W64, DM * 2>;
using C5 = Cfg<64, 1, 2, 2, 4, FH, A_IMG, 0, B_W64, DM * 2>;
template<class C, bool FIRST, int KID> DI void phase_res(const Ctx& c, bool dep_on, unsigned a_off, const u16* W, unsigned w_bytes, const u16* x, u16* out) {
  const unsigned lds0 = c.lds0; const int tid = c.tid, lane = c.lane, wid = c.wid, pm = c.pm, pn = c.pn; unsigned char* ws = c.ws;
  ST0(KID, 0);
  if (wid >= 4) {
    NoSide side;
    if constexpr (FIRST)
      producer_loop<C, false>(lds0, wid - 4, lane, c.ws_srd(), make_srd(W, w_bytes), a_off + (unsigned)pm * (C::NKT * 16384), (unsigned)pn * 128, 0,
                              make_dep(c, dep_on, FLG_O + pm, pn, pn, [](int kt) { return 1u << kt; }), side);
    else
      producer_loop<C, false>(lds0, wid - 4, lane, c.ws_srd(), make_srd(W, w_bytes), a_off + (unsigned)pm * (C::NKT * 16384), (unsigned)pn * 128, 0,
                              make_dep(c, dep_on, FLG_G + pm, pn, (7 * pn + 3) >> 2, [](int kt) { return (1u << ((4u * kt) / 7u)) | (1u << ((8u * kt + 7u) / 14u)); }), side);
    bar();
  } else {
    u32x4 rv[4];
    f32x4 acc[C::NT][C::MT];
    consumer_loop<C, KID>(acc, lds0, wid, lane, ws, [&] {
#pragma unroll
      for (int i = 0; i < 4; ++i) { const int p = tid + i * 256, row = p >> 3, cc = (p & 7) ^ ((row >> 1) & 7);
        rv[i] = FIRST ? *(const u32x4*)((const unsigned char*)x + (size_t)(pm * 128 + row) * (DM * 2) + pn * 128 + cc * 16) : ld16(c, WS_H + (pm * 16 + pn) * 16384 + p * 16); } });
    const int g = lane >> 4, i16 = lane & 15, wr = wid >> 1, wc = wid & 1;
#pragma unroll
    for (int n = 0; n < 2; ++n)
#pragma unroll
      for (int m = 0; m < 4; ++m)
        ldsst<u32x2>(lds0 + aimg(wr * 64 + m * 16 + i16, wc * 4 + n * 2 + (g >> 1)) + (g & 1) * 8, (u32x2){cvtpk(acc[n][m][0], acc[n][m][1]), cvtpk(acc[n][m][2], acc[n][m][3])});
    lbar();
#pragma unroll
    for (int i = 0; i < 4; ++i) { const int p = tid + i * 256, row = p >> 3, cc = (p & 7) ^ ((row >> 1) & 7);
      const u32x4 a = ldsld<u32x4>(lds0 + p * 16); u32x4 o; float s = 0.f, qq = 0.f;
#pragma unroll
      for (int j = 0; j < 4; ++j) { o[j] = cvtpk(bflo(rv[i][j]) + bflo(a[j]), bfhi(rv[i][j]) + bfhi(a[j]));
        if (FIRST) { const float lo = bflo(o[j]), hi = bfhi(o[j]); s += lo + hi; qq += lo * lo + hi * hi; } }
      if (FIRST) {
        st16(c, WS_H + (pm * 16 + pn) * 16384 + p * 16, o);
#pragma unroll
        for (int of = 1; of < 8; of <<= 1) { s += __shfl_xor(s, of); qq += __shfl_xor(qq, of); }
        if ((p & 7) == 0) st8(c, WS_HST + ((pm * 16 + pn) * 128 + row) * 8, (f32x2){s, qq});
      } else *(u32x4*)((unsigned char*)out + (size_t)(pm * 128 + row) * (DM * 2) + pn * 128 + cc * 16) = o;
    }
  }
  ST0(KID, 7);
}

using C3 = Cfg<112, 2, 2, 2, 3, DM, A_IMG, 0, B_W112, FF * 2>;
constexpr int K3_RST = LDS_TAB;
constexpr int K3_CS = K3_RST + 1024;
constexpr int K3_XF = 0;
constexpr int K3_STG = 32768, STG_RS = 240;
static_assert(C3::RING <= LDS_TAB && K3_STG + 8 * 16 * STG_RS <= LDS_Z1, "P3 map");
DI f32x2 gelu2(f32x2 x) {
  constexpr float C0 = -2.0f * 0.7978845608028654f * 1.4426950408889634f, C1_ = C0 * 0.044715f;
  const f32x2 a = x * (x * x * C1_ + C0);
  const f32x2 d = (f32x2){__builtin_amdgcn_exp2f(a[0]), __builtin_amdgcn_exp2f(a[1])} + 1.0f;
  return x * (f32x2){__builtin_amdgcn_rcpf(d[0]), __builtin_amdgcn_rcpf(d[1])};
}
DI unsigned gelu_pk(unsigned u) { const f32x2 y = gelu2((f32x2){bflo(u), bfhi(u)}); return cvtpk(y[0], y[1]); }
DI float dot2(unsigned a, unsigned b, float c) { return __builtin_amdgcn_fdot2_f32_bf16(__builtin_bit_cast(bf16x2, a), __builtin_bit_cast(bf16x2, b), c, false); }
struct Side3 {
  static constexpr int EXTRA = 12;
  const Ctx& c; f32x2 st[8]; float cs4[4];
  DI Side3(const Ctx& c_) : c(c_) {}
  DI void tile(int, unsigned) {}
  DI void late() {
    const int t = c.tid - 256;
#pragma unroll
    for (int p = 0; p < 8; ++p) st[p] = ld8(c, WS_HST + (((c.pm * 16 + (t & 1) * 8) + p) * 128 + (t >> 1)) * 8);
    const float* cs = (const float*)(c.ws + WS_CSUM) + (t < 112 ? c.pn * 112 + t : t < 224 ? FH + c.pn * 112 + t - 112 : 0);
#pragma unroll
    for (int p = 0; p < 4; ++p) cs4[p] = cs[p * FF];
  }
  DI void table() {
    const int t = c.tid - 256;
    f32x2 s = (st[0] + st[1]) + (st[2] + st[3]) + ((st[4] + st[5]) + (st[6] + st[7]));
    s[0] += __shfl_xor(s[0], 1); s[1] += __shfl_xor(s[1], 1);
    const float mean = s[0] * (1.0f / DM), var = fmaxf(s[1] * (1.0f / DM) - mean * mean, 0.f);
    if ((t & 1) == 0) ldsst<f32x2>(c.lds0 + K3_RST + (t >> 1) * 8, (f32x2){mean, 1.0f / sqrtf(var + 1e-5f)});
    if (t < 224) ldsst<float>(c.lds0 + K3_CS + (t < 112 ? t : t + 16) * 4, (cs4[0] + cs4[1]) + (cs4[2] + cs4[3]));
  }
};
DI void phase3(const Ctx& c, bool dep_on, const u16* w_in, const u16* w_sp) {
  const unsigned lds0 = c.lds0; const int lane = c.lane, wid = c.wid, pm = c.pm, pn = c.pn; unsigned char* ws = c.ws;
  const int g = lane >> 4, i16 = lane & 15;
  const int w4 = wid & 3, wr = w4 >> 1, wc = w4 & 1;
  ST0(3, 0);
  if (wid < 4) {
    f32x4 acc[C3::NT][C3::MT];
    consumer_loop<C3, 3>(acc, lds0, wid, lane, ws, [] {});
    f32x2 rs[4]; f32x4 cs[7];
#pragma unroll
    for (int m = 0; m < 4; ++m) rs[m] = ldsld<f32x2>(lds0 + K3_RST + (wr * 64 + m * 16 + i16) * 8);
#pragma unroll
    for (int n = 0; n < 7; ++n) cs[n] = ldsld<f32x4>(lds0 + K3_CS + wc * 512 + (n * 16 + g * 4) * 4);
    u32x2 w[7][4];
#pragma unroll
    for (int m = 0; m < 4; ++m) { const float mr = -rs[m][0] * rs[m][1], r = rs[m][1];
#pragma unroll
      for (int n = 0; n < 7; ++n) { const f32x4 v = acc[n][m] * r + cs[n] * mr; w[n][m] = (u32x2){cvtpk(v[0], v[1]), cvtpk(v[2], v[3])}; } }
    if (wc == 0) {
#pragma unroll
      for (int n = 0; n < 7; ++n)
#pragma unroll
        for (int mp = 0; mp < 2; ++mp)
          ldsst<u32x4>(lds0 + LDS_Z1 + ((wr * 2 + mp) * 7 + n) * 1024 + lane * 16, (u32x4){w[n][2 * mp][0], w[n][2 * mp][1], w[n][2 * mp + 1][0], w[n][2 * mp + 1][1]});
    } else {
#pragma unroll
      for (int n = 0; n < 7; ++n)
#pragma unroll
        for (int m = 0; m < 4; ++m) ldsst<u32x2>(lds0 + K3_XF + ((wr * 4 + m) * 7 + n) * 512 + lane * 8, w[n][m]);
    }
    lbar();
  } else {
    Side3 side(c);
    producer_loop<C3, false>(lds0, w4, lane, c.ws_srd(), make_srd(w_in, DM * FF * 2), (unsigned)WS_H + pm * (16 * 16384),
                             (unsigned)((w4 & 1) * 32 * C3::LDB + ((w4 >> 1) * FH + pn * 112) * 2), 0, make_dep(c, dep_on, FLG_H + pm, pn, pn, [](int kt) { return 1u << kt; }), side);
    prewarm<256>(lds0 + LDS_DUMMY, w4, lane, make_srd(w_sp, SEQ * SEQ * 2), [&](unsigned i) { return (blockIdx.x * 256 + i) * 128; });
    bar();
  }
  u32x2 z[7];
#pragma unroll
  for (int n = 0; n < 7; ++n) z[n] = ldsld<u32x2>(lds0 + K3_XF + (wid * 7 + n) * 512 + lane * 8);
  float s = 0.f, qq = 0.f;
#pragma unroll
  for (int n = 0; n < 7; ++n) { z[n] = (u32x2){gelu_pk(z[n][0]), gelu_pk(z[n][1])};
    s = dot2(z[n][1], 0x3f803f80u, dot2(z[n][0], 0x3f803f80u, s)); qq = dot2(z[n][1], z[n][1], dot2(z[n][0], z[n][0], qq)); }
  s = sum4(s); qq = sum4(qq);
  const int row = wid * 16 + i16;
  if (g == 0) { const unsigned qb = __float_as_uint(qq);
    st8(c, WS_ZST + ((pm * 16 + pn) * 128 + row) * 8, (f32x2){s, __uint_as_float(qb == ZST_EMPTY ? 0x7fc00000u : qb)}); }
  ST0(3, 4);
  {
    u32x2 v[7];
#pragma unroll
    for (int i = 0; i < 7; ++i) { const int h = wid * 7 + i; v[i] = ldsld<u32x2>(lds0 + LDS_Z1 + (h >> 1) * 1024 + lane * 16 + (h & 1) * 8); }
#pragma unroll
    for (int i = 0; i < 7; ++i) { const int h = wid * 7 + i; ldsst<u32x2>(lds0 + LDS_Z1 + (h >> 1) * 1024 + lane * 16 + (h & 1) * 8, (u32x2){gelu_pk(v[i][0]), gelu_pk(v[i][1])}); } }
  f32x2 t;
  for (unsigned spins = 0;; ++spins) {
    f32x2 w[4];
#pragma unroll
    for (int p = 0; p < 4; ++p) w[p] = ld8(c, WS_ZST + ((pm * 16 + 4 * g + p) * 128 + row) * 8);
    bool ok = true;
#pragma unroll
    for (int p = 0; p < 4; ++p) ok &= __float_as_uint(w[p][1]) != ZST_EMPTY;
    t = (w[0] + w[1]) + (w[2] + w[3]);
    if (__all(ok)) break;
    if (spins > (1u << 16)) { __hip_atomic_store(c.cnt(CNT_TMO), 1u, RLX_AGENT); break; }
    if ((spins & 255u) == 255u && __builtin_amdgcn_readfirstlane(__hip_atomic_load(c.cnt(CNT_TMO), RLX_AGENT))) break;
    __builtin_amdgcn_s_sleep(1);
  }
  ST0(3, 5);
  const float mean = sum4(t[0]) * (1.0f / FH), var = fmaxf(sum4(t[1]) * (1.0f / FH) - mean * mean, 0.f), rstd = 1.0f / sqrtf(var + 1e-5f);
  const unsigned stg = lds0 + K3_STG + wid * 16 * STG_RS;
#pragma unroll
  for (int n = 0; n < 7; ++n)
    ldsst<u32x2>(stg + kswap(i16) * STG_RS + n * 32 + g * 8,
                 (u32x2){cvtpk((bflo(z[n][0]) - mean) * rstd, (bfhi(z[n][0]) - mean) * rstd), cvtpk((bflo(z[n][1]) - mean) * rstd, (bfhi(z[n][1]) - mean) * rstd)});
  waitl0();
  const unsigned z2 = WS_Z2 + (pn * SEQ + pm * 128 + wid * 16) * 224;
#pragma unroll
  for (int i = 0; i < 4; ++i) { const unsigned ci = i * 64 + lane, r = ci / 14u, ch = ci - r * 14u;
    if (i < 3 || lane < 32) st16(c, z2 + ci * 16, ldsld<u32x4>(stg + r * STG_RS + ch * 16)); }
  ST0(3, 7);
}

using C4 = Cfg<112, 1, 4, 1, 4, SEQ, A_ROWS, SEQ * 2, B_IMG, 0>;
static_assert(C4::RING <= LDS_Z1, "P4 map");
DI void phase4(const Ctx& c, bool dep_on, const u16* w_sp, const u16* b_sp) {
  const unsigned lds0 = c.lds0; const int lane = c.lane, wid = c.wid, pm = c.pm, pn = c.pn; unsigned char* ws = c.ws;
  ST0(4, 0);
  if (wid >= 4) {
    NoSide side;
    producer_loop<C4, true>(lds0, wid - 4, lane, make_srd(w_sp, SEQ * SEQ * 2), c.ws_srd(), (unsigned)pm * 128 * C4::LDA, (unsigned)WS_Z2 + pn * (SEQ * 224), 0,
                            make_dep(c, dep_on, FLG_Z2 + pn, pm, 2 * pm, [](int kt) { return 1u << (kt >> 1); }), side);
  } else {
    const int g = lane >> 4, i16 = lane & 15;
    unsigned bs[2];
    f32x4 acc[C4::NT][C4::MT];
    consumer_loop<C4, 4>(acc, lds0, wid, lane, ws, [&] {
#pragma unroll
      for (int m = 0; m < 2; ++m) bs[m] = b_sp[pm * 128 + wid * 32 + m * 16 + i16]; });
    const unsigned stg = lds0 + wid * 32 * STG_RS;
#pragma unroll
    for (int n = 0; n < 7; ++n) { const u32x4 z1 = ldsld<u32x4>(lds0 + LDS_Z1 + (wid * 7 + n) * 1024 + lane * 16);
#pragma unroll
      for (int m = 0; m < 2; ++m) { const unsigned a = z1[2 * m], b = z1[2 * m + 1]; const f32x4 v = acc[n][m] + bflo(bs[m]);
        ldsst<u32x2>(stg + (m * 16 + i16) * STG_RS + n * 32 + g * 8, (u32x2){cvtpk(bflo(a) * v[0], bfhi(a) * v[1]), cvtpk(bflo(b) * v[2], bfhi(b) * v[3])}); } }
    waitl0();
    const unsigned gp = WS_G + pm * (28 * 16384);
#pragma unroll
    for (int i = 0; i < 7; ++i) { const unsigned ci = i * 64 + lane, r = ci / 14u, ch = ci - r * 14u, cc = pn * 14 + ch, row = wid * 32 + r;
      st16(c, gp + (cc >> 3) * 16384 + aimg(row, cc & 7), ldsld<u32x4>(stg + r * STG_RS + ch * 16)); }
  }
  ST0(4, 7);
}

struct Args { const u16 *x, *wqkv, *wphi, *wo, *w_in, *w_sp, *b_sp, *w_out; unsigned char* ws; u16* out; };
template<int P0, int P1> __global__ __launch_bounds__(512, 2) void k_main(Args a) {
  extern __shared__ __attribute__((aligned(16))) unsigned char shm[];
  Ctx c;
  c.lds0 = (unsigned)(size_t)(LAS unsigned char*)shm;
  c.tid = threadIdx.x; c.lane = c.tid & 63; c.wid = __builtin_amdgcn_readfirstlane(c.tid >> 6);
  { const int b = blockIdx.x, xl = b & 7, j = b >> 3;
    c.pm = (xl & 3) * 4 + (j & 3); c.pn = (xl >> 2) * 8 + (j >> 2); }
  c.ws = a.ws; c.rs = make_rsrc(a.ws, (unsigned)WS_END);
  if constexpr (P0 <= 1 && 1 <= P1) phase1(c, a.wqkv, a.wphi, a.wo, a.w_out);
  if constexpr (P0 <= 1 && 2 <= P1) publish(c, FLG_O + c.pm, c.pn);
  if constexpr (P0 <= 2 && 2 <= P1) phase_res<C2, true, 2>(c, P0 < 2, (unsigned)WS_O, a.wo, DM * DM * 2, a.x, nullptr);
  if constexpr (P0 <= 2 && 3 <= P1) publish(c, FLG_H + c.pm, c.pn);
  if constexpr (P0 <= 3 && 3 <= P1) phase3(c, P0 < 3, a.w_in, a.w_sp);
  if constexpr (P0 <= 3 && 4 <= P1) publish(c, FLG_Z2 + c.pn, c.pm);
  if constexpr (P0 <= 4 && 4 <= P1) phase4(c, P0 < 4, a.w_sp, a.b_sp);
  if constexpr (P0 <= 4 && 5 <= P1) publish(c, FLG_G + c.pm, c.pn);
  if constexpr (P0 <= 5 && 5 <= P1) phase_res<C5, false, 5>(c, P0 < 5, (unsigned)WS_G, a.w_out, FH * DM * 2, nullptr, a.out);
}
template<int P0, int P1> void launch_main(const Args& a, hipStream_t stream) {
  (void)hipFuncSetAttribute((const void*)k_main<P0, P1>, hipFuncAttributeMaxDynamicSharedMemorySize, LDS_TOTAL);
  k_main<P0, P1><<<256, 512, LDS_TOTAL, stream>>>(a);
}
}

extern "C" void kernel_launch(void* const* d_in, const int* in_sizes, int n_in, void* d_out, int out_size, void* d_ws, size_t ws_size, hipStream_t stream) {
  Args a;
  a.x = (const u16*)d_in[0]; a.wqkv = (const u16*)d_in[1]; a.wphi = (const u16*)d_in[2]; a.wo = (const u16*)d_in[3];
  a.w_in = (const u16*)d_in[5]; a.w_sp = (const u16*)d_in[6]; a.b_sp = (const u16*)d_in[7]; a.w_out = (const u16*)d_in[8];
  a.ws = (unsigned char*)d_ws; a.out = (u16*)d_out;
  (void)in_sizes; (void)n_in; (void)out_size; (void)ws_size;
  k0_prep<<<256, 256, 0, stream>>>(a.x, a.wqkv, a.w_in, a.ws);
  launch_main<1, 5>(a, stream);
}
```

```cpp
#include <hip/hip_runtime.h>
#include <hip/hip_bf16.h>
#include <utility>

namespace {
typedef short s16x4 __attribute__((ext_vector_type(4)));
typedef short s16x8 __attribute__((ext_vector_type(8)));
typedef __bf16 bf16x8 __attribute__((ext_vector_type(8)));
typedef __bf16 bf16x2 __attribute__((ext_vector_type(2)));
typedef float f32x2 __attribute__((ext_vector_type(2)));
typedef float f32x4 __attribute__((ext_vector_type(4)));
typedef int i32x4 __attribute__((ext_vector_type(4)));
typedef unsigned u32x4 __attribute__((ext_vector_type(4)));
typedef unsigned u32x2 __attribute__((ext_vector_type(2)));
typedef unsigned short u16;
#define LAS __attribute__((address_space(3)))
#define GAS __attribute__((address_space(1)))
#define DI __device__ __forceinline__
#define FENCE() __builtin_amdgcn_sched_barrier(0)
#define RLX_AGENT __ATOMIC_RELAXED, __HIP_MEMORY_SCOPE_AGENT

constexpr int SEQ = 2048, DM = 1024, FH = 1792, FF = 3584;
constexpr size_t WS_CTL = 0;
enum { CNT_KV = 0, FLG_O = 16, FLG_H = 32, CNT_ZS = 48, FLG_Z2 = 64, FLG_G = 80, CNT_TMO = 96, CNT_N = 128 };
constexpr size_t CTL_BYTES = CNT_N * 256;
constexpr size_t WS_XN = CTL_BYTES;
constexpr size_t WS_CSUM = WS_XN + (size_t)SEQ * DM * 2;
constexpr size_t WS_KVP = WS_CSUM + 4 * FF * 4;
constexpr size_t WS_ZP = WS_KVP + (size_t)16 * 16 * 8192;
constexpr size_t WS_O = WS_ZP + 16 * 16 * 512;
constexpr size_t WS_H = WS_O + (size_t)SEQ * DM * 2;
constexpr size_t WS_HST = WS_H + (size_t)SEQ * DM * 2;
constexpr size_t WS_Z2 = WS_HST + 16 * 16 * 128 * 8;
constexpr size_t WS_ZST = WS_Z2 + (size_t)SEQ * FH * 2;
constexpr size_t WS_G = WS_ZST + 16 * 16 * 128 * 8;
constexpr size_t WS_END = WS_G + (size_t)SEQ * FH * 2;

DI i32x4 make_srd(const void* p, unsigned bytes) {
  const unsigned long long a = (unsigned long long)p; i32x4 rs;
  rs[0] = __builtin_amdgcn_readfirstlane((int)(unsigned)a); rs[1] = __builtin_amdgcn_readfirstlane((int)(unsigned)(a >> 32)) & 0xffff;
  rs[2] = (int)bytes; rs[3] = 0x00020000; return rs;
}
DI __amdgpu_buffer_rsrc_t make_rsrc(const void* p, unsigned bytes) { return __builtin_amdgcn_make_buffer_rsrc(const_cast<void*>(p), 0, bytes, 0x00020000); }
template<bool SC1> DI void dma16(unsigned ldsaddr, unsigned voff, i32x4 rs, unsigned soff) {
  if constexpr (SC1) asm volatile("s_mov_b32 m0, %0\n\ts_nop 0\n\tbuffer_load_dwordx4 %1, %2, %3 offen sc1 lds" :: "s"(ldsaddr), "v"(voff), "s"(rs), "s"(soff));
  else asm volatile("s_mov_b32 m0, %0\n\ts_nop 0\n\tbuffer_load_dwordx4 %1, %2, %3 offen lds" :: "s"(ldsaddr), "v"(voff), "s"(rs), "s"(soff));
}
DI void touch64(unsigned ldsdummy, unsigned voff, i32x4 rs) {
  asm volatile("s_mov_b32 m0, %0\n\ts_nop 0\n\tbuffer_load_dword %1, %2, 0 offen lds" :: "s"(ldsdummy), "v"(voff), "s"(rs));
}
template<int N> DI void waitv() { asm volatile("s_waitcnt vmcnt(%0)" :: "i"(N) : "memory"); }
DI void waitl0() { asm volatile("" ::: "memory"); __builtin_amdgcn_s_waitcnt(0xC07F); asm volatile("" ::: "memory"); }
DI void bar() { asm volatile("s_barrier" ::: "memory"); }
DI void lbar() { waitl0(); bar(); }
DI unsigned cvtpk(float lo, float hi) { f32x2 v = {lo, hi}; bf16x2 b = __builtin_convertvector(v, bf16x2); return __builtin_bit_cast(unsigned, b); }
DI float bflo(unsigned u) { return __uint_as_float(u << 16); }
DI float bfhi(unsigned u) { return __uint_as_float(u & 0xffff0000u); }
DI bf16x8 asbf(u32x4 v) { return __builtin_bit_cast(bf16x8, v); }
DI f32x4 mfma(bf16x8 a, bf16x8 b, f32x4 c) { return __builtin_amdgcn_mfma_f32_16x16x32_bf16(a, b, c, 0, 0, 0); }
DI s16x4 trread(unsigned addr) { return __builtin_amdgcn_ds_read_tr16_b64_v4i16((LAS s16x4*)addr); }
DI bf16x8 join(s16x4 lo, s16x4 hi) { return __builtin_bit_cast(bf16x8, __builtin_shufflevector(lo, hi, 0, 1, 2, 3, 4, 5, 6, 7)); }
template<class T> DI T ldsld(unsigned addr) { return *(const LAS T*)addr; }
template<class T> DI void ldsst(unsigned addr, T v) { *(LAS T*)addr = v; }
template<class F> DI float red4(float x, F f) {
  const unsigned u = __float_as_uint(x);
  auto a = __builtin_amdgcn_permlane16_swap(u, u, false, false);
  const float y = f(__uint_as_float(a[0]), __uint_as_float(a[1]));
  const unsigned v = __float_as_uint(y);
  auto b = __builtin_amdgcn_permlane32_swap(v, v, false, false);
  return f(__uint_as_float(b[0]), __uint_as_float(b[1]));
}
template<int CTRL, int ROWMASK> DI float dpp_add(float x) {
  return x + __int_as_float(__builtin_amdgcn_update_dpp(0, __float_as_int(x), CTRL, ROWMASK, 0xf, true));
}
DI float wave_sum(float x) {
  x = dpp_add<0x111, 0xf>(x); x = dpp_add<0x112, 0xf>(x); x = dpp_add<0x114, 0xf>(x); x = dpp_add<0x118, 0xf>(x);
  x = dpp_add<0x142, 0xa>(x); x = dpp_add<0x143, 0xc>(x);
  return __int_as_float(__builtin_amdgcn_readlane(__float_as_int(x), 63));
}
DI float sum4(float x) { return red4(x, [](float a, float b) { return a + b; }); }
DI float max4(float x) { return red4(x, [](float a, float b) { return fmaxf(a, b); }); }
DI float min4(float x) { return red4(x, [](float a, float b) { return fminf(a, b); }); }

#define ST(k, i)
#define ST0(k, i)
#define ST4(k, i)
DI void wait_count(GAS unsigned* cnt, unsigned want, GAS unsigned* tmo) {
  for (unsigned spins = 0;; ++spins) {
    if ((unsigned)__builtin_amdgcn_readfirstlane(__hip_atomic_load(cnt, RLX_AGENT)) >= want) break;
    if (spins > (1u << 16)) { __hip_atomic_store(tmo, 1u, RLX_AGENT); break; }
    if ((spins & 255u) == 255u && __builtin_amdgcn_readfirstlane(__hip_atomic_load(tmo, RLX_AGENT))) break;
    __builtin_amdgcn_s_sleep(1);
  }
}

template<int SW> __host__ __device__ constexpr int bmask(int pos) { return SW == 64 ? ((pos >> 1) & 3) : 0; }
__host__ __device__ constexpr int kswap(int k) { return (k & ~12) | ((k & 4) << 1) | ((k & 8) >> 1); }

enum { A_IMG = 0, A_ROWS = 1 };
enum { B_W64 = 0, B_W112 = 1, B_IMG = 2 };
template<int SW_, int NS_, int WM_, int WN_, int ST_, int K_, int AMODE_, int LDA_, int BMODE_, int LDB_> struct Cfg {
  static constexpr int BM = 128, SW = SW_, NS = NS_, BN = SW_ * NS_, WM = WM_, WN = WN_, ST = ST_, K = K_, AMODE = AMODE_, LDA = LDA_, BMODE = BMODE_, LDB = LDB_;
  static constexpr int NKT = K / 64;
  static constexpr int MT = BM / WM / 16, NT = BN / WN / 16;
  static constexpr int A_BYTES = BM * 128, ROWB = SW * 2, STRIP_BYTES = 64 * ROWB, B_BYTES = NS * STRIP_BYTES, STAGE = A_BYTES + B_BYTES, RING = ST * STAGE;
  static constexpr int PA = 16, PB = B_BYTES / 1024, PT = PA + PB, NL = 4;
  static constexpr int IA = PA / NL, IBMAX = (PB + NL - 1) / NL, IBMIN = PB / NL, NBIG = PB - IBMIN * NL;
  static constexpr int PMAX = IA + IBMAX, PMIN = IA + IBMIN;
  static constexpr bool STRIP_PER_WAVE = (NS == WN) && (WN > 1);
  static constexpr int TPS = STRIP_PER_WAVE ? SW / 16 : SW / 16 / WN;
  static constexpr bool SWZ = (SW == 64);
  static constexpr int NV = SWZ ? TPS : 1;
  static constexpr int NM = MT * NT, NR = MT + 2 * NT;
  static_assert(WM * WN == 4 && STRIP_BYTES % 1024 == 0 && NT == (STRIP_PER_WAVE ? TPS : TPS * NS) && ST >= 3 && NKT >= ST + 1, "config");
  static_assert((ST - 1) * PMAX <= 63, "vmcnt range");
};
template<class C> struct Frags { bf16x8 a[C::MT]; s16x4 bl[C::NT], bh[C::NT]; };

template<class C, int I> DI void read_item(Frags<C>& f, unsigned aA, const unsigned (&aB)[C::NV], int ksimm) {
  if constexpr (I >= 2 && I < 2 + C::MT) {
    constexpr int m = I - 2;
    f.a[m] = ldsld<bf16x8>(aA + m * 2048);
  } else {
    constexpr int r = I < 2 ? I : I - C::MT, j = r >> 1, u = r & 1;
    constexpr int strip = C::STRIP_PER_WAVE ? 0 : j / C::TPS, tj = C::STRIP_PER_WAVE ? j : j % C::TPS;
    constexpr int imm = strip * C::STRIP_BYTES + u * 8 * C::ROWB + (C::SWZ ? 0 : tj * 32);
    const s16x4 v = trread(aB[C::SWZ ? tj : 0] + imm + ksimm);
    if constexpr (u == 0) f.bl[j] = v; else f.bh[j] = v;
  }
}
template<class C, int I> DI void mfma_item(f32x4 (&acc)[C::NT][C::MT], const Frags<C>& f) {
  constexpr int n = I / C::MT, m = I % C::MT;
  acc[n][m] = mfma(join(f.bl[n], f.bh[n]), f.a[m], acc[n][m]);
}
template<class C, int... Is>
DI void step_impl(f32x4 (&acc)[C::NT][C::MT], const Frags<C>& cur, Frags<C>& nxt, unsigned aA, const unsigned (&aB)[C::NV], int ksimm, std::integer_sequence<int, Is...>) {
  ((mfma_item<C, Is>(acc, cur), FENCE(),
    [&] { if constexpr (C::NR <= C::NM) { if constexpr (Is < C::NR) read_item<C, Is>(nxt, aA, aB, ksimm); }
          else { if constexpr (2 * Is < C::NR) read_item<C, 2 * Is>(nxt, aA, aB, ksimm); if constexpr (2 * Is + 1 < C::NR) read_item<C, 2 * Is + 1>(nxt, aA, aB, ksimm); } }(),
    FENCE()), ...);
}
template<class C> DI void step(f32x4 (&acc)[C::NT][C::MT], const Frags<C>& cur, Frags<C>& nxt, unsigned aA, const unsigned (&aB)[C::NV], int ksimm) {
  static_assert(2 * C::NM >= C::NR, "reads per mfma");
  step_impl<C>(acc, cur, nxt, aA, aB, ksimm, std::make_integer_sequence<int, C::NM>{});
}
template<class C, int... Is> DI void read_all_impl(Frags<C>& f, unsigned aA, const unsigned (&aB)[C::NV], std::integer_sequence<int, Is...>) { (read_item<C, Is>(f, aA, aB, 0), ...); }

struct NoMid { DI void operator()(int) const {} };
template<class C, int KID, class Hook, class Mid = NoMid>
DI void consumer_loop(f32x4 (&acc)[C::NT][C::MT], unsigned lds0, int wid, int lane, unsigned char* ws, Hook hook, Mid mid = Mid{}) {
  const int wc = wid % C::WN, wr = wid / C::WN;
  const int g = lane >> 4, i16 = lane & 15, q = i16 >> 2, p = lane & 3;
  unsigned aA0, aA1, aB[C::NV];
  { const int row = wr * (C::BM / C::WM) + i16, f = (row >> 1) & 7; aA0 = lds0 + row * 128 + (g ^ f) * 16; aA1 = aA0 ^ 64; }
  { const int pos = (g >> 1) * 16 + (g & 1) * 4 + q;
    const unsigned base = lds0 + C::A_BYTES + (C::STRIP_PER_WAVE ? wc * C::STRIP_BYTES : 0) + pos * C::ROWB + (p >> 1) * 16 + (p & 1) * 8;
    const int t0 = C::STRIP_PER_WAVE ? 0 : wc * C::TPS;
#pragma unroll
    for (int v = 0; v < C::NV; ++v) aB[v] = base + (C::SWZ ? (((t0 + v) ^ bmask<C::SW>(pos)) * 32) : t0 * 32); }
#pragma unroll
  for (int n = 0; n < C::NT; ++n)
#pragma unroll
    for (int m = 0; m < C::MT; ++m) { acc[n][m] = (f32x4){0.f, 0.f, 0.f, 0.f}; asm volatile("" : "+v"(acc[n][m])); }
  Frags<C> F0, F1;
  ST0(KID, 1);
  waitl0(); bar();
  ST0(KID, 2); waitl0();
  read_all_impl<C>(F0, aA0, aB, std::make_integer_sequence<int, C::NR>{});
  FENCE();
  hook();
  FENCE();
  int slot = 0;
#pragma unroll 1
  for (int t = 0; t < C::NKT; ++t) {
    unsigned a, b[C::NV];
    unsigned o = slot * C::STAGE;
    a = aA1 + o;
#pragma unroll
    for (int v = 0; v < C::NV; ++v) b[v] = aB[v] + o;
    step<C>(acc, F0, F1, a, b, 32 * C::ROWB);
    mid(t);
    slot = slot + 1 == C::ST ? 0 : slot + 1;
    waitl0(); bar();
    o = slot * C::STAGE;
    a = aA0 + o;
#pragma unroll
    for (int v = 0; v < C::NV; ++v) b[v] = aB[v] + o;
    step<C>(acc, F1, F0, a, b, 0);
  }
  waitl0();
  ST0(KID, 3);
}

template<class C> struct Loader {
  static constexpr int NVB = C::BMODE == B_W112 ? 7 : 1;
  unsigned va, vb[NVB]; int lw; unsigned lds0, a0, b0; i32x4 rsA, rsB;
  DI void init_a(int lane) {
    if constexpr (C::AMODE == A_IMG) va = lane * 16;
    else va = (lane >> 3) * C::LDA + (((lane & 7) ^ (((lw & 1) << 2) | (lane >> 4))) * 16);
  }
  DI void init_b(int lane) {
    if constexpr (C::BMODE == B_IMG) vb[0] = lane * 16;
    else if constexpr (C::BMODE == B_W64) vb[0] = (8 * (lane >> 5) + ((lane >> 3) & 3)) * C::LDB + (((lane & 7) ^ (((lane >> 4) & 3) << 1)) * 16);
    else {
#pragma unroll
      for (int i = 0; i < 7; ++i) { const unsigned lin = i * 64 + lane, pos = lin / 14u, ch = lin - pos * 14u; vb[i] = kswap(pos) * C::LDB + ch * 16; }
    }
  }
  template<int I> DI void piece_a(unsigned sa, int t) const {
    const int p = lw + I * 4;
    if constexpr (C::AMODE == A_IMG) dma16<true>(sa + p * 1024, va, rsA, a0 + t * 16384 + p * 1024);
    else dma16<false>(sa + p * 1024, va, rsA, a0 + p * (8 * C::LDA) + t * 128);
  }
  template<int I> DI void piece_b(unsigned sa, int t, unsigned stripbytes) const {
    if constexpr (C::BMODE == B_W112) dma16<false>(sa + C::A_BYTES + lw * 7168 + I * 1024, vb[I], rsB, b0 + t * (64 * C::LDB));
    else {
      const int p = lw + I * 4;
      if (I < C::IBMIN || lw < C::NBIG) {
        if constexpr (C::BMODE == B_IMG) dma16<true>(sa + C::A_BYTES + p * 1024, vb[0], rsB, b0 + t * C::B_BYTES + p * 1024);
        else { constexpr int strip = I / 2; const int pp = lw + (I & 1) * 4;
          dma16<false>(sa + C::A_BYTES + strip * 8192 + pp * 1024, vb[0], rsB, b0 + strip * stripbytes + (t * 64 + 16 * (pp >> 1) + 4 * (pp & 1)) * C::LDB); }
      }
    }
  }
  template<int... Is> DI void tile_a_impl(int t, int slot, std::integer_sequence<int, Is...>) const { const unsigned sa = lds0 + slot * C::STAGE; (piece_a<Is>(sa, t), ...); }
  template<int... Is> DI void tile_b_impl(int t, int slot, unsigned sb, std::integer_sequence<int, Is...>) const { const unsigned sa = lds0 + slot * C::STAGE; (piece_b<Is>(sa, t, sb), ...); }
  DI void tile_a(int t, int slot) const { tile_a_impl(t, slot, std::make_integer_sequence<int, C::IA>{}); }
  DI void tile_b(int t, int slot, unsigned sb) const { tile_b_impl(t, slot, sb, std::make_integer_sequence<int, C::IBMAX>{}); }
  template<int NA, int NB, int X = 0> DI void wait() const {
    if constexpr (C::NBIG == 0) waitv<NA * C::IA + NB * C::IBMAX + X>();
    else { if (lw < C::NBIG) waitv<NA * C::IA + NB * C::IBMAX + X>(); else waitv<NA * C::IA + NB * C::IBMIN + X>(); }
  }
};
struct NoDep { static constexpr bool ON = false; int kt0 = 0; DI void ensure(int, int) {} };
template<class NeedFn> struct Dep {
  static constexpr bool ON = true;
  int kt0; __amdgpu_buffer_rsrc_t rs; unsigned flagoff; GAS unsigned* tmo; NeedFn need; unsigned seen;
  DI void ensure(int kt, int lane) {
    const unsigned n = need(kt);
    for (unsigned spins = 0; (seen & n) != n; ++spins) {
      const unsigned v = __builtin_amdgcn_raw_buffer_load_b32(rs, flagoff + (lane & 15) * 4, 0, 16);
      seen |= (unsigned)__ballot(v != 0u) & 0xffffu;
      if ((seen & n) == n) break;
      if (spins > (1u << 16)) { __hip_atomic_store(tmo, 1u, RLX_AGENT); seen = 0xffffu; }
      else if ((spins & 255u) == 255u && __builtin_amdgcn_readfirstlane(__hip_atomic_load(tmo, RLX_AGENT))) seen = 0xffffu;
      else __builtin_amdgcn_s_sleep(1);
    }
  }
};
struct NoSide { static constexpr int EXTRA = 0; DI void tile(int, unsigned) {} DI void late() {} DI void table() {} };
template<class C, bool DEPB, int NPRE, class D, class Side>
DI void producer_loop(unsigned lds0, int lw, int lane, i32x4 rsA, i32x4 rsB, unsigned a0, unsigned b0, unsigned stripbytes, D dep, Side& side) {
  constexpr int ST = C::ST, NKT = C::NKT;
  Loader<C> L; L.rsA = rsA; L.rsB = rsB; L.lw = lw; L.lds0 = lds0;
  L.a0 = __builtin_amdgcn_readfirstlane(a0); L.b0 = __builtin_amdgcn_readfirstlane(b0);
  const int kt0 = __builtin_amdgcn_readfirstlane(dep.kt0);
  auto ktof = [&](int i) { const int k = kt0 + i; return k >= NKT ? k - NKT : k; };
  auto dtile = [&](int i, int slot) { const int k = ktof(i); dep.ensure(k, lane); if (DEPB) L.tile_b(k, slot, stripbytes); else L.tile_a(k, slot); };
  auto wtile = [&](int i, int slot) { const int k = ktof(i); if (DEPB) L.tile_a(k, slot); else L.tile_b(k, slot, stripbytes); };
  if constexpr (!D::ON) {
    L.init_a(lane); L.tile_a(0, 0); FENCE();
    L.init_b(lane); FENCE();
    L.tile_b(0, 0, stripbytes); L.tile_a(1, 1); L.tile_b(1, 1, stripbytes);
    L.template wait<1, 1>(); bar();
#pragma unroll
    for (int s = 2; s < ST; ++s) { L.tile_a(s, s); L.tile_b(s, s, stripbytes); }
  } else {
    if (DEPB) { L.init_b(lane); L.tile_b(kt0, 0, stripbytes); FENCE(); L.init_a(lane); } else { L.init_a(lane); L.tile_a(kt0, 0); FENCE(); L.init_b(lane); }
    FENCE();
#pragma unroll
    for (int s = NPRE; s < ST; ++s) wtile(s, s);
    constexpr int WY = NPRE > 0 ? ST - NPRE : ST - 1;
    L.template wait<DEPB ? WY : 0, DEPB ? 0 : WY>(); bar();
#pragma unroll
    for (int s = 1; s < ST; ++s) dtile(s, s);
  }
  side.tile(0, lds0);
  int slot = 0;
  auto advance = [&](int t) {
    dtile(t - 1 + ST, slot); wtile(t - 1 + ST, slot);
    slot = slot + 1 == ST ? 0 : slot + 1;
    side.tile(t, lds0 + slot * C::STAGE);
  };
  [&]<int... Is>(std::integer_sequence<int, Is...>) {
    (([&] { constexpr int t = Is + 1;
            if constexpr (!D::ON) L.template wait<ST - 2, ST - 2>();
            else if constexpr (DEPB) L.template wait<t - 1, ST - 2>(); else L.template wait<ST - 2, t - 1>();
            waitl0(); bar(); advance(t); }()), ...);
  }(std::make_integer_sequence<int, ST - 1>{});
#pragma unroll 1
  for (int t = ST; t <= NKT - ST; ++t) {
    L.template wait<ST - 2, ST - 2>(); waitl0(); bar();
    advance(t);
  }
  [&]<int... Is>(std::integer_sequence<int, Is...>) {
    (([&] { constexpr int left = ST - 2 - Is > 0 ? ST - 2 - Is : 0, t = NKT - ST + 1 + Is;
            L.template wait<left, left, Is == 0 ? 0 : Side::EXTRA>();
            if constexpr (t == NKT) side.table();
            waitl0(); bar();
            if constexpr (Is == 0) side.late();
            if constexpr (t < NKT) { slot = slot + 1 == ST ? 0 : slot + 1; side.tile(t, lds0 + slot * C::STAGE); } }()), ...);
  }(std::make_integer_sequence<int, ST>{});
}
template<class C, bool DEPB, int NPRE> DI void prefetch_weights(unsigned lds0, int lw, int lane, i32x4 rsW, unsigned w0, int kt0) {
  Loader<C> L; L.lw = lw; L.lds0 = lds0;
  if constexpr (DEPB) { L.rsA = rsW; L.a0 = __builtin_amdgcn_readfirstlane(w0); L.init_a(lane); } else { L.rsB = rsW; L.b0 = __builtin_amdgcn_readfirstlane(w0); L.init_b(lane); }
  kt0 = __builtin_amdgcn_readfirstlane(kt0);
#pragma unroll
  for (int s = 0; s < NPRE; ++s) { const int k = kt0 + s >= C::NKT ? kt0 + s - C::NKT : kt0 + s; if constexpr (DEPB) L.tile_a(k, s); else L.tile_b(k, s, 0); }
}
template<int NLINES, class F> DI void prewarm(unsigned ldsdummy, int lw, int lane, i32x4 rs, F off) {
#pragma unroll
  for (int i = 0; i < (NLINES + 255) / 256; ++i) {
    const unsigned idx = (i * 4 + lw) * 64 + lane;
    if ((i * 4 + 4) * 64 <= NLINES || (i * 4 + lw) * 64 < NLINES) touch64(ldsdummy, off(idx < NLINES ? idx : NLINES - 1), rs);
  }
}
DI unsigned aimg(int r, int c) { return r * 128 + ((c ^ ((r >> 1) & 7)) * 16); }
DI void tile_of_block(int b, int rx, int& pm, int& pn) {
  const int xcd = b & 7, j = b >> 3, px = 16 / rx, cx = 32 / rx;
  pm = (xcd % px) * rx + j % rx; pn = (xcd / px) * cx + j / rx;
}

__global__ __launch_bounds__(256) void k0_prep(const u16* __restrict__ x, const u16* __restrict__ wqkv, const u16* __restrict__ w_in, unsigned char* __restrict__ ws) {
  const int tid = threadIdx.x, lane = tid & 63, wid = tid >> 6, b = blockIdx.x;
  ST0(0, 0);
  if (tid < 8) *(u32x4*)(ws + WS_CTL + b * 128 + tid * 16) = (u32x4){0u, 0u, 0u, 0u};
  static_assert(CTL_BYTES == 256 * 128, "control block");
  u32x4 v[2][2];
#pragma unroll
  for (int r = 0; r < 2; ++r)
#pragma unroll
    for (int c = 0; c < 2; ++c) v[r][c] = *(const u32x4*)(x + (size_t)(b * 8 + wid * 2 + r) * DM + c * 512 + lane * 8);
  f32x4 cs0 = {0, 0, 0, 0}, cs1 = {0, 0, 0, 0};
  const int cb = b % 56, kq = b / 56;
  u32x4 w[8];
  if (b < 224) {
#pragma unroll
    for (int i = 0; i < 8; ++i) w[i] = *(const u32x4*)(w_in + (size_t)(kq * 256 + i * 32 + (tid >> 3)) * FF + cb * 64 + (tid & 7) * 8);
  }
  unsigned pf = 0;
  if (tid < 192) pf = *(const unsigned*)((const char*)wqkv + (size_t)(b * 192 + tid) * 128);
#pragma unroll
  for (int r = 0; r < 2; ++r) {
    float f[16]; float s = 0;
#pragma unroll
    for (int c = 0; c < 2; ++c)
#pragma unroll
      for (int j = 0; j < 4; ++j) { f[c * 8 + 2 * j] = bflo(v[r][c][j]); f[c * 8 + 2 * j + 1] = bfhi(v[r][c][j]); }
#pragma unroll
    for (int j = 0; j < 16; ++j) s += f[j];
    const float mean = wave_sum(s) * (1.0f / DM); float q = 0;
#pragma unroll
    for (int j = 0; j < 16; ++j) { f[j] -= mean; q += f[j] * f[j]; }
    const float rstd = __builtin_amdgcn_rsqf(wave_sum(q) * (1.0f / DM) + 1e-5f);
    const int row = (b & 15) * 8 + wid * 2 + r;
#pragma unroll
    for (int c = 0; c < 2; ++c) { u32x4 o;
#pragma unroll
      for (int j = 0; j < 4; ++j) o[j] = cvtpk(f[c * 8 + 2 * j] * rstd, f[c * 8 + 2 * j + 1] * rstd);
      *(u32x4*)(ws + WS_XN + (size_t)((b >> 4) * 16 + c * 8 + (lane >> 3)) * 16384 + aimg(row, lane & 7)) = o; }
  }
  if (b < 224) {
    __shared__ f32x4 red[4][8][2];
#pragma unroll
    for (int i = 0; i < 8; ++i) { cs0 += (f32x4){bflo(w[i][0]), bfhi(w[i][0]), bflo(w[i][1]), bfhi(w[i][1])}; cs1 += (f32x4){bflo(w[i][2]), bfhi(w[i][2]), bflo(w[i][3]), bfhi(w[i][3])}; }
#pragma unroll
    for (int o = 8; o < 64; o <<= 1)
#pragma unroll
      for (int j = 0; j < 4; ++j) { cs0[j] += __shfl_xor(cs0[j], o); cs1[j] += __shfl_xor(cs1[j], o); }
    if (lane < 8) { red[wid][lane][0] = cs0; red[wid][lane][1] = cs1; }
    __syncthreads();
    if (tid < 16) { const f32x4 s = red[0][tid >> 1][tid & 1] + red[1][tid >> 1][tid & 1] + red[2][tid >> 1][tid & 1] + red[3][tid >> 1][tid & 1];
      *(f32x4*)(ws + WS_CSUM + ((size_t)kq * FF + cb * 64 + tid * 4) * 4) = s; }
  }
  asm volatile("" :: "v"(pf));
  ST0(0, 7);
}

struct Ctx {
  unsigned lds0; int tid, lane, wid, pm, pn;
  unsigned char* ws; __amdgpu_buffer_rsrc_t rs;
  DI GAS unsigned* cnt(int i) const { return (GAS unsigned*)(ws + WS_CTL) + i * 64; }
  DI i32x4 ws_srd() const { return make_srd(ws, (unsigned)WS_END); }
};
constexpr int AUX_SC1 = 16;
constexpr unsigned ZST_EMPTY = 0xFFFFFFFFu;
DI void st16(const Ctx& c, unsigned off, u32x4 v) { __builtin_amdgcn_raw_buffer_store_b128(v, c.rs, off, 0, AUX_SC1); }
DI u32x4 ld16(const Ctx& c, unsigned off) { return __builtin_amdgcn_raw_buffer_load_b128(c.rs, off, 0, AUX_SC1); }
DI f32x2 ld8(const Ctx& c, unsigned off) { return __builtin_bit_cast(f32x2, __builtin_amdgcn_raw_buffer_load_b64(c.rs, off, 0, AUX_SC1)); }
DI void st8(const Ctx& c, unsigned off, f32x2 v) { __builtin_amdgcn_raw_buffer_store_b64(__builtin_bit_cast(u32x2, v), c.rs, off, 0, AUX_SC1); }
DI void publish(const Ctx& c, int line, int word) {
  waitl0(); waitv<0>(); bar();
  if (c.wid == 4 && c.lane == 0) __builtin_amdgcn_raw_buffer_store_b32(1u, c.rs, WS_CTL + line * 256 + word * 4, 0, AUX_SC1);
}
template<class F> DI Dep<F> make_dep(const Ctx& c, bool on, int line, int own, int kt0, F need) {
  Dep<F> d{kt0, c.rs, (unsigned)WS_CTL + line * 256, c.cnt(CNT_TMO), need, on ? 1u << own : 0xffffu}; return d;
}
constexpr int LDS_Z1 = 122880;
constexpr int LDS_TAB = LDS_Z1 + 28672;
constexpr int LDS_DUMMY = LDS_TAB + 3072, LDS_TOTAL = LDS_DUMMY + 256;

using C1 = Cfg<64, 3, 2, 2, 3, DM, A_IMG, 0, B_W64, 3 * DM * 2>;
using C2 = Cfg<64, 1, 2, 2, 4, DM, A_IMG, 0, B_W64, DM * 2>;
using C3 = Cfg<112, 2, 2, 2, 3, DM, A_IMG, 0, B_W112, FF * 2>;
using C4 = Cfg<112, 1, 4, 1, 4, SEQ, A_ROWS, SEQ * 2, B_IMG, 0>;
using C5 = Cfg<64, 1, 2, 2, 4, FH, A_IMG, 0, B_W64, DM * 2>;
constexpr int PRE2 = 4, PRE3 = 1, PRE4 = 4, PRE5 = 4;
DI unsigned w3_off(int lw, int pn) { return (unsigned)((lw & 1) * 32 * C3::LDB + ((lw >> 1) * FH + pn * 112) * 2); }
DI int kt0_5(int pn) { return (7 * pn + 3) >> 2; }

constexpr int K1_WPHI = C1::RING;
constexpr int K1_QX = 0, K1_KX = 16384;
constexpr int K1_PHI = 32768, PHI_RS = 136, PHI_HEAD = 128 * PHI_RS;
constexpr int K1_V = K1_PHI + 2 * PHI_HEAD, V_RS = 72, V_HEAD = 128 * V_RS;
constexpr int K1_KVT = C2::RING;
constexpr int K1_ZT = K1_KVT + 8192;
constexpr int K1_OST = K1_ZT + 256;
static_assert(K1_V + 2 * V_HEAD <= K1_KVT && K1_OST + 16384 <= LDS_DUMMY && K1_OST % 16 == 0 && K1_V % 8 == 0 && K1_WPHI + 4096 <= LDS_DUMMY, "P1 epilogue map");

DI float max3(float a, float b, float c) { float r; asm("v_max3_f32 %0, %1, %2, %3" : "=v"(r) : "v"(a), "v"(b), "v"(c)); return r; }
DI float min3(float a, float b, float c) { float r; asm("v_min3_f32 %0, %1, %2, %3" : "=v"(r) : "v"(a), "v"(b), "v"(c)); return r; }
DI void feature_maps(const bf16x8 wf0, const bf16x8 wf1, const bf16x8 (&fr)[2], u32x4 (&pos)[2], u32x4 (&neg)[2]) {
  constexpr float L2E = 1.4426950408889634f;
  const f32x4 z4 = {0.f, 0.f, 0.f, 0.f};
  f32x4 pa0 = mfma(wf0, fr[0], z4), pa1 = mfma(wf1, fr[0], z4), pb0 = mfma(wf0, fr[1], z4), pb1 = mfma(wf1, fr[1], z4);
  asm volatile("s_nop 7" : "+v"(pa0), "+v"(pa1), "+v"(pb0), "+v"(pb1));
  const f32x4 p[2][2] = {{pa0, pa1}, {pb0, pb1}};
  float mx[2], mn[2];
#pragma unroll
  for (int i = 0; i < 2; ++i) {
    mx[i] = max4(max3(max3(p[i][0][0], p[i][0][1], p[i][0][2]), max3(p[i][0][3], p[i][1][0], p[i][1][1]), max3(p[i][1][2], p[i][1][3], p[i][1][3])));
    mn[i] = min4(min3(min3(p[i][0][0], p[i][0][1], p[i][0][2]), min3(p[i][0][3], p[i][1][0], p[i][1][1]), min3(p[i][1][2], p[i][1][3], p[i][1][3])));
  }
#pragma unroll
  for (int i = 0; i < 2; ++i) {
    const float cx = -mx[i] * L2E, cn = mn[i] * L2E;
    float ep[8], en[8], sp = 0.f, sn = 0.f;
#pragma unroll
    for (int j = 0; j < 8; ++j) { const float xv = p[i][j >> 2][j & 3];
      ep[j] = __builtin_amdgcn_exp2f(__builtin_fmaf(xv, L2E, cx)); en[j] = __builtin_amdgcn_exp2f(__builtin_fmaf(xv, -L2E, cn)); sp += ep[j]; sn += en[j]; }
    const float rp = __builtin_amdgcn_rcpf(sum4(sp)), rn = __builtin_amdgcn_rcpf(sum4(sn));
#pragma unroll
    for (int j = 0; j < 4; ++j) { pos[i][j] = cvtpk(ep[2 * j] * rp, ep[2 * j + 1] * rp); neg[i][j] = cvtpk(en[2 * j] * rn, en[2 * j + 1] * rn); }
  }
}
DI u32x4 pack8(const f32x4 a, const f32x4 b) { return (u32x4){cvtpk(a[0], a[1]), cvtpk(a[2], a[3]), cvtpk(b[0], b[1]), cvtpk(b[2], b[3])}; }

template<bool NEXT> DI void phase1(const Ctx& c, const u16* wqkv, const u16* wphi, const u16* wo, const u16* w_out) {
  const unsigned lds0 = c.lds0; const int tid = c.tid, lane = c.lane, wid = c.wid, pm = c.pm, pn = c.pn; unsigned char* ws = c.ws;
  const int g = lane >> 4, i16 = lane & 15, q4 = i16 >> 2, p4 = lane & 3;
  const int w4 = wid & 3, wr = w4 >> 1, wc = w4 & 1, mp = wid >> 2;
  ST0(1, 0);
  if (wid == 3) st16(c, WS_ZST + (pm * 16 + pn) * 1024 + lane * 16, (u32x4){ZST_EMPTY, ZST_EMPTY, ZST_EMPTY, ZST_EMPTY});
  if (wid < 4) {
    const u32x4 wp = *(const u32x4*)(wphi + (size_t)pn * 2048 + tid * 8);
    f32x4 acc[C1::NT][C1::MT];
    consumer_loop<C1, 1>(acc, lds0, wid, lane, ws, [&] { ldsst<u32x4>(lds0 + K1_WPHI + tid * 16, wp); });
#pragma unroll
    for (int m = 0; m < 4; ++m) {
      ldsst<u32x4>(lds0 + K1_KX + (w4 * 4 + m) * 1024 + lane * 16, pack8(acc[2][m], acc[3][m]));
      const u32x4 vv = pack8(acc[4][m], acc[5][m]);
      const unsigned va = lds0 + K1_V + wc * V_HEAD + (wr * 64 + m * 16 + i16) * V_RS + g * 8;
      ldsst<u32x2>(va, (u32x2){vv[0], vv[1]}); ldsst<u32x2>(va + 32, (u32x2){vv[2], vv[3]});
      ldsst<u32x4>(lds0 + K1_QX + (w4 * 4 + m) * 1024 + lane * 16, pack8(acc[0][m], acc[1][m]));
    }
    waitl0();
  } else {
    NoSide side;
    producer_loop<C1, false, 0>(lds0, w4, lane, c.ws_srd(), make_srd(wqkv, DM * 3 * DM * 2), (unsigned)WS_XN + pm * (16 * 16384), (unsigned)pn * 128, DM * 2, NoDep{}, side);
    const unsigned b = blockIdx.x;
    prewarm<64>(lds0 + LDS_DUMMY, w4, lane, make_srd(wo, DM * DM * 2), [&](unsigned i) { return (b * 64 + i) * 128; });
    prewarm<112>(lds0 + LDS_DUMMY, 3 - w4, lane, make_srd(w_out, FH * DM * 2), [&](unsigned i) { return (b * 112 + i) * 128; });
  }
  bar();
  const unsigned wph = lds0 + K1_WPHI + wc * 2048 + (4 * g + q4) * 64 + p4 * 8;
  const bf16x8 wf0 = join(trread(wph), trread(wph + 1024)), wf1 = join(trread(wph + 32), trread(wph + 1024 + 32));
  { bf16x8 kf[2]; u32x4 pos[2], neg[2];
#pragma unroll
    for (int i = 0; i < 2; ++i) kf[i] = ldsld<bf16x8>(lds0 + K1_KX + (w4 * 4 + 2 * mp + i) * 1024 + lane * 16);
    feature_maps(wf0, wf1, kf, pos, neg);
#pragma unroll
    for (int i = 0; i < 2; ++i) {
      const unsigned pa = lds0 + K1_PHI + wc * PHI_HEAD + (wr * 64 + (2 * mp + i) * 16 + i16) * PHI_RS + g * 8;
      ldsst<u32x2>(pa, (u32x2){pos[i][0], pos[i][1]}); ldsst<u32x2>(pa + 32, (u32x2){pos[i][2], pos[i][3]});
      ldsst<u32x2>(pa + 64, (u32x2){neg[i][0], neg[i][1]}); ldsst<u32x2>(pa + 96, (u32x2){neg[i][2], neg[i][3]});
    } }
  lbar();
  ST0(1, 4);
  { f32x4 d[2] = {{0.f, 0.f, 0.f, 0.f}, {0.f, 0.f, 0.f, 0.f}}, dz = {0.f, 0.f, 0.f, 0.f};
    const bf16x8 ones = asbf((u32x4){0x3f803f80u, 0x3f803f80u, 0x3f803f80u, 0x3f803f80u});
    const unsigned pr = lds0 + K1_PHI + wc * PHI_HEAD + (4 * g + q4) * PHI_RS + wr * 64 + p4 * 8;
    const unsigned vr = lds0 + K1_V + wc * V_HEAD + (4 * g + q4) * V_RS + mp * 32 + p4 * 8;
    bf16x8 pf[4][2], vf[4];
#pragma unroll
    for (int s = 0; s < 4; ++s) {
#pragma unroll
      for (int a = 0; a < 2; ++a) pf[s][a] = join(trread(pr + s * 32 * PHI_RS + a * 32), trread(pr + (s * 32 + 16) * PHI_RS + a * 32));
      vf[s] = join(trread(vr + s * 32 * V_RS), trread(vr + (s * 32 + 16) * V_RS)); }
#pragma unroll
    for (int s = 0; s < 4; ++s) { d[0] = mfma(pf[s][0], vf[s], d[0]); d[1] = mfma(pf[s][1], vf[s], d[1]); dz = mfma(mp ? pf[s][1] : pf[s][0], ones, dz); }
    st16(c, WS_KVP + ((pn * 16 + pm) * 2 + wc) * 4096 + (wr * 2 + mp) * 1024 + lane * 16, pack8(d[0], d[1]));
    if (i16 == 0) st16(c, WS_ZP + ((pn * 16 + pm) * 2 + wc) * 256 + wr * 128 + g * 32 + mp * 16, __builtin_bit_cast(u32x4, dz)); }
  waitv<0>(); bar();
  if (wid == 4 && lane == 0) __hip_atomic_fetch_add(c.cnt(CNT_KV + pn), 1u, RLX_AGENT);
  u32x4 phiq[2][2];
  { bf16x8 qf[2]; u32x4 pos[2], neg[2];
#pragma unroll
    for (int i = 0; i < 2; ++i) qf[i] = ldsld<bf16x8>(lds0 + K1_QX + (w4 * 4 + 2 * mp + i) * 1024 + lane * 16);
    feature_maps(wf0, wf1, qf, pos, neg);
#pragma unroll
    for (int i = 0; i < 2; ++i) { phiq[i][0] = pos[i]; phiq[i][1] = neg[i]; } }
  if (wid == 4) wait_count(c.cnt(CNT_KV + pn), 16u, c.cnt(CNT_TMO));
  bar();
  ST0(1, 5);
  { const int head = wid & 1, fr = wid >> 1;
    u32x4 pv[16];
#pragma unroll
    for (int p = 0; p < 16; ++p) pv[p] = ld16(c, WS_KVP + ((pn * 16 + p) * 2 + head) * 4096 + fr * 1024 + lane * 16);
    float zs = 0.f;
    if (wid < 2) {
      float zv[16];
#pragma unroll
      for (int p = 0; p < 16; ++p) zv[p] = __uint_as_float(__builtin_amdgcn_raw_buffer_load_b32(c.rs, WS_ZP + ((pn * 16 + p) * 2 + head) * 256 + lane * 4, 0, AUX_SC1));
#pragma unroll
      for (int p = 0; p < 16; ++p) zs += zv[p];
    }
    f32x4 s0 = {0.f, 0.f, 0.f, 0.f}, s1 = s0;
#pragma unroll
    for (int p = 0; p < 16; ++p) { s0 += (f32x4){bflo(pv[p][0]), bfhi(pv[p][0]), bflo(pv[p][1]), bfhi(pv[p][1])}; s1 += (f32x4){bflo(pv[p][2]), bfhi(pv[p][2]), bflo(pv[p][3]), bfhi(pv[p][3])}; }
    ldsst<u32x4>(lds0 + K1_KVT + (head * 4 + fr) * 1024 + lane * 16, pack8(s0, s1));
    if (wid < 2) ldsst<u16>(lds0 + K1_ZT + head * 128 + lane * 2, (u16)cvtpk(zs, 0.f));
  }
  if (NEXT && wid >= 4) prefetch_weights<C2, false, PRE2>(lds0, w4, lane, make_srd(wo, DM * DM * 2), (unsigned)pn * 128, pn);
  lbar();
  ST0(1, 6);
  { bf16x8 kv[2][2], zf[2];
#pragma unroll
    for (int ks = 0; ks < 2; ++ks) { zf[ks] = ldsld<bf16x8>(lds0 + K1_ZT + wc * 128 + ks * 64 + g * 16);
#pragma unroll
      for (int dt = 0; dt < 2; ++dt) kv[ks][dt] = ldsld<bf16x8>(lds0 + K1_KVT + (wc * 4 + ks * 2 + dt) * 1024 + lane * 16); }
#pragma unroll
    for (int i = 0; i < 2; ++i) {
      const f32x4 z4 = {0.f, 0.f, 0.f, 0.f};
      const bf16x8 b0 = asbf(phiq[i][0]), b1 = asbf(phiq[i][1]);
      const f32x4 den = mfma(zf[1], b1, mfma(zf[0], b0, z4));
      const f32x4 n0 = mfma(kv[1][0], b1, mfma(kv[0][0], b0, z4)), n1 = mfma(kv[1][1], b1, mfma(kv[0][1], b0, z4));
      const float r = __builtin_amdgcn_rcpf(den[0] + 1e-6f);
      const int tok = wr * 64 + (2 * mp + i) * 16 + i16;
      const unsigned oa = lds0 + K1_OST + (g & 1) * 8;
      ldsst<u32x2>(oa + aimg(tok, wc * 4 + (g >> 1)), (u32x2){cvtpk(n0[0] * r, n0[1] * r), cvtpk(n0[2] * r, n0[3] * r)});
      ldsst<u32x2>(oa + aimg(tok, wc * 4 + 2 + (g >> 1)), (u32x2){cvtpk(n1[0] * r, n1[1] * r), cvtpk(n1[2] * r, n1[3] * r)});
    } }
  lbar();
#pragma unroll
  for (int i = 0; i < 2; ++i) { const int p = tid + i * 512; st16(c, WS_O + (pm * 16 + pn) * 16384 + p * 16, ldsld<u32x4>(lds0 + K1_OST + p * 16)); }
  ST0(1, 7);
}

template<class C, bool FIRST, int KID, int NPRE, bool NEXT>
DI void phase_res(const Ctx& c, bool dep_on, unsigned a_off, const u16* W, unsigned w_bytes, const u16* x, u16* out, const u16* wnext) {
  const unsigned lds0 = c.lds0; const int tid = c.tid, lane = c.lane, wid = c.wid, pm = c.pm, pn = c.pn; unsigned char* ws = c.ws;
  ST0(KID, 0);
  if (wid >= 4) {
    NoSide side;
    if constexpr (FIRST)
      producer_loop<C, false, NPRE>(lds0, wid - 4, lane, c.ws_srd(), make_srd(W, w_bytes), a_off + (unsigned)pm * (C::NKT * 16384), (unsigned)pn * 128, 0,
                                    make_dep(c, dep_on, FLG_O + pm, pn, pn, [](int kt) { return 1u << kt; }), side);
    else
      producer_loop<C, false, NPRE>(lds0, wid - 4, lane, c.ws_srd(), make_srd(W, w_bytes), a_off + (unsigned)pm * (C::NKT * 16384), (unsigned)pn * 128, 0,
                                    make_dep(c, dep_on, FLG_G + pm, pn, kt0_5(pn), [](int kt) { return (1u << ((4u * kt) / 7u)) | (1u << ((8u * kt + 7u) / 14u)); }), side);
    if constexpr (NEXT) prefetch_weights<C3, false, PRE3>(lds0, wid - 4, lane, make_srd(wnext, DM * FF * 2), w3_off(wid - 4, pn), pn);
    bar();
  } else {
    u32x4 rv[4];
    f32x4 acc[C::NT][C::MT];
    consumer_loop<C, KID>(acc, lds0, wid, lane, ws, [&] {
#pragma unroll
      for (int i = 0; i < 4; ++i) { const int p = tid + i * 256, row = p >> 3, cc = (p & 7) ^ ((row >> 1) & 7);
        rv[i] = FIRST ? *(const u32x4*)((const unsigned char*)x + (size_t)(pm * 128 + row) * (DM * 2) + pn * 128 + cc * 16) : ld16(c, WS_H + (pm * 16 + pn) * 16384 + p * 16); } });
    const int g = lane >> 4, i16 = lane & 15, wr = wid >> 1, wc = wid & 1;
#pragma unroll
    for (int n = 0; n < 2; ++n)
#pragma unroll
      for (int m = 0; m < 4; ++m)
        ldsst<u32x2>(lds0 + aimg(wr * 64 + m * 16 + i16, wc * 4 + n * 2 + (g >> 1)) + (g & 1) * 8, (u32x2){cvtpk(acc[n][m][0], acc[n][m][1]), cvtpk(acc[n][m][2], acc[n][m][3])});
    lbar();
#pragma unroll
    for (int i = 0; i < 4; ++i) { const int p = tid + i * 256, row = p >> 3, cc = (p & 7) ^ ((row >> 1) & 7);
      const u32x4 a = ldsld<u32x4>(lds0 + p * 16); u32x4 o; float s = 0.f, qq = 0.f;
#pragma unroll
      for (int j = 0; j < 4; ++j) { o[j] = cvtpk(bflo(rv[i][j]) + bflo(a[j]), bfhi(rv[i][j]) + bfhi(a[j]));
        if (FIRST) { const float lo = bflo(o[j]), hi = bfhi(o[j]); s += lo + hi; qq += lo * lo + hi * hi; } }
      if (FIRST) {
        st16(c, WS_H + (pm * 16 + pn) * 16384 + p * 16, o);
#pragma unroll
        for (int of = 1; of < 8; of <<= 1) { s += __shfl_xor(s, of); qq += __shfl_xor(qq, of); }
        if ((p & 7) == 0) st8(c, WS_HST + ((pm * 16 + pn) * 128 + row) * 8, (f32x2){s, qq});
      } else *(u32x4*)((unsigned char*)out + (size_t)(pm * 128 + row) * (DM * 2) + pn * 128 + cc * 16) = o;
    }
  }
  ST0(KID, 7);
}

constexpr int K3_RST = LDS_TAB;
constexpr int K3_CS = K3_RST + 1024;
DI unsigned k3_xf(int rt) { return (rt >> 2) * C4::STAGE + C4::A_BYTES + (rt & 3) * 3584; }
constexpr int STG_RS = 240;
static_assert(C3::RING <= LDS_TAB && 4 * 3584 <= C4::B_BYTES, "P3 map");
DI f32x2 gelu2(f32x2 x) {
  constexpr float C0 = -2.0f * 0.7978845608028654f * 1.4426950408889634f, C1_ = C0 * 0.044715f;
  const f32x2 a = x * (x * x * C1_ + C0);
  const f32x2 d = (f32x2){__builtin_amdgcn_exp2f(a[0]), __builtin_amdgcn_exp2f(a[1])} + 1.0f;
  return x * (f32x2){__builtin_amdgcn_rcpf(d[0]), __builtin_amdgcn_rcpf(d[1])};
}
DI unsigned gelu_pk(unsigned u) { const f32x2 y = gelu2((f32x2){bflo(u), bfhi(u)}); return cvtpk(y[0], y[1]); }
DI float dot2(unsigned a, unsigned b, float c) { return __builtin_amdgcn_fdot2_f32_bf16(__builtin_bit_cast(bf16x2, a), __builtin_bit_cast(bf16x2, b), c, false); }
struct Side3 {
  static constexpr int EXTRA = 12;
  const Ctx& c; f32x2 st[8]; float cs4[4];
  DI Side3(const Ctx& c_) : c(c_) {}
  DI void tile(int, unsigned) {}
  DI void late() {
    const int t = c.tid - 256;
#pragma unroll
    for (int p = 0; p < 8; ++p) st[p] = ld8(c, WS_HST + (((c.pm * 16 + (t & 1) * 8) + p) * 128 + (t >> 1)) * 8);
    const float* cs = (const float*)(c.ws + WS_CSUM) + (t < 112 ? c.pn * 112 + t : t < 224 ? FH + c.pn * 112 + t - 112 : 0);
#pragma unroll
    for (int p = 0; p < 4; ++p) cs4[p] = cs[p * FF];
  }
  DI void table() {
    const int t = c.tid - 256;
#pragma unroll
    for (int p = 0; p < 8; ++p) asm volatile("" : "+v"(st[p]));
#pragma unroll
    for (int p = 0; p < 4; ++p) asm volatile("" : "+v"(cs4[p]));
    f32x2 s = (st[0] + st[1]) + (st[2] + st[3]) + ((st[4] + st[5]) + (st[6] + st[7]));
    s[0] += __shfl_xor(s[0], 1); s[1] += __shfl_xor(s[1], 1);
    const float mean = s[0] * (1.0f / DM), var = fmaxf(s[1] * (1.0f / DM) - mean * mean, 0.f);
    if ((t & 1) == 0) ldsst<f32x2>(c.lds0 + K3_RST + (t >> 1) * 8, (f32x2){mean, __builtin_amdgcn_rsqf(var + 1e-5f)});
    if (t < 224) ldsst<float>(c.lds0 + K3_CS + (t < 112 ? t : t + 16) * 4, (cs4[0] + cs4[1]) + (cs4[2] + cs4[3]));
  }
};
template<int NPRE, bool NEXT> DI void phase3(const Ctx& c, bool dep_on, const u16* w_in, const u16* w_sp) {
  const unsigned lds0 = c.lds0; const int lane = c.lane, wid = c.wid, pm = c.pm, pn = c.pn; unsigned char* ws = c.ws;
  const int g = lane >> 4, i16 = lane & 15;
  const int w4 = wid & 3, wr = w4 >> 1, wc = w4 & 1;
  ST0(3, 0);
  if (wid < 4) {
    f32x4 acc[C3::NT][C3::MT];
    consumer_loop<C3, 3>(acc, lds0, wid, lane, ws, [] {});
    f32x2 rs[4]; f32x4 cs[7];
#pragma unroll
    for (int m = 0; m < 4; ++m) rs[m] = ldsld<f32x2>(lds0 + K3_RST + (wr * 64 + m * 16 + i16) * 8);
#pragma unroll
    for (int n = 0; n < 7; ++n) cs[n] = ldsld<f32x4>(lds0 + K3_CS + wc * 512 + (n * 16 + g * 4) * 4);
    u32x2 w[7][4];
#pragma unroll
    for (int m = 0; m < 4; ++m) { const float mr = -rs[m][0] * rs[m][1], r = rs[m][1];
#pragma unroll
      for (int n = 0; n < 7; ++n) { const f32x4 v = acc[n][m] * r + cs[n] * mr; w[n][m] = (u32x2){cvtpk(v[0], v[1]), cvtpk(v[2], v[3])}; } }
    if (wc == 0) {
#pragma unroll
      for (int n = 0; n < 7; ++n)
#pragma unroll
        for (int mp = 0; mp < 2; ++mp)
          ldsst<u32x4>(lds0 + LDS_Z1 + ((wr * 2 + mp) * 7 + n) * 1024 + lane * 16, (u32x4){w[n][2 * mp][0], w[n][2 * mp][1], w[n][2 * mp + 1][0], w[n][2 * mp + 1][1]});
    } else {
#pragma unroll
      for (int n = 0; n < 7; ++n)
#pragma unroll
        for (int m = 0; m < 4; ++m) ldsst<u32x2>(lds0 + k3_xf(wr * 4 + m) + n * 512 + lane * 8, w[n][m]);
    }
    lbar();
  } else {
    Side3 side(c);
    producer_loop<C3, false, NPRE>(lds0, w4, lane, c.ws_srd(), make_srd(w_in, DM * FF * 2), (unsigned)WS_H + pm * (16 * 16384),
                                   w3_off(w4, pn), 0, make_dep(c, dep_on, FLG_H + pm, pn, pn, [](int kt) { return 1u << kt; }), side);
    prewarm<256>(lds0 + LDS_DUMMY, w4, lane, make_srd(w_sp, SEQ * SEQ * 2), [&](unsigned i) { return (blockIdx.x * 256 + i) * 128; });
    if constexpr (NEXT) prefetch_weights<C4, true, PRE4>(lds0, w4, lane, make_srd(w_sp, SEQ * SEQ * 2), (unsigned)pm * 128 * C4::LDA, 2 * pm);
    bar();
  }
  u32x2 z[7];
#pragma unroll
  for (int n = 0; n < 7; ++n) z[n] = ldsld<u32x2>(lds0 + k3_xf(wid) + n * 512 + lane * 8);
  float s = 0.f, qq = 0.f;
#pragma unroll
  for (int n = 0; n < 7; ++n) { z[n] = (u32x2){gelu_pk(z[n][0]), gelu_pk(z[n][1])};
    s = dot2(z[n][1], 0x3f803f80u, dot2(z[n][0], 0x3f803f80u, s)); qq = dot2(z[n][1], z[n][1], dot2(z[n][0], z[n][0], qq)); }
  s = sum4(s); qq = sum4(qq);
  const int row = wid * 16 + i16;
  if (g == 0) { const unsigned qb = __float_as_uint(qq);
    st8(c, WS_ZST + ((pm * 16 + pn) * 128 + row) * 8, (f32x2){s, __uint_as_float(qb == ZST_EMPTY ? 0x7fc00000u : qb)}); }
  ST0(3, 4);
  {
    u32x2 v[7];
#pragma unroll
    for (int i = 0; i < 7; ++i) { const int h = wid * 7 + i; v[i] = ldsld<u32x2>(lds0 + LDS_Z1 + (h >> 1) * 1024 + lane * 16 + (h & 1) * 8); }
#pragma unroll
    for (int i = 0; i < 7; ++i) { const int h = wid * 7 + i; ldsst<u32x2>(lds0 + LDS_Z1 + (h >> 1) * 1024 + lane * 16 + (h & 1) * 8, (u32x2){gelu_pk(v[i][0]), gelu_pk(v[i][1])}); } }
  f32x2 t;
  for (unsigned spins = 0;; ++spins) {
    f32x2 w[4];
#pragma unroll
    for (int p = 0; p < 4; ++p) w[p] = ld8(c, WS_ZST + ((pm * 16 + 4 * g + p) * 128 + row) * 8);
    bool ok = true;
#pragma unroll
    for (int p = 0; p < 4; ++p) ok &= __float_as_uint(w[p][1]) != ZST_EMPTY;
    t = (w[0] + w[1]) + (w[2] + w[3]);
    if (__all(ok)) break;
    if (spins > (1u << 16)) { __hip_atomic_store(c.cnt(CNT_TMO), 1u, RLX_AGENT); break; }
    if ((spins & 255u) == 255u && __builtin_amdgcn_readfirstlane(__hip_atomic_load(c.cnt(CNT_TMO), RLX_AGENT))) break;
    __builtin_amdgcn_s_sleep(1);
  }
  ST0(3, 5);
  const float mean = sum4(t[0]) * (1.0f / FH), var = fmaxf(sum4(t[1]) * (1.0f / FH) - mean * mean, 0.f), rstd = __builtin_amdgcn_rsqf(var + 1e-5f);
  const unsigned stg = lds0 + k3_xf(wid);
#pragma unroll
  for (int n = 0; n < 7; ++n)
    ldsst<u32x2>(stg + kswap(i16) * 224 + n * 32 + g * 8,
                 (u32x2){cvtpk((bflo(z[n][0]) - mean) * rstd, (bfhi(z[n][0]) - mean) * rstd), cvtpk((bflo(z[n][1]) - mean) * rstd, (bfhi(z[n][1]) - mean) * rstd)});
  waitl0();
  const unsigned z2 = WS_Z2 + (pn * SEQ + pm * 128 + wid * 16) * 224;
#pragma unroll
  for (int i = 0; i < 4; ++i) { const unsigned ci = i * 64 + lane;
    if (i < 3 || lane < 32) st16(c, z2 + ci * 16, ldsld<u32x4>(stg + ci * 16)); }
  ST0(3, 7);
}

static_assert(C4::RING <= LDS_Z1, "P4 map");
template<int NPRE, bool NEXT> DI void phase4(const Ctx& c, bool dep_on, const u16* w_sp, const u16* b_sp, const u16* w_out) {
  const unsigned lds0 = c.lds0; const int lane = c.lane, wid = c.wid, pm = c.pm, pn = c.pn; unsigned char* ws = c.ws;
  ST0(4, 0);
  if (wid >= 4) {
    NoSide side;
    producer_loop<C4, true, NPRE>(lds0, wid - 4, lane, make_srd(w_sp, SEQ * SEQ * 2), c.ws_srd(), (unsigned)pm * 128 * C4::LDA, (unsigned)WS_Z2 + pn * (SEQ * 224), 0,
                                  make_dep(c, dep_on, FLG_Z2 + pn, pm, 2 * pm, [](int kt) { return 1u << (kt >> 1); }), side);
    if constexpr (NEXT) prefetch_weights<C5, false, PRE5>(lds0, wid - 4, lane, make_srd(w_out, FH * DM * 2), (unsigned)pn * 128, kt0_5(pn));
  } else {
    const int g = lane >> 4, i16 = lane & 15;
    unsigned bs[2];
    f32x4 acc[C4::NT][C4::MT];
    consumer_loop<C4, 4>(acc, lds0, wid, lane, ws, [&] {
#pragma unroll
      for (int m = 0; m < 2; ++m) bs[m] = b_sp[pm * 128 + wid * 32 + m * 16 + i16]; });
    const unsigned stg = lds0 + (wid >> 1) * C5::STAGE + (wid & 1) * 32 * STG_RS;
#pragma unroll
    for (int n = 0; n < 7; ++n) { const u32x4 z1 = ldsld<u32x4>(lds0 + LDS_Z1 + (wid * 7 + n) * 1024 + lane * 16);
#pragma unroll
      for (int m = 0; m < 2; ++m) { const unsigned a = z1[2 * m], b = z1[2 * m + 1]; const f32x4 v = acc[n][m] + bflo(bs[m]);
        ldsst<u32x2>(stg + (m * 16 + i16) * STG_RS + n * 32 + g * 8, (u32x2){cvtpk(bflo(a) * v[0], bfhi(a) * v[1]), cvtpk(bflo(b) * v[2], bfhi(b) * v[3])}); } }
    waitl0();
    const unsigned gp = WS_G + pm * (28 * 16384);
#pragma unroll
    for (int i = 0; i < 7; ++i) { const unsigned ci = i * 64 + lane, r = ci / 14u, ch = ci - r * 14u, cc = pn * 14 + ch, row = wid * 32 + r;
      st16(c, gp + (cc >> 3) * 16384 + aimg(row, cc & 7), ldsld<u32x4>(stg + r * STG_RS + ch * 16)); }
  }
  ST0(4, 7);
}

struct Args { const u16 *x, *wqkv, *wphi, *wo, *w_in, *w_sp, *b_sp, *w_out; unsigned char* ws; u16* out; };
template<int P0, int P1> __global__ __launch_bounds__(512, 2) void k_main(Args a) {
  extern __shared__ __attribute__((aligned(16))) unsigned char shm[];
  Ctx c;
  c.lds0 = (unsigned)(size_t)(LAS unsigned char*)shm;
  c.tid = threadIdx.x; c.lane = c.tid & 63; c.wid = __builtin_amdgcn_readfirstlane(c.tid >> 6);
  { const int b = blockIdx.x, xl = b & 7, j = b >> 3;
    c.pm = (xl & 3) * 4 + (j & 3); c.pn = (xl >> 2) * 8 + (j >> 2); }
  c.ws = a.ws; c.rs = make_rsrc(a.ws, (unsigned)WS_END);
  if constexpr (P0 <= 1 && 1 <= P1) phase1<2 <= P1>(c, a.wqkv, a.wphi, a.wo, a.w_out);
  if constexpr (P0 <= 1 && 2 <= P1) publish(c, FLG_O + c.pm, c.pn);
  if constexpr (P0 <= 2 && 2 <= P1) phase_res<C2, true, 2, P0 < 2 ? PRE2 : 0, 3 <= P1>(c, P0 < 2, (unsigned)WS_O, a.wo, DM * DM * 2, a.x, nullptr, a.w_in);
  if constexpr (P0 <= 2 && 3 <= P1) publish(c, FLG_H + c.pm, c.pn);
  if constexpr (P0 <= 3 && 3 <= P1) phase3<P0 < 3 ? PRE3 : 0, 4 <= P1>(c, P0 < 3, a.w_in, a.w_sp);
  if constexpr (P0 <= 3 && 4 <= P1) publish(c, FLG_Z2 + c.pn, c.pm);
  if constexpr (P0 <= 4 && 4 <= P1) phase4<P0 < 4 ? PRE4 : 0, 5 <= P1>(c, P0 < 4, a.w_sp, a.b_sp, a.w_out);
  if constexpr (P0 <= 4 && 5 <= P1) publish(c, FLG_G + c.pm, c.pn);
  if constexpr (P0 <= 5 && 5 <= P1) phase_res<C5, false, 5, P0 < 5 ? PRE5 : 0, false>(c, P0 < 5, (unsigned)WS_G, a.w_out, FH * DM * 2, nullptr, a.out, nullptr);
}
template<int P0, int P1> void launch_main(const Args& a, hipStream_t stream) {
  (void)hipFuncSetAttribute((const void*)k_main<P0, P1>, hipFuncAttributeMaxDynamicSharedMemorySize, LDS_TOTAL);
  k_main<P0, P1><<<256, 512, LDS_TOTAL, stream>>>(a);
}
}

extern "C" void kernel_launch(void* const* d_in, const int* in_sizes, int n_in, void* d_out, int out_size, void* d_ws, size_t ws_size, hipStream_t stream) {
  Args a;
  a.x = (const u16*)d_in[0]; a.wqkv = (const u16*)d_in[1]; a.wphi = (const u16*)d_in[2]; a.wo = (const u16*)d_in[3];
  a.w_in = (const u16*)d_in[5]; a.w_sp = (const u16*)d_in[6]; a.b_sp = (const u16*)d_in[7]; a.w_out = (const u16*)d_in[8];
  a.ws = (unsigned char*)d_ws; a.out = (u16*)d_out;
  (void)in_sizes; (void)n_in; (void)out_size; (void)ws_size;
  k0_prep<<<256, 256, 0, stream>>>(a.x, a.wqkv, a.w_in, a.ws);
  launch_main<1, 5>(a, stream);
}
```

```cpp
#include <hip/hip_runtime.h>
#include <hip/hip_bf16.h>
#include <utility>

namespace {
typedef short s16x4 __attribute__((ext_vector_type(4)));
typedef short s16x8 __attribute__((ext_vector_type(8)));
typedef __bf16 bf16x8 __attribute__((ext_vector_type(8)));
typedef __bf16 bf16x2 __attribute__((ext_vector_type(2)));
typedef float f32x2 __attribute__((ext_vector_type(2)));
typedef float f32x4 __attribute__((ext_vector_type(4)));
typedef int i32x4 __attribute__((ext_vector_type(4)));
typedef unsigned u32x4 __attribute__((ext_vector_type(4)));
typedef unsigned u32x2 __attribute__((ext_vector_type(2)));
typedef unsigned short u16;
#define LAS __attribute__((address_space(3)))
#define GAS __attribute__((address_space(1)))
#define DI __device__ __forceinline__
#define FENCE() __builtin_amdgcn_sched_barrier(0)
#define RLX_AGENT __ATOMIC_RELAXED, __HIP_MEMORY_SCOPE_AGENT
template<int N> using iseq = std::make_integer_sequence<int, N>;

constexpr int SEQ = 2048, DM = 1024, FH = 1792, FF = 3584;
constexpr size_t WS_CTL = 0;
enum { CNT_KV = 0, FLG_O = 16, FLG_H = 32, FLG_Z2 = 48, FLG_G = 64, FLG_W = 80, CNT_TMO = 96, CNT_N = 128 };
constexpr size_t CTL_BYTES = CNT_N * 256;
constexpr size_t WS_XN = CTL_BYTES;
constexpr size_t WS_H = WS_XN;
constexpr size_t WS_G = WS_XN + (size_t)SEQ * DM * 2;
constexpr size_t WS_KVP = WS_G;
constexpr size_t WS_ZP = WS_KVP + (size_t)16 * 16 * 8192;
constexpr size_t WS_O = WS_ZP + 16 * 16 * 512;
constexpr size_t WS_HST = WS_G + (size_t)SEQ * FH * 2;
constexpr size_t WS_Z2 = WS_HST + 16 * 16 * 128 * 8;
constexpr size_t WS_ZST = WS_Z2 + (size_t)SEQ * FH * 2;
constexpr size_t WS_WIN = WS_ZST + 16 * 16 * 128 * 8;
constexpr size_t WS_CSP = WS_WIN + (size_t)DM * FF * 2;
constexpr size_t WS_END = WS_CSP + 16 * 16 * 224 * 4;
static_assert(WS_O + (size_t)SEQ * DM * 2 <= WS_HST, "aliases");

DI i32x4 make_srd(const void* p, unsigned bytes) {
  const unsigned long long a = (unsigned long long)p; i32x4 rs;
  rs[0] = __builtin_amdgcn_readfirstlane((int)(unsigned)a); rs[1] = __builtin_amdgcn_readfirstlane((int)(unsigned)(a >> 32)) & 0xffff;
  rs[2] = (int)bytes; rs[3] = 0x00020000; return rs;
}
DI __amdgpu_buffer_rsrc_t make_rsrc(const void* p, unsigned bytes) { return __builtin_amdgcn_make_buffer_rsrc(const_cast<void*>(p), 0, bytes, 0x00020000); }
DI int uni(int v) { return __builtin_amdgcn_readfirstlane(v); }
template<bool SC1> DI void dma16(unsigned ldsaddr, unsigned voff, i32x4 rs, unsigned soff) {
  if constexpr (SC1) asm volatile("s_mov_b32 m0, %0\n\ts_nop 0\n\tbuffer_load_dwordx4 %1, %2, %3 offen sc1 lds" :: "s"(ldsaddr), "v"(voff), "s"(rs), "s"(soff));
  else asm volatile("s_mov_b32 m0, %0\n\ts_nop 0\n\tbuffer_load_dwordx4 %1, %2, %3 offen lds" :: "s"(ldsaddr), "v"(voff), "s"(rs), "s"(soff));
}
DI void touch64(unsigned ldsdummy, unsigned voff, i32x4 rs) {
  asm volatile("s_mov_b32 m0, %0\n\ts_nop 0\n\tbuffer_load_dword %1, %2, 0 offen lds" :: "s"(ldsdummy), "v"(voff), "s"(rs));
}
template<int N> DI void waitv() { asm volatile("s_waitcnt vmcnt(%0)" :: "i"(N) : "memory"); }
DI void waitl0() { asm volatile("" ::: "memory"); __builtin_amdgcn_s_waitcnt(0xC07F); asm volatile("" ::: "memory"); }
DI void bar() { asm volatile("s_barrier" ::: "memory"); }
DI void lbar() { waitl0(); bar(); }
DI unsigned cvtpk(float lo, float hi) { f32x2 v = {lo, hi}; bf16x2 b = __builtin_convertvector(v, bf16x2); return __builtin_bit_cast(unsigned, b); }
DI float bflo(unsigned u) { return __uint_as_float(u << 16); }
DI float bfhi(unsigned u) { return __uint_as_float(u & 0xffff0000u); }
DI bf16x8 asbf(u32x4 v) { return __builtin_bit_cast(bf16x8, v); }
DI f32x4 mfma(bf16x8 a, bf16x8 b, f32x4 c) { return __builtin_amdgcn_mfma_f32_16x16x32_bf16(a, b, c, 0, 0, 0); }
DI s16x4 trread(unsigned addr) { return __builtin_amdgcn_ds_read_tr16_b64_v4i16((LAS s16x4*)addr); }
DI bf16x8 join(s16x4 lo, s16x4 hi) { return __builtin_bit_cast(bf16x8, __builtin_shufflevector(lo, hi, 0, 1, 2, 3, 4, 5, 6, 7)); }
template<class T> DI T ldsld(unsigned addr) { return *(const LAS T*)addr; }
template<class T> DI void ldsst(unsigned addr, T v) { *(LAS T*)addr = v; }
DI u32x4 pack8(const f32x4 a, const f32x4 b) { return (u32x4){cvtpk(a[0], a[1]), cvtpk(a[2], a[3]), cvtpk(b[0], b[1]), cvtpk(b[2], b[3])}; }
DI float dot2(unsigned a, unsigned b, float c) { return __builtin_amdgcn_fdot2_f32_bf16(__builtin_bit_cast(bf16x2, a), __builtin_bit_cast(bf16x2, b), c, false); }
constexpr unsigned ONES2 = 0x3f803f80u;
#define RED4X4(OP) \
  "s_nop 1\n\tv_mov_b32 %4, %0\n\tv_mov_b32 %5, %1\n\tv_mov_b32 %6, %2\n\tv_mov_b32 %7, %3\n\t" \
  "v_permlane16_swap_b32 %0, %4\n\tv_permlane16_swap_b32 %1, %5\n\tv_permlane16_swap_b32 %2, %6\n\tv_permlane16_swap_b32 %3, %7\n\t" \
  OP " %0, %0, %4\n\t" OP " %1, %1, %5\n\t" OP " %2, %2, %6\n\t" OP " %3, %3, %7\n\t" \
  "v_mov_b32 %4, %0\n\tv_mov_b32 %5, %1\n\tv_mov_b32 %6, %2\n\tv_mov_b32 %7, %3\n\ts_nop 0\n\t" \
  "v_permlane32_swap_b32 %0, %4\n\tv_permlane32_swap_b32 %1, %5\n\tv_permlane32_swap_b32 %2, %6\n\tv_permlane32_swap_b32 %3, %7\n\t" \
  OP " %0, %0, %4\n\t" OP " %1, %1, %5\n\t" OP " %2, %2, %6\n\t" OP " %3, %3, %7"
DI void max4x4(float& a, float& b, float& c, float& d) { float t0, t1, t2, t3;
  asm(RED4X4("v_max_f32") : "+v"(a), "+v"(b), "+v"(c), "+v"(d), "=&v"(t0), "=&v"(t1), "=&v"(t2), "=&v"(t3)); }
DI void sum4x4(float& a, float& b, float& c, float& d) { float t0, t1, t2, t3;
  asm(RED4X4("v_add_f32") : "+v"(a), "+v"(b), "+v"(c), "+v"(d), "=&v"(t0), "=&v"(t1), "=&v"(t2), "=&v"(t3)); }
DI float sum4(float x) {
  const unsigned u = __float_as_uint(x);
  auto a = __builtin_amdgcn_permlane16_swap(u, u, false, false);
  const unsigned v = __float_as_uint(__uint_as_float(a[0]) + __uint_as_float(a[1]));
  auto b = __builtin_amdgcn_permlane32_swap(v, v, false, false);
  return __uint_as_float(b[0]) + __uint_as_float(b[1]);
}
template<int CTRL, int ROWMASK> DI float dpp_add(float x) {
  return x + __int_as_float(__builtin_amdgcn_update_dpp(0, __float_as_int(x), CTRL, ROWMASK, 0xf, true));
}
DI float wave_sum(float x) {
  x = dpp_add<0x111, 0xf>(x); x = dpp_add<0x112, 0xf>(x); x = dpp_add<0x114, 0xf>(x); x = dpp_add<0x118, 0xf>(x);
  x = dpp_add<0x142, 0xa>(x); x = dpp_add<0x143, 0xc>(x);
  return __int_as_float(__builtin_amdgcn_readlane(__float_as_int(x), 63));
}

#define ST0(k, i)
DI bool spin_fail(unsigned spins, GAS unsigned* tmo) {
  if (spins > (1u << 16)) { __hip_atomic_store(tmo, 1u, RLX_AGENT); return true; }
  if ((spins & 255u) == 255u && uni(__hip_atomic_load(tmo, RLX_AGENT))) return true;
  __builtin_amdgcn_s_sleep(1);
  return false;
}
DI void wait_count(GAS unsigned* cnt, unsigned want, GAS unsigned* tmo) {
  for (unsigned spins = 0;; ++spins) {
    if ((unsigned)uni(__hip_atomic_load(cnt, RLX_AGENT)) >= want) break;
    if (spin_fail(spins, tmo)) break;
  }
}

template<int SW> __host__ __device__ constexpr int bmask(int pos) { return SW == 64 ? ((pos >> 1) & 3) : 0; }
__host__ __device__ constexpr int kswap(int k) { return (k & ~12) | ((k & 4) << 1) | ((k & 8) >> 1); }
DI unsigned aimg(int r, int c) { return r * 128 + ((c ^ ((r >> 1) & 7)) * 16); }

template<int SW_, int NS_, int WM_, int WN_, int ST_, int NKT_, bool BT_ = false> struct Cfg {
  static constexpr int BM = 128, SW = SW_, NS = NS_, BN = SW_ * NS_, WM = WM_, WN = WN_, ST = ST_, NKT = NKT_;
  static constexpr bool BT = BT_;
  static constexpr int MT = BM / WM / 16, NT = BN / WN / 16;
  static constexpr int A_BYTES = BM * 128, ROWB = SW * 2, STRIP_BYTES = 64 * ROWB, B_BYTES = NS * STRIP_BYTES, STAGE = A_BYTES + B_BYTES, RING = ST * STAGE;
  static constexpr int PB = B_BYTES / 1024;
  static constexpr int IA = 4, IBMAX = (PB + 3) / 4, IBMIN = PB / 4, NBIG = PB - IBMIN * 4;
  static constexpr int TPS = SW / 16 / WN;
  static constexpr bool SWZ = (SW == 64);
  static constexpr int NV = SWZ ? TPS : 1;
  static constexpr int NM = MT * NT, NR = BT ? MT + NT : MT + 2 * NT;
  static_assert(WM * WN == 4 && STRIP_BYTES % 1024 == 0 && NT == TPS * NS && ST >= 3 && NKT >= 2 * ST && 2 * NM >= NR, "config");
  static_assert((ST - 1) * (IA + IBMAX) + 16 <= 63, "vmcnt range");
};
template<class C> struct Frags { bf16x8 a[C::MT]; s16x4 bl[C::NT], bh[C::NT]; bf16x8 b[C::NT]; };

template<class C, int I> DI void read_item(Frags<C>& f, unsigned aA, const unsigned (&aB)[C::NV], int ksimm) {
  if constexpr (C::BT) {
    if constexpr (I >= 1 && I < 1 + C::MT) f.a[I - 1] = ldsld<bf16x8>(aA + (I - 1) * 2048);
    else { constexpr int j = I == 0 ? 0 : I - C::MT; f.b[j] = ldsld<bf16x8>(aB[0] + j * 2048); }
  } else if constexpr (I >= 2 && I < 2 + C::MT) {
    constexpr int m = I - 2;
    f.a[m] = ldsld<bf16x8>(aA + m * 2048);
  } else {
    constexpr int r = I < 2 ? I : I - C::MT, j = r >> 1, u = r & 1;
    constexpr int strip = j / C::TPS, tj = j % C::TPS;
    constexpr int imm = strip * C::STRIP_BYTES + u * 8 * C::ROWB + (C::SWZ ? 0 : tj * 32);
    const s16x4 v = trread(aB[C::SWZ ? tj : 0] + imm + ksimm);
    if constexpr (u == 0) f.bl[j] = v; else f.bh[j] = v;
  }
}
template<class C, int I> DI void mfma_item(f32x4 (&acc)[C::NT][C::MT], const Frags<C>& f) {
  constexpr int n = I / C::MT, m = I % C::MT;
  if constexpr (C::BT) acc[n][m] = mfma(f.b[n], f.a[m], acc[n][m]); else acc[n][m] = mfma(join(f.bl[n], f.bh[n]), f.a[m], acc[n][m]);
}
template<class C, int... Is>
DI void step(f32x4 (&acc)[C::NT][C::MT], const Frags<C>& cur, Frags<C>& nxt, unsigned aA, const unsigned (&aB)[C::NV], int ksimm, std::integer_sequence<int, Is...>) {
  ((mfma_item<C, Is>(acc, cur), FENCE(),
    [&] { if constexpr (C::NR <= C::NM) { if constexpr (Is < C::NR) read_item<C, Is>(nxt, aA, aB, ksimm); }
          else { if constexpr (2 * Is < C::NR) read_item<C, 2 * Is>(nxt, aA, aB, ksimm); if constexpr (2 * Is + 1 < C::NR) read_item<C, 2 * Is + 1>(nxt, aA, aB, ksimm); } }(),
    FENCE()), ...);
}
template<class C, int... Is> DI void read_all(Frags<C>& f, unsigned aA, const unsigned (&aB)[C::NV], std::integer_sequence<int, Is...>) { (read_item<C, Is>(f, aA, aB, 0), ...); }
template<class C> DI void zero_acc(f32x4 (&acc)[C::NT][C::MT]) {
#pragma unroll
  for (int n = 0; n < C::NT; ++n)
#pragma unroll
    for (int m = 0; m < C::MT; ++m) { acc[n][m] = (f32x4){0.f, 0.f, 0.f, 0.f}; asm volatile("" : "+v"(acc[n][m])); }
}

template<class C> struct Consumer {
  unsigned aA, aB[C::NV]; int slot; Frags<C> F0, F1;
  DI void init(unsigned lds0, int wid, int lane) {
    const int wc = wid % C::WN, wr = wid / C::WN, g = lane >> 4, i16 = lane & 15, q = i16 >> 2, p = lane & 3;
    const int row = wr * (C::BM / C::WM) + i16;
    aA = lds0 + aimg(row, g);
    const int pos = (g >> 1) * 16 + (g & 1) * 4 + q;
    const unsigned base = lds0 + C::A_BYTES + pos * C::ROWB + (p >> 1) * 16 + (p & 1) * 8;
#pragma unroll
    for (int v = 0; v < C::NV; ++v) aB[v] = base + (C::SWZ ? (((wc * C::TPS + v) ^ bmask<C::SW>(pos)) * 32) : wc * C::TPS * 32);
    if constexpr (C::BT) aB[0] = lds0 + C::A_BYTES + aimg(wc * C::TPS * 16 + i16, g);
    slot = 0;
  }
  DI void start() { lbar(); read_all<C>(F0, aA, aB, iseq<C::NR>{}); FENCE(); }
  DI void run(f32x4 (&acc)[C::NT][C::MT], int tiles) {
#pragma unroll 1
    for (int t = 0; t < tiles; ++t) {
      unsigned b[C::NV];
      unsigned o = slot * C::STAGE;
#pragma unroll
      for (int v = 0; v < C::NV; ++v) b[v] = aB[v] + o;
      if constexpr (C::BT) b[0] = (aB[0] ^ 64) + o;
      step<C>(acc, F0, F1, (aA ^ 64) + o, b, 32 * C::ROWB, iseq<C::NM>{});
      slot = slot + 1 == C::ST ? 0 : slot + 1;
      lbar();
      o = slot * C::STAGE;
#pragma unroll
      for (int v = 0; v < C::NV; ++v) b[v] = aB[v] + o;
      step<C>(acc, F1, F0, aA + o, b, 0, iseq<C::NM>{});
    }
  }
};

struct Dep {
  __amdgpu_buffer_rsrc_t rs; unsigned flagoff; GAS unsigned* tmo; unsigned seen;
  DI void ensure(unsigned n, int lane) {
    for (unsigned spins = 0; (seen & n) != n; ++spins) {
      seen |= (unsigned)__ballot(__builtin_amdgcn_raw_buffer_load_b32(rs, flagoff + (lane & 15) * 4, 0, 16) != 0u) & 0xffffu;
      if ((seen & n) == n) break;
      if (spin_fail(spins, tmo)) seen = 0xffffu;
    }
  }
};
template<class C> struct Pieces {
  int lw; unsigned lds0;
  template<bool SC1, int... Is> DI void a_img(int slot, unsigned v, i32x4 rs, unsigned off, std::integer_sequence<int, Is...>) const {
    (dma16<SC1>(lds0 + slot * C::STAGE + (lw + 4 * Is) * 1024, v, rs, off + (lw + 4 * Is) * 1024), ...); }
  template<int... Is> DI void a_rows(int slot, unsigned v, i32x4 rs, unsigned off, unsigned lda, std::integer_sequence<int, Is...>) const {
    (dma16<false>(lds0 + slot * C::STAGE + (lw + 4 * Is) * 1024, v, rs, off + (lw + 4 * Is) * 8 * lda), ...); }
  template<int... Is> DI void b_img(int slot, unsigned v, i32x4 rs, unsigned off, std::integer_sequence<int, Is...>) const {
    ([&] { if (Is < C::IBMIN || lw < C::NBIG) dma16<true>(lds0 + slot * C::STAGE + C::A_BYTES + (lw + 4 * Is) * 1024, v, rs, off + (lw + 4 * Is) * 1024); }(), ...); }
  template<int... Is> DI void b_w64(int slot, unsigned v, i32x4 rs, unsigned off, unsigned ldb, unsigned stripbytes, std::integer_sequence<int, Is...>) const {
    ([&] { constexpr int strip = Is / 2; const int pp = lw + (Is & 1) * 4;
           dma16<false>(lds0 + slot * C::STAGE + C::A_BYTES + strip * 8192 + pp * 1024, v, rs, off + strip * stripbytes + (16 * (pp >> 1) + 4 * (pp & 1)) * ldb); }(), ...); }
  template<int NA, int NB, int X = 0> DI void wait() const {
    if constexpr (C::NBIG == 0) waitv<NA * C::IA + NB * C::IBMAX + X>();
    else { if (lw < C::NBIG) waitv<NA * C::IA + NB * C::IBMAX + X>(); else waitv<NA * C::IA + NB * C::IBMIN + X>(); }
  }
};
DI unsigned voff_rows(int lw, int lane, unsigned lda) { return (lane >> 3) * lda + (((lane & 7) ^ (((lw & 1) << 2) | (lane >> 4))) * 16); }
DI unsigned voff_w64(int lane, unsigned ldb) { return (8 * (lane >> 5) + ((lane >> 3) & 3)) * ldb + (((lane & 7) ^ (((lane >> 4) & 3) << 1)) * 16); }

template<class C, int NPRE, class Src> DI void producer_loop(const Pieces<C>& P, Src& s) {
  constexpr int ST = C::ST, NKT = C::NKT; constexpr bool DEPB = Src::DEPB;
  if constexpr (!Src::DEP) {
    s.dep_half(0, 0); s.w_half(0, 0); s.dep_half(1, 1); s.w_half(1, 1);
    P.template wait<1, 1>(); bar();
#pragma unroll
    for (int i = 2; i < ST; ++i) { s.dep_half(i, i); s.w_half(i, i); }
  } else {
    s.dep_half(0, 0);
#pragma unroll
    for (int i = NPRE; i < ST; ++i) s.w_half(i, i);
    constexpr int WY = NPRE > 0 ? ST - NPRE : ST - 1;
    P.template wait<DEPB ? WY : 0, DEPB ? 0 : WY>(); bar();
#pragma unroll
    for (int i = 1; i < ST; ++i) s.dep_half(i, i);
  }
  s.early();
  int slot = 0;
  auto advance = [&](int t) {
    s.dep_half(t - 1 + ST, slot); s.w_half(t - 1 + ST, slot);
    slot = slot + 1 == ST ? 0 : slot + 1;
  };
  [&]<int... Is>(std::integer_sequence<int, Is...>) {
    (([&] { constexpr int t = Is + 1;
            if constexpr (!Src::DEP) P.template wait<ST - 2, ST - 2, Src::EARLY>();
            else if constexpr (DEPB) P.template wait<t - 1, ST - 2, Src::EARLY>(); else P.template wait<ST - 2, t - 1, Src::EARLY>();
            lbar(); advance(t); }()), ...);
  }(iseq<ST - 1>{});
  s.steady(P, slot);
  [&]<int... Is>(std::integer_sequence<int, Is...>) {
    (([&] { constexpr int left = ST - 2 - Is > 0 ? ST - 2 - Is : 0, t = NKT - ST + 1 + Is;
            P.template wait<left, left, Is == 0 ? 0 : Src::LATE>();
            if constexpr (t == NKT) s.last();
            lbar();
            if constexpr (Is == 0) s.late(); }()), ...);
  }(iseq<ST>{});
}
template<class C, class F> DI void tile_loop(const Pieces<C>& P, int& slot, int t0, int t1, F issue) {
#pragma unroll 1
  for (int t = t0; t <= t1; ++t) {
    P.template wait<C::ST - 2, C::ST - 2>(); lbar();
    issue(t - 1 + C::ST, slot);
    slot = slot + 1 == C::ST ? 0 : slot + 1;
  }
}
struct SrcHooks { static constexpr int EARLY = 0, LATE = 0; DI void early() {} DI void late() {} DI void last() {} };
template<int NLINES, class F> DI void prewarm(unsigned ldsdummy, int lw, int lane, i32x4 rs, F off) {
#pragma unroll
  for (int i = 0; i < (NLINES + 255) / 256; ++i) {
    const unsigned idx = (i * 4 + lw) * 64 + lane;
    if ((i * 4 + 4) * 64 <= NLINES || (i * 4 + lw) * 64 < NLINES) touch64(ldsdummy, off(idx < NLINES ? idx : NLINES - 1), rs);
  }
}

__global__ __launch_bounds__(256) void k0_prep(const u16* __restrict__ x, const u16* __restrict__ wqkv, unsigned char* __restrict__ ws) {
  const int tid = threadIdx.x, lane = tid & 63, wid = tid >> 6, b = blockIdx.x;
  ST0(0, 0);
  if (tid < 8) *(u32x4*)(ws + WS_CTL + b * 128 + tid * 16) = (u32x4){0u, 0u, 0u, 0u};
  static_assert(CTL_BYTES == 256 * 128, "control block");
  u32x4 v[2][2];
#pragma unroll
  for (int r = 0; r < 2; ++r)
#pragma unroll
    for (int c = 0; c < 2; ++c) v[r][c] = *(const u32x4*)(x + (size_t)(b * 8 + wid * 2 + r) * DM + c * 512 + lane * 8);
  unsigned pf = 0;
  if (tid < 192) pf = *(const unsigned*)((const char*)wqkv + (size_t)(b * 192 + tid) * 128);
#pragma unroll
  for (int r = 0; r < 2; ++r) {
    float f[16]; float s = 0;
#pragma unroll
    for (int c = 0; c < 2; ++c)
#pragma unroll
      for (int j = 0; j < 4; ++j) { f[c * 8 + 2 * j] = bflo(v[r][c][j]); f[c * 8 + 2 * j + 1] = bfhi(v[r][c][j]); }
#pragma unroll
    for (int j = 0; j < 16; ++j) s += f[j];
    const float mean = wave_sum(s) * (1.0f / DM); float q = 0;
#pragma unroll
    for (int j = 0; j < 16; ++j) { f[j] -= mean; q += f[j] * f[j]; }
    const float rstd = __builtin_amdgcn_rsqf(wave_sum(q) * (1.0f / DM) + 1e-5f);
    const int row = (b & 15) * 8 + wid * 2 + r;
#pragma unroll
    for (int c = 0; c < 2; ++c) { u32x4 o;
#pragma unroll
      for (int j = 0; j < 4; ++j) o[j] = cvtpk(f[c * 8 + 2 * j] * rstd, f[c * 8 + 2 * j + 1] * rstd);
      *(u32x4*)(ws + WS_XN + (size_t)((b >> 4) * 16 + c * 8 + (lane >> 3)) * 16384 + aimg(row, lane & 7)) = o; }
  }
  asm volatile("" :: "v"(pf));
  ST0(0, 7);
}

struct Ctx {
  unsigned lds0; int tid, lane, wid, pm, pn;
  unsigned char* ws; __amdgpu_buffer_rsrc_t rs;
  DI GAS unsigned* cnt(int i) const { return (GAS unsigned*)(ws + WS_CTL) + i * 64; }
  DI i32x4 ws_srd() const { return make_srd(ws, (unsigned)WS_END); }
  DI Dep dep(bool on, int line, int own) const { return Dep{rs, (unsigned)WS_CTL + line * 256, cnt(CNT_TMO), on ? 1u << own : 0xffffu}; }
};
constexpr int AUX_SC1 = 16;
constexpr unsigned ZST_EMPTY = 0xFFFFFFFFu;
DI void st16(const Ctx& c, unsigned off, u32x4 v) { __builtin_amdgcn_raw_buffer_store_b128(v, c.rs, off, 0, AUX_SC1); }
DI void st8(const Ctx& c, unsigned off, f32x2 v) { __builtin_amdgcn_raw_buffer_store_b64(__builtin_bit_cast(u32x2, v), c.rs, off, 0, AUX_SC1); }
DI void st4(const Ctx& c, unsigned off, unsigned v) { __builtin_amdgcn_raw_buffer_store_b32(v, c.rs, off, 0, AUX_SC1); }
DI u32x4 ld16(const Ctx& c, unsigned off) { return __builtin_amdgcn_raw_buffer_load_b128(c.rs, off, 0, AUX_SC1); }
DI f32x2 ld8(const Ctx& c, unsigned off) { return __builtin_bit_cast(f32x2, __builtin_amdgcn_raw_buffer_load_b64(c.rs, off, 0, AUX_SC1)); }
DI float ld4(const Ctx& c, unsigned off) { return __uint_as_float(__builtin_amdgcn_raw_buffer_load_b32(c.rs, off, 0, AUX_SC1)); }
DI void set_flag(const Ctx& c, int line, int word) { if (c.wid == 4 && c.lane == 0) st4(c, WS_CTL + line * 256 + word * 4, 1u); }
DI void publish(const Ctx& c, int line, int word) { waitl0(); waitv<0>(); bar(); set_flag(c, line, word); }

using C1 = Cfg<64, 3, 2, 2, 3, 16>;
using C2 = Cfg<64, 1, 2, 2, 4, 16>;
using C3 = Cfg<112, 1, 4, 1, 4, 64, true>;
using C5 = Cfg<64, 1, 2, 2, 4, 28>;
constexpr int PRE2 = 4, PRE3 = 2, PRE5 = 4;
constexpr int LDS_DUMMY = 155648, LDS_CS4 = LDS_DUMMY + 256, LDS_TOTAL = LDS_CS4 + 2048;

template<class C, bool DEP_, int LDB, class Need> struct SrcW64 : SrcHooks {
  static constexpr bool DEP = DEP_, DEPB = false;
  Pieces<C> P; int lane, kt0; i32x4 rsA, rsB; unsigned a0, b0, strip, va, vb; Dep dep; Need need;
  DI SrcW64(const Pieces<C>& P_, int lane_, int kt0_, i32x4 rsA_, unsigned a0_, i32x4 rsB_, unsigned b0_, unsigned strip_, Dep dep_, Need need_)
    : P(P_), lane(lane_), kt0(uni(kt0_)), rsA(rsA_), rsB(rsB_), a0(uni(a0_)), b0(uni(b0_)), strip(strip_), va(lane_ * 16), vb(voff_w64(lane_, LDB)), dep(dep_), need(need_) {}
  DI int kt(int i) const { const int k = kt0 + i; return k >= C::NKT ? k - C::NKT : k; }
  DI void dep_half(int i, int slot) { const int k = kt(i); if (DEP && i > 0) dep.ensure(need(k), lane); P.template a_img<DEP>(slot, va, rsA, a0 + k * 16384, iseq<C::IA>{}); }
  DI void w_half(int i, int slot) const { P.b_w64(slot, vb, rsB, b0 + kt(i) * (64 * LDB), LDB, strip, iseq<C::IBMAX>{}); }
  DI void steady(const Pieces<C>&, int& slot) { tile_loop<C>(P, slot, C::ST, C::NKT - C::ST, [&](int i, int sl) { dep_half(i, sl); w_half(i, sl); }); }
};
template<class C, int NPRE, int LDB> DI void prefetch_w64(unsigned lds0, int lw, int lane, const u16* W, unsigned bytes, unsigned b0, int kt0) {
  const Pieces<C> P{lw, lds0}; const i32x4 rs = make_srd(W, bytes); const unsigned vb = voff_w64(lane, LDB);
  b0 = uni(b0); kt0 = uni(kt0);
#pragma unroll
  for (int s = 0; s < NPRE; ++s) { const int k = kt0 + s >= C::NKT ? kt0 + s - C::NKT : kt0 + s; P.b_w64(s, vb, rs, b0 + k * (64 * LDB), LDB, 0, iseq<C::IBMAX>{}); }
}
DI int kt0_5(int pn) { return (7 * pn + 3) >> 2; }
DI unsigned win_off(int pn, int half, int kt) { return (unsigned)WS_WIN + ((pn * 2 + half) * 16 + kt) * C3::B_BYTES; }

constexpr int K1_WPHI = C1::RING;
constexpr int K1_RW = K1_WPHI + 4096;
constexpr int K1_CSP = K1_RW + 2 * C3::B_BYTES;
constexpr int K1_QX = 0, K1_KX = 16384;
constexpr int K1_PHI = 32768, PHI_RS = 136, PHI_HEAD = 128 * PHI_RS;
constexpr int K1_V = K1_PHI + 2 * PHI_HEAD, V_RS = 72, V_HEAD = 128 * V_RS;
constexpr int K1_CS4 = LDS_CS4;
constexpr int K1_KVT = C2::RING;
constexpr int K1_ZT = K1_KVT + 8192;
constexpr int K1_OST = K1_ZT + 256;
static_assert(K1_CSP <= LDS_DUMMY && K1_V + 2 * V_HEAD <= K1_KVT && K1_OST + 16384 <= LDS_DUMMY && K1_OST % 16 == 0 && K1_V % 8 == 0, "P1 map");

DI void win_fetch(const Ctx& c, const u16* w_in, int lw) {
  const i32x4 rs = make_srd(w_in, DM * FF * 2);
  const unsigned off = uni((64 * c.pm + 32 * (lw & 1)) * (FF * 2) + ((lw >> 1) * FH + c.pn * 112) * 2);
#pragma unroll
  for (int i = 0; i < 7; ++i) { const unsigned lin = i * 64 + c.lane, pos = lin / 14u, ch = lin - pos * 14u;
    dma16<false>(c.lds0 + K1_RW + lw * 7168 + i * 1024, kswap(pos) * (FF * 2) + ch * 16, rs, off); }
}
DI void win_put(const Ctx& c, int lw) {
  const int lane = c.lane, g = lane >> 4, i16 = lane & 15;
  const unsigned rd = c.lds0 + K1_RW + (lw >> 1) * C3::B_BYTES + ((lw & 1) * 32 + (g >> 1) * 16 + (g & 1) * 4 + (i16 >> 2)) * 224 + (lane & 3) * 8;
  u32x4 v[7];
#pragma unroll
  for (int j = 0; j < 7; ++j) v[j] = __builtin_bit_cast(u32x4, join(trread(rd + j * 32), trread(rd + 8 * 224 + j * 32)));
#pragma unroll
  for (int j = 0; j < 7; ++j) st16(c, win_off(c.pn, lw >> 1, c.pm) + aimg(j * 16 + i16, (lw & 1) * 4 + g), v[j]);
#pragma unroll
  for (int j = 0; j < 7; ++j) { const float s = sum4(dot2(v[j][3], ONES2, dot2(v[j][2], ONES2, dot2(v[j][1], ONES2, dot2(v[j][0], ONES2, 0.f)))));
    if (lane < 16) ldsst<float>(c.lds0 + K1_CS4 + lw * 512 + (j * 16 + lane) * 4, s); }
}

DI float max3(float a, float b, float c) { float r; asm("v_max3_f32 %0, %1, %2, %3" : "=v"(r) : "v"(a), "v"(b), "v"(c)); return r; }
DI float nmax3(float a, float b, float c) { float r; asm("v_max3_f32 %0, -%1, -%2, -%3" : "=v"(r) : "v"(a), "v"(b), "v"(c)); return r; }
DI void feature_maps(const bf16x8 wf0, const bf16x8 wf1, const bf16x8 (&fr)[2], u32x4 (&pos)[2], u32x4 (&neg)[2]) {
  constexpr float L2E = 1.4426950408889634f;
  const f32x4 z4 = {0.f, 0.f, 0.f, 0.f};
  f32x4 pa0 = mfma(wf0, fr[0], z4), pa1 = mfma(wf1, fr[0], z4), pb0 = mfma(wf0, fr[1], z4), pb1 = mfma(wf1, fr[1], z4);
  asm volatile("s_nop 7" : "+v"(pa0), "+v"(pa1), "+v"(pb0), "+v"(pb1));
  const f32x4 p[2][2] = {{pa0, pa1}, {pb0, pb1}};
  float mx[2], nm[2];
#pragma unroll
  for (int i = 0; i < 2; ++i) {
    mx[i] = max3(max3(p[i][0][0], p[i][0][1], p[i][0][2]), max3(p[i][0][3], p[i][1][0], p[i][1][1]), max3(p[i][1][2], p[i][1][3], p[i][1][3]));
    nm[i] = max3(nmax3(p[i][0][0], p[i][0][1], p[i][0][2]), nmax3(p[i][0][3], p[i][1][0], p[i][1][1]), nmax3(p[i][1][2], p[i][1][3], p[i][1][3]));
  }
  max4x4(mx[0], nm[0], mx[1], nm[1]);
  float ep[2][8], en[2][8], sp[2] = {0.f, 0.f}, sn[2] = {0.f, 0.f};
#pragma unroll
  for (int i = 0; i < 2; ++i) {
    const float cx = -mx[i] * L2E, cn = -nm[i] * L2E;
#pragma unroll
    for (int j = 0; j < 8; ++j) { const float xv = p[i][j >> 2][j & 3];
      ep[i][j] = __builtin_amdgcn_exp2f(__builtin_fmaf(xv, L2E, cx)); en[i][j] = __builtin_amdgcn_exp2f(__builtin_fmaf(xv, -L2E, cn)); sp[i] += ep[i][j]; sn[i] += en[i][j]; }
  }
  sum4x4(sp[0], sn[0], sp[1], sn[1]);
#pragma unroll
  for (int i = 0; i < 2; ++i) {
    const float rp = __builtin_amdgcn_rcpf(sp[i]), rn = __builtin_amdgcn_rcpf(sn[i]);
#pragma unroll
    for (int j = 0; j < 4; ++j) { pos[i][j] = cvtpk(ep[i][2 * j] * rp, ep[i][2 * j + 1] * rp); neg[i][j] = cvtpk(en[i][2 * j] * rn, en[i][2 * j + 1] * rn); }
  }
}

template<bool NEXT> DI void phase1(const Ctx& c, const u16* wqkv, const u16* wphi, const u16* wo, const u16* w_in, const u16* w_out) {
  const unsigned lds0 = c.lds0; const int tid = c.tid, lane = c.lane, wid = c.wid, pm = c.pm, pn = c.pn; unsigned char* ws = c.ws;
  const int g = lane >> 4, i16 = lane & 15, q4 = i16 >> 2, p4 = lane & 3;
  const int w4 = wid & 3, wr = w4 >> 1, wc = w4 & 1, mp = wid >> 2;
  ST0(1, 0);
  if (wid == 3) st16(c, WS_ZST + (pm * 16 + pn) * 1024 + lane * 16, (u32x4){ZST_EMPTY, ZST_EMPTY, ZST_EMPTY, ZST_EMPTY});
  if (wid < 4) {
    const u32x4 wp = *(const u32x4*)(wphi + (size_t)pn * 2048 + tid * 8);
    f32x4 acc[C1::NT][C1::MT];
    Consumer<C1> K; K.init(lds0, wid, lane); zero_acc<C1>(acc);
    ST0(1, 1);
    K.start();
    ST0(1, 2);
    ldsst<u32x4>(lds0 + K1_WPHI + tid * 16, wp);
    K.run(acc, C1::NKT);
    ST0(1, 3);
#pragma unroll
    for (int m = 0; m < 4; ++m) {
      ldsst<u32x4>(lds0 + K1_KX + (w4 * 4 + m) * 1024 + lane * 16, pack8(acc[2][m], acc[3][m]));
      const u32x4 vv = pack8(acc[4][m], acc[5][m]);
      const unsigned va = lds0 + K1_V + wc * V_HEAD + (wr * 64 + m * 16 + i16) * V_RS + g * 8;
      ldsst<u32x2>(va, (u32x2){vv[0], vv[1]}); ldsst<u32x2>(va + 32, (u32x2){vv[2], vv[3]});
      ldsst<u32x4>(lds0 + K1_QX + (w4 * 4 + m) * 1024 + lane * 16, pack8(acc[0][m], acc[1][m]));
    }
  } else {
    const Pieces<C1> P{w4, lds0};
    auto none = [](int) { return 0u; };
    SrcW64<C1, false, 3 * DM * 2, decltype(none)> src(P, lane, 0, c.ws_srd(), (unsigned)WS_XN + pm * (16 * 16384), make_srd(wqkv, DM * 3 * DM * 2), (unsigned)pn * 128,
                                                      (unsigned)DM * 2, c.dep(false, 0, 0), none);
    producer_loop<C1, 0>(P, src);
    win_fetch(c, w_in, w4);
    const unsigned b = blockIdx.x;
    prewarm<64>(lds0 + LDS_DUMMY, w4, lane, make_srd(wo, DM * DM * 2), [&](unsigned i) { return (b * 64 + i) * 128; });
    prewarm<112>(lds0 + LDS_DUMMY, 3 - w4, lane, make_srd(w_out, FH * DM * 2), [&](unsigned i) { return (b * 112 + i) * 128; });
  }
  lbar();
  const unsigned wph = lds0 + K1_WPHI + wc * 2048 + (4 * g + q4) * 64 + p4 * 8;
  const bf16x8 wf0 = join(trread(wph), trread(wph + 1024)), wf1 = join(trread(wph + 32), trread(wph + 1024 + 32));
  { bf16x8 kf[2]; u32x4 pos[2], neg[2];
#pragma unroll
    for (int i = 0; i < 2; ++i) kf[i] = ldsld<bf16x8>(lds0 + K1_KX + (w4 * 4 + 2 * mp + i) * 1024 + lane * 16);
    FENCE();
    feature_maps(wf0, wf1, kf, pos, neg);
#pragma unroll
    for (int i = 0; i < 2; ++i) {
      const unsigned pa = lds0 + K1_PHI + wc * PHI_HEAD + (wr * 64 + (2 * mp + i) * 16 + i16) * PHI_RS + g * 8;
      ldsst<u32x2>(pa, (u32x2){pos[i][0], pos[i][1]}); ldsst<u32x2>(pa + 32, (u32x2){pos[i][2], pos[i][3]});
      ldsst<u32x2>(pa + 64, (u32x2){neg[i][0], neg[i][1]}); ldsst<u32x2>(pa + 96, (u32x2){neg[i][2], neg[i][3]});
    } }
  lbar();
  ST0(1, 4);
  { f32x4 d[2] = {{0.f, 0.f, 0.f, 0.f}, {0.f, 0.f, 0.f, 0.f}}, dz = {0.f, 0.f, 0.f, 0.f};
    const bf16x8 ones = asbf((u32x4){ONES2, ONES2, ONES2, ONES2});
    const unsigned pr = lds0 + K1_PHI + wc * PHI_HEAD + (4 * g + q4) * PHI_RS + wr * 64 + p4 * 8;
    const unsigned vr = lds0 + K1_V + wc * V_HEAD + (4 * g + q4) * V_RS + mp * 32 + p4 * 8;
    bf16x8 pf[4][2], vf[4];
#pragma unroll
    for (int s = 0; s < 4; ++s) {
#pragma unroll
      for (int a = 0; a < 2; ++a) pf[s][a] = join(trread(pr + s * 32 * PHI_RS + a * 32), trread(pr + (s * 32 + 16) * PHI_RS + a * 32));
      vf[s] = join(trread(vr + s * 32 * V_RS), trread(vr + (s * 32 + 16) * V_RS)); }
#pragma unroll
    for (int s = 0; s < 4; ++s) { d[0] = mfma(pf[s][0], vf[s], d[0]); d[1] = mfma(pf[s][1], vf[s], d[1]); dz = mfma(mp ? pf[s][1] : pf[s][0], ones, dz); }
    st16(c, WS_KVP + ((pn * 16 + pm) * 2 + wc) * 4096 + (wr * 2 + mp) * 1024 + lane * 16, pack8(d[0], d[1]));
    if (i16 == 0) st16(c, WS_ZP + ((pn * 16 + pm) * 2 + wc) * 256 + wr * 128 + g * 32 + mp * 16, __builtin_bit_cast(u32x4, dz)); }
  waitv<0>(); bar();
  if (wid == 4 && lane == 0) __hip_atomic_fetch_add(c.cnt(CNT_KV + pn), 1u, RLX_AGENT);
  u32x4 phiq[2][2];
  { bf16x8 qf[2]; u32x4 pos[2], neg[2];
#pragma unroll
    for (int i = 0; i < 2; ++i) qf[i] = ldsld<bf16x8>(lds0 + K1_QX + (w4 * 4 + 2 * mp + i) * 1024 + lane * 16);
    FENCE();
    feature_maps(wf0, wf1, qf, pos, neg);
#pragma unroll
    for (int i = 0; i < 2; ++i) { phiq[i][0] = pos[i]; phiq[i][1] = neg[i]; } }
  if (wid == 4) wait_count(c.cnt(CNT_KV + pn), 16u, c.cnt(CNT_TMO));
  lbar();
  ST0(1, 5);
  { const int head = wid & 1, fr = wid >> 1;
    u32x4 pv[16];
#pragma unroll
    for (int p = 0; p < 16; ++p) pv[p] = ld16(c, WS_KVP + ((pn * 16 + p) * 2 + head) * 4096 + fr * 1024 + lane * 16);
    float zs = 0.f;
    if (wid < 2) {
      float zv[16];
#pragma unroll
      for (int p = 0; p < 16; ++p) zv[p] = ld4(c, WS_ZP + ((pn * 16 + p) * 2 + head) * 256 + lane * 4);
#pragma unroll
      for (int p = 0; p < 16; ++p) zs += zv[p];
    }
    f32x4 s0 = {0.f, 0.f, 0.f, 0.f}, s1 = s0;
#pragma unroll
    for (int p = 0; p < 16; ++p) { s0 += (f32x4){bflo(pv[p][0]), bfhi(pv[p][0]), bflo(pv[p][1]), bfhi(pv[p][1])}; s1 += (f32x4){bflo(pv[p][2]), bfhi(pv[p][2]), bflo(pv[p][3]), bfhi(pv[p][3])}; }
    ldsst<u32x4>(lds0 + K1_KVT + (head * 4 + fr) * 1024 + lane * 16, pack8(s0, s1));
    if (wid < 2) ldsst<u16>(lds0 + K1_ZT + head * 128 + lane * 2, (u16)cvtpk(zs, 0.f));
  }
  if (NEXT && wid >= 4) prefetch_w64<C2, PRE2, DM * 2>(lds0, w4, lane, wo, DM * DM * 2, (unsigned)pn * 128, pn);
  lbar();
  ST0(1, 6);
  { bf16x8 kv[2][2], zf[2];
#pragma unroll
    for (int ks = 0; ks < 2; ++ks) { zf[ks] = ldsld<bf16x8>(lds0 + K1_ZT + wc * 128 + ks * 64 + g * 16);
#pragma unroll
      for (int dt = 0; dt < 2; ++dt) kv[ks][dt] = ldsld<bf16x8>(lds0 + K1_KVT + (wc * 4 + ks * 2 + dt) * 1024 + lane * 16); }
#pragma unroll
    for (int i = 0; i < 2; ++i) {
      const f32x4 z4 = {0.f, 0.f, 0.f, 0.f};
      const bf16x8 b0 = asbf(phiq[i][0]), b1 = asbf(phiq[i][1]);
      const f32x4 den = mfma(zf[1], b1, mfma(zf[0], b0, z4));
      const f32x4 n0 = mfma(kv[1][0], b1, mfma(kv[0][0], b0, z4)), n1 = mfma(kv[1][1], b1, mfma(kv[0][1], b0, z4));
      const float r = __builtin_amdgcn_rcpf(den[0] + 1e-6f);
      const int tok = wr * 64 + (2 * mp + i) * 16 + i16;
      const unsigned oa = lds0 + K1_OST + (g & 1) * 8;
      ldsst<u32x2>(oa + aimg(tok, wc * 4 + (g >> 1)), (u32x2){cvtpk(n0[0] * r, n0[1] * r), cvtpk(n0[2] * r, n0[3] * r)});
      ldsst<u32x2>(oa + aimg(tok, wc * 4 + 2 + (g >> 1)), (u32x2){cvtpk(n1[0] * r, n1[1] * r), cvtpk(n1[2] * r, n1[3] * r)});
    } }
  lbar();
#pragma unroll
  for (int i = 0; i < 2; ++i) { const int p = tid + i * 512; st16(c, WS_O + (pm * 16 + pn) * 16384 + p * 16, ldsld<u32x4>(lds0 + K1_OST + p * 16)); }
  ST0(1, 7);
}

template<class Base> struct Src2 : Base {
  const Ctx& c;
  template<class... A> DI Src2(const Ctx& c_, A... a) : Base(a...), c(c_) {}
  DI void steady(const Pieces<C2>&, int& slot) {
    auto is = [&](int i, int sl) { this->dep_half(i, sl); this->w_half(i, sl); };
    tile_loop<C2>(this->P, slot, C2::ST, 10, is);
    set_flag(c, FLG_W + c.pn, c.pm);
    tile_loop<C2>(this->P, slot, 11, C2::NKT - C2::ST, is);
  }
};
template<class C, bool FIRST, int KID, int NPRE, bool NEXT>
DI void phase_res(const Ctx& c, bool dep_on, unsigned a_off, const u16* W, unsigned w_bytes, const u16* x, u16* out) {
  const unsigned lds0 = c.lds0; const int tid = c.tid, lane = c.lane, wid = c.wid, pm = c.pm, pn = c.pn; unsigned char* ws = c.ws;
  ST0(KID, 0);
  if (wid >= 4) {
    const Pieces<C> P{wid - 4, lds0};
    if constexpr (FIRST) {
      auto need = [](int kt) { return 1u << kt; };
      Src2<SrcW64<C, true, DM * 2, decltype(need)>> src(c, P, lane, pn, c.ws_srd(), a_off + (unsigned)pm * (C::NKT * 16384), make_srd(W, w_bytes), (unsigned)pn * 128, 0, c.dep(dep_on, FLG_O + pm, pn), need);
      producer_loop<C, NPRE>(P, src);
    } else {
      auto need = [](int kt) { return (1u << ((4u * kt) / 7u)) | (1u << ((8u * kt + 7u) / 14u)); };
      SrcW64<C, true, DM * 2, decltype(need)> src(P, lane, kt0_5(pn), c.ws_srd(), a_off + (unsigned)pm * (C::NKT * 16384), make_srd(W, w_bytes), (unsigned)pn * 128, 0, c.dep(dep_on, FLG_G + pm, pn), need);
      producer_loop<C, NPRE>(P, src);
    }
    bar();
    if constexpr (NEXT) {
      Dep dw = c.dep(true, FLG_W + pn, pm); dw.ensure(0xffffu, lane);
      const Pieces<C3> P3{wid - 4, lds0}; const i32x4 rs = c.ws_srd();
#pragma unroll
      for (int s = 0; s < PRE3; ++s) P3.b_img(s, lane * 16, rs, uni(win_off(pn, 1, (pn + s) & 15)), iseq<C3::IBMAX>{});
    }
  } else {
    u32x4 rv[4];
    f32x4 acc[C::NT][C::MT];
    Consumer<C> K; K.init(lds0, wid, lane); zero_acc<C>(acc);
    ST0(KID, 1);
    K.start();
    ST0(KID, 2);
#pragma unroll
    for (int i = 0; i < 4; ++i) { const int p = tid + i * 256, row = p >> 3, cc = (p & 7) ^ ((row >> 1) & 7);
      rv[i] = FIRST ? *(const u32x4*)((const unsigned char*)x + (size_t)(pm * 128 + row) * (DM * 2) + pn * 128 + cc * 16) : ld16(c, WS_H + (pm * 16 + pn) * 16384 + p * 16); }
    FENCE();
    if constexpr (FIRST) {
      K.run(acc, 3);
      win_put(c, wid);
      K.run(acc, 1);
      if (tid < 224) { const unsigned a = lds0 + K1_CS4 + (tid < 112 ? tid * 4 : 1024 + (tid - 112) * 4);
        st4(c, WS_CSP + ((pn * 16 + pm) * 224 + tid) * 4, __float_as_uint(ldsld<float>(a) + ldsld<float>(a + 512))); }
      K.run(acc, 5);
      waitv<0>();
      K.run(acc, C::NKT - 9);
    } else K.run(acc, C::NKT);
    ST0(KID, 3);
    const int g = lane >> 4, i16 = lane & 15, wr = wid >> 1, wc = wid & 1;
#pragma unroll
    for (int n = 0; n < 2; ++n)
#pragma unroll
      for (int m = 0; m < 4; ++m)
        ldsst<u32x2>(lds0 + aimg(wr * 64 + m * 16 + i16, wc * 4 + n * 2 + (g >> 1)) + (g & 1) * 8, (u32x2){cvtpk(acc[n][m][0], acc[n][m][1]), cvtpk(acc[n][m][2], acc[n][m][3])});
    lbar();
#pragma unroll
    for (int i = 0; i < 4; ++i) { const int p = tid + i * 256, row = p >> 3, cc = (p & 7) ^ ((row >> 1) & 7);
      const u32x4 a = ldsld<u32x4>(lds0 + p * 16); u32x4 o; float s = 0.f, qq = 0.f;
#pragma unroll
      for (int j = 0; j < 4; ++j) { o[j] = cvtpk(bflo(rv[i][j]) + bflo(a[j]), bfhi(rv[i][j]) + bfhi(a[j]));
        if (FIRST) { s = dot2(o[j], ONES2, s); qq = dot2(o[j], o[j], qq); } }
      if (FIRST) {
        st16(c, WS_H + (pm * 16 + pn) * 16384 + p * 16, o);
        s = dpp_add<0x111, 0xf>(s); qq = dpp_add<0x111, 0xf>(qq); s = dpp_add<0x112, 0xf>(s); qq = dpp_add<0x112, 0xf>(qq);
        s = dpp_add<0x114, 0xf>(s); qq = dpp_add<0x114, 0xf>(qq);
        if ((p & 7) == 7) st8(c, WS_HST + ((pm * 16 + pn) * 128 + row) * 8, (f32x2){s, qq});
      } else *(u32x4*)((unsigned char*)out + (size_t)(pm * 128 + row) * (DM * 2) + pn * 128 + cc * 16) = o;
    }
  }
  ST0(KID, 7);
}

constexpr int Z_RS = 232;
constexpr int K3_STG = C3::RING;
constexpr int K3_RST = K3_STG + 4 * 32 * Z_RS;
constexpr int K3_CS = K3_RST + 1024;
constexpr int K3_SEEN = K3_CS + 1024;
constexpr int G_RS = 240;
static_assert(K3_SEEN + 16 <= LDS_DUMMY, "P34 map");
DI f32x2 gelu2(f32x2 x) {
  constexpr float C0 = -2.0f * 0.7978845608028654f * 1.4426950408889634f, C1_ = C0 * 0.044715f;
  const f32x2 a = x * (x * x * C1_ + C0);
  const f32x2 d = (f32x2){__builtin_amdgcn_exp2f(a[0]), __builtin_amdgcn_exp2f(a[1])} + 1.0f;
  return x * (f32x2){__builtin_amdgcn_rcpf(d[0]), __builtin_amdgcn_rcpf(d[1])};
}
DI void fold_gelu(const f32x4 (&acc)[7][2], u32x2 (&z)[2][7], unsigned lds0, int half, int wid, int lane) {
  const int g = lane >> 4, i16 = lane & 15;
  f32x2 rs[2]; f32x4 cs[7];
#pragma unroll
  for (int m = 0; m < 2; ++m) rs[m] = ldsld<f32x2>(lds0 + K3_RST + (wid * 32 + m * 16 + i16) * 8);
#pragma unroll
  for (int n = 0; n < 7; ++n) cs[n] = ldsld<f32x4>(lds0 + K3_CS + half * 512 + (n * 16 + g * 4) * 4);
  FENCE();
#pragma unroll
  for (int m = 0; m < 2; ++m) {
    const float r = rs[m][1], mr = -rs[m][0] * rs[m][1];
#pragma unroll
    for (int n = 0; n < 7; ++n) {
      const f32x4 v = acc[n][m] * r + cs[n] * mr;
      const f32x2 lo = gelu2((f32x2){v[0], v[1]}), hi = gelu2((f32x2){v[2], v[3]});
      z[m][n] = (u32x2){cvtpk(lo[0], lo[1]), cvtpk(hi[0], hi[1])};
    }
  }
#pragma unroll
  for (int m = 0; m < 2; ++m)
#pragma unroll
    for (int n = 0; n < 7; ++n) asm volatile("" : "+v"(z[m][n]));
}
struct Src34 : SrcHooks {
  static constexpr bool DEP = true, DEPB = false;
  const Ctx& c; Pieces<C3> P; i32x4 rsW, rsS; unsigned vi, vr, h0, w0[2], z0, s0; Dep depH, depZ;
  DI Src34(const Ctx& c_, const Pieces<C3>& P_, const u16* w_sp, bool dep_on)
    : c(c_), P(P_), rsW(c_.ws_srd()), rsS(make_srd(w_sp, SEQ * SEQ * 2)), vi(c_.lane * 16), vr(voff_rows(P_.lw, c_.lane, SEQ * 2)),
      h0(uni((unsigned)WS_H + c_.pm * (16 * 16384))), w0{(unsigned)uni(win_off(c_.pn, 1, 0)), (unsigned)uni(win_off(c_.pn, 0, 0))}, z0(uni((unsigned)WS_Z2 + c_.pn * (32 * C3::B_BYTES))),
      s0(uni(c_.pm * 128 * (SEQ * 2))), depH(c_.dep(dep_on, FLG_H + c_.pm, c_.pn)), depZ(c_.dep(true, FLG_Z2 + c_.pn, c_.pm)) {}
  static_assert(C3::ST == 4, "the segment boundaries below");
  template<int SEG> DI void issue(int i, int slot) {
    if constexpr (SEG < 2) {
      const int k = (c.pn + i) & 15;
      if constexpr (SEG == 0) depH.ensure(1u << k, c.lane);
      P.a_img<true>(slot, vi, rsW, h0 + k * 16384, iseq<C3::IA>{});
      P.b_img(slot, vi, rsW, w0[SEG] + k * C3::B_BYTES, iseq<C3::IBMAX>{});
    } else {
      const int k = (2 * c.pm + i) & 31;
      depZ.ensure(1u << (k >> 1), c.lane);
      P.b_img(slot, vi, rsW, z0 + k * C3::B_BYTES, iseq<C3::IBMAX>{});
      P.a_rows(slot, vr, rsS, s0 + k * 128, SEQ * 2, iseq<C3::IA>{});
    }
  }
  DI void dep_half(int i, int slot) { const int k = (c.pn + i) & 15; if (i > 0) depH.ensure(1u << k, c.lane); P.a_img<true>(slot, vi, rsW, h0 + k * 16384, iseq<C3::IA>{}); }
  DI void w_half(int i, int slot) const { P.b_img(slot, vi, rsW, w0[0] + ((c.pn + i) & 15) * C3::B_BYTES, iseq<C3::IBMAX>{}); }
  DI void steady(const Pieces<C3>&, int& slot) {
    auto s0_ = [&](int i, int sl) { issue<0>(i, sl); }; auto s1_ = [&](int i, int sl) { issue<1>(i, sl); }; auto s2_ = [&](int i, int sl) { issue<2>(i, sl); };
    tile_loop<C3>(P, slot, 4, 12, s0_);
    tile_loop<C3>(P, slot, 13, 16, s1_);
    prewarm<256>(c.lds0 + LDS_DUMMY, P.lw, c.lane, rsS, [&](unsigned i) { return (blockIdx.x * 256 + i) * 128; });
    tile_loop<C3>(P, slot, 17, 23, s1_);
    set_flag(c, FLG_Z2 + c.pn, c.pm);
    tile_loop<C3>(P, slot, 24, 28, s1_);
    depZ.seen |= (unsigned)uni(ldsld<unsigned>(c.lds0 + K3_SEEN));
    tile_loop<C3>(P, slot, 29, C3::NKT - C3::ST, s2_);
  }
};
template<int NPRE, bool NEXT> DI void phase34(const Ctx& c, bool dep_on, const u16* w_sp, const u16* b_sp, const u16* w_out) {
  const unsigned lds0 = c.lds0; const int tid = c.tid, lane = c.lane, wid = c.wid, pm = c.pm, pn = c.pn; unsigned char* ws = c.ws;
  ST0(3, 0);
  if (wid >= 4) {
    const Pieces<C3> P{wid - 4, lds0};
    Src34 src(c, P, w_sp, dep_on);
    producer_loop<C3, NPRE>(P, src);
    if constexpr (NEXT) prefetch_w64<C5, PRE5, DM * 2>(lds0, wid - 4, lane, w_out, FH * DM * 2, (unsigned)pn * 128, kt0_5(pn));
    return;
  }
  const int g = lane >> 4, i16 = lane & 15;
  f32x4 acc[7][2];
  Consumer<C3> K; K.init(lds0, wid, lane); zero_acc<C3>(acc);
  ST0(3, 1);
  K.start();
  ST0(3, 2);
  K.run(acc, 13);
  f32x2 st[8]; float cs[16];
#pragma unroll
  for (int p = 0; p < 8; ++p) st[p] = ld8(c, WS_HST + (((pm * 16 + (tid & 1) * 8) + p) * 128 + (tid >> 1)) * 8);
#pragma unroll
  for (int p = 0; p < 16; ++p) cs[p] = ld4(c, WS_CSP + ((pn * 16 + p) * 224 + (tid < 224 ? tid : 0)) * 4);
  FENCE();
  K.run(acc, 2);
  { f32x2 s = (st[0] + st[1]) + (st[2] + st[3]) + ((st[4] + st[5]) + (st[6] + st[7]));
    s[0] = dpp_add<0x111, 0xf>(s[0]); s[1] = dpp_add<0x111, 0xf>(s[1]);
    const float mean = s[0] * (1.0f / DM), var = fmaxf(s[1] * (1.0f / DM) - mean * mean, 0.f);
    if (tid & 1) ldsst<f32x2>(lds0 + K3_RST + (tid >> 1) * 8, (f32x2){mean, __builtin_amdgcn_rsqf(var + 1e-5f)});
    float t = 0.f;
#pragma unroll
    for (int p = 0; p < 16; ++p) t += cs[p];
    if (tid < 224) ldsst<float>(lds0 + K3_CS + ((tid < 112 ? tid : tid + 16)) * 4, t); }
  K.run(acc, 1);
  ST0(3, 3);
  u32x2 z[2][7];
  fold_gelu(acc, z, lds0, 1, wid, lane);
  zero_acc<C3>(acc);
  { float s0 = 0.f, s1 = 0.f, q0 = 0.f, q1 = 0.f;
#pragma unroll
    for (int n = 0; n < 7; ++n) {
      s0 = dot2(z[0][n][1], ONES2, dot2(z[0][n][0], ONES2, s0)); q0 = dot2(z[0][n][1], z[0][n][1], dot2(z[0][n][0], z[0][n][0], q0));
      s1 = dot2(z[1][n][1], ONES2, dot2(z[1][n][0], ONES2, s1)); q1 = dot2(z[1][n][1], z[1][n][1], dot2(z[1][n][0], z[1][n][0], q1)); }
    asm volatile("s_nop 2" : "+v"(s0), "+v"(s1), "+v"(q0), "+v"(q1));
    sum4x4(s0, q0, s1, q1);
    const float s = g & 1 ? s1 : s0, q = g & 1 ? q1 : q0; const unsigned qb = __float_as_uint(q);
    if (g < 2) st8(c, WS_ZST + ((pm * 16 + pn) * 128 + wid * 32 + g * 16 + i16) * 8, (f32x2){s, __uint_as_float(qb == ZST_EMPTY ? 0x7fc00000u : qb)}); }
  ST0(3, 4);
  K.run(acc, 3);
  { f32x2 t[2];
    for (unsigned spins = 0;; ++spins) {
      f32x2 w[2][4]; bool ok = true;
#pragma unroll
      for (int m = 0; m < 2; ++m)
#pragma unroll
        for (int p = 0; p < 4; ++p) w[m][p] = ld8(c, WS_ZST + ((pm * 16 + 4 * g + p) * 128 + wid * 32 + m * 16 + i16) * 8);
#pragma unroll
      for (int m = 0; m < 2; ++m) {
#pragma unroll
        for (int p = 0; p < 4; ++p) ok &= __float_as_uint(w[m][p][1]) != ZST_EMPTY;
        t[m] = (w[m][0] + w[m][1]) + (w[m][2] + w[m][3]); }
      if (__all(ok) || spin_fail(spins, c.cnt(CNT_TMO))) break;
    }
    float a0 = t[0][0], a1 = t[0][1], a2 = t[1][0], a3 = t[1][1];
    sum4x4(a0, a1, a2, a3);
    const unsigned stg = lds0 + K3_STG + wid * (32 * Z_RS);
#pragma unroll
    for (int m = 0; m < 2; ++m) {
      const float mean = (m ? a2 : a0) * (1.0f / FH), var = fmaxf((m ? a3 : a1) * (1.0f / FH) - mean * mean, 0.f), rstd = __builtin_amdgcn_rsqf(var + 1e-5f), mr = -mean * rstd;
#pragma unroll
      for (int n = 0; n < 7; ++n)
        ldsst<u32x2>(stg + (m * 16 + i16) * Z_RS + n * 32 + g * 8,
                     (u32x2){cvtpk(bflo(z[m][n][0]) * rstd + mr, bfhi(z[m][n][0]) * rstd + mr), cvtpk(bflo(z[m][n][1]) * rstd + mr, bfhi(z[m][n][1]) * rstd + mr)});
    }
    waitl0();
    const unsigned rd = stg + (8 * g + (i16 >> 2)) * Z_RS + (lane & 3) * 8, z2 = WS_Z2 + (pn * 32 + 2 * pm + (wid >> 1)) * C3::B_BYTES;
#pragma unroll
    for (int j = 0; j < 7; ++j)
      st16(c, z2 + aimg(j * 16 + i16, (wid & 1) * 4 + g), __builtin_bit_cast(u32x4, join(trread(rd + j * 32), trread(rd + 4 * Z_RS + j * 32)))); }
  ST0(3, 5);
  K.run(acc, 3);
  waitv<0>();
  K.run(acc, 4);
  { const unsigned v = __builtin_amdgcn_raw_buffer_load_b32(c.rs, WS_CTL + (FLG_Z2 + pn) * 256 + (lane & 15) * 4, 0, AUX_SC1);
    FENCE();
    K.run(acc, 1);
    const unsigned seen = (unsigned)__ballot(v != 0u) & 0xffffu;
    if (wid == 0) ldsst<unsigned>(lds0 + K3_SEEN, seen); }
  K.run(acc, 5);
  ST0(3, 6);
  fold_gelu(acc, z, lds0, 0, wid, lane);
  zero_acc<C3>(acc);
  unsigned bs[2];
#pragma unroll
  for (int m = 0; m < 2; ++m) bs[m] = b_sp[pm * 128 + wid * 32 + m * 16 + i16];
  FENCE();
  ST0(3, 7);
  ST0(4, 0); ST0(4, 2);
  K.run(acc, 32);
  waitl0();
  ST0(4, 3);
  const unsigned stg = lds0 + (wid >> 1) * C5::STAGE + (wid & 1) * 32 * G_RS;
#pragma unroll
  for (int n = 0; n < 7; ++n)
#pragma unroll
    for (int m = 0; m < 2; ++m) { const unsigned a = z[m][n][0], b = z[m][n][1]; const f32x4 v = acc[n][m] + bflo(bs[m]);
      ldsst<u32x2>(stg + (m * 16 + i16) * G_RS + n * 32 + g * 8, (u32x2){cvtpk(bflo(a) * v[0], bfhi(a) * v[1]), cvtpk(bflo(b) * v[2], bfhi(b) * v[3])}); }
  waitl0();
  const unsigned gp = WS_G + pm * (28 * 16384);
#pragma unroll
  for (int i = 0; i < 7; ++i) { const unsigned ci = i * 64 + lane, r = ci / 14u, ch = ci - r * 14u, cc = pn * 14 + ch, row = wid * 32 + r;
    st16(c, gp + (cc >> 3) * 16384 + aimg(row, cc & 7), ldsld<u32x4>(stg + r * G_RS + ch * 16)); }
  ST0(4, 7);
}

struct Args { const u16 *x, *wqkv, *wphi, *wo, *w_in, *w_sp, *b_sp, *w_out; unsigned char* ws; u16* out; };
template<int P0, int P1> __global__ __launch_bounds__(512, 2) void k_main(Args a) {
  extern __shared__ __attribute__((aligned(16))) unsigned char shm[];
  Ctx c;
  c.lds0 = (unsigned)(size_t)(LAS unsigned char*)shm;
  c.tid = threadIdx.x; c.lane = c.tid & 63; c.wid = uni(c.tid >> 6);
  { const int b = blockIdx.x, xl = b & 7, j = b >> 3;
    c.pm = (xl & 3) * 4 + (j & 3); c.pn = (xl >> 2) * 8 + (j >> 2); }
  c.ws = a.ws; c.rs = make_rsrc(a.ws, (unsigned)WS_END);
  if constexpr (P0 <= 1 && 1 <= P1) phase1<2 <= P1>(c, a.wqkv, a.wphi, a.wo, a.w_in, a.w_out);
  if constexpr (P0 <= 1 && 2 <= P1) publish(c, FLG_O + c.pm, c.pn);
  if constexpr (P0 <= 2 && 2 <= P1) phase_res<C2, true, 2, P0 < 2 ? PRE2 : 0, 3 <= P1>(c, P0 < 2, (unsigned)WS_O, a.wo, DM * DM * 2, a.x, nullptr);
  if constexpr (P0 <= 2 && 3 <= P1) publish(c, FLG_H + c.pm, c.pn);
  if constexpr (P0 <= 3 && 3 <= P1) phase34<P0 < 3 ? PRE3 : 0, 4 <= P1>(c, P0 < 3, a.w_sp, a.b_sp, a.w_out);
  if constexpr (P0 <= 3 && 4 <= P1) publish(c, FLG_G + c.pm, c.pn);
  if constexpr (P0 <= 4 && 4 <= P1) phase_res<C5, false, 5, P0 < 4 ? PRE5 : 0, false>(c, P0 < 4, (unsigned)WS_G, a.w_out, FH * DM * 2, nullptr, a.out);
}
template<int P0, int P1> void launch_main(const Args& a, hipStream_t stream) {
  (void)hipFuncSetAttribute((const void*)k_main<P0, P1>, hipFuncAttributeMaxDynamicSharedMemorySize, LDS_TOTAL);
  k_main<P0, P1><<<256, 512, LDS_TOTAL, stream>>>(a);
}
}

extern "C" void kernel_launch(void* const* d_in, const int* in_sizes, int n_in, void* d_out, int out_size, void* d_ws, size_t ws_size, hipStream_t stream) {
  Args a;
  a.x = (const u16*)d_in[0]; a.wqkv = (const u16*)d_in[1]; a.wphi = (const u16*)d_in[2]; a.wo = (const u16*)d_in[3];
  a.w_in = (const u16*)d_in[5]; a.w_sp = (const u16*)d_in[6]; a.b_sp = (const u16*)d_in[7]; a.w_out = (const u16*)d_in[8];
  a.ws = (unsigned char*)d_ws; a.out = (u16*)d_out;
  (void)in_sizes; (void)n_in; (void)out_size; (void)ws_size;
  k0_prep<<<256, 256, 0, stream>>>(a.x, a.wqkv, a.ws);
  launch_main<1, 4>(a, stream);
}
```

```cpp
#include <hip/hip_runtime.h>
#include <hip/hip_bf16.h>
#include <utility>

#pragma clang diagnostic ignored "-Wint-to-pointer-cast"
#pragma clang diagnostic ignored "-Wc++20-extensions"
namespace {
typedef short s16x4 __attribute__((ext_vector_type(4)));
typedef short s16x8 __attribute__((ext_vector_type(8)));
typedef __bf16 bf16x8 __attribute__((ext_vector_type(8)));
typedef __bf16 bf16x2 __attribute__((ext_vector_type(2)));
typedef float f32x2 __attribute__((ext_vector_type(2)));
typedef float f32x4 __attribute__((ext_vector_type(4)));
typedef int i32x4 __attribute__((ext_vector_type(4)));
typedef unsigned u32x4 __attribute__((ext_vector_type(4)));
typedef unsigned u32x2 __attribute__((ext_vector_type(2)));
typedef unsigned short u16;
#define LAS __attribute__((address_space(3)))
#define GAS __attribute__((address_space(1)))
#define DI __device__ __forceinline__
#define FENCE() __builtin_amdgcn_sched_barrier(0)
#define RLX_AGENT __ATOMIC_RELAXED, __HIP_MEMORY_SCOPE_AGENT
template<int N> using iseq = std::make_integer_sequence<int, N>;

constexpr int SEQ = 2048, DM = 1024, FH = 1792, FF = 3584;
constexpr size_t WS_CTL = 0;
enum { CNT_KV = 0, FLG_O = 16, FLG_H = 32, FLG_Z2 = 48, FLG_G = 64, FLG_W = 80, CNT_TMO = 96, CNT_N = 128 };
constexpr size_t CTL_BYTES = CNT_N * 256;
constexpr size_t WS_XN = CTL_BYTES;
constexpr size_t WS_H = WS_XN;
constexpr size_t WS_G = WS_XN + (size_t)SEQ * DM * 2;
constexpr size_t WS_KVP = WS_G;
constexpr size_t WS_ZP = WS_KVP + (size_t)16 * 16 * 8192;
constexpr size_t WS_O = WS_ZP + 16 * 16 * 512;
constexpr size_t WS_HST = WS_G + (size_t)SEQ * FH * 2;
constexpr size_t WS_Z2 = WS_HST + 16 * 16 * 128 * 8;
constexpr size_t WS_ZST = WS_Z2 + (size_t)SEQ * FH * 2;
constexpr size_t WS_WIN = WS_ZST + 16 * 16 * 128 * 8;
constexpr size_t WS_CSP = WS_WIN + (size_t)DM * FF * 2;
constexpr size_t WS_END = WS_CSP + 16 * 16 * 224 * 4;
static_assert(WS_O + (size_t)SEQ * DM * 2 <= WS_HST, "aliases");

DI i32x4 make_srd(const void* p, unsigned bytes) {
  const unsigned long long a = (unsigned long long)p; i32x4 rs;
  rs[0] = __builtin_amdgcn_readfirstlane((int)(unsigned)a); rs[1] = __builtin_amdgcn_readfirstlane((int)(unsigned)(a >> 32)) & 0xffff;
  rs[2] = (int)bytes; rs[3] = 0x00020000; return rs;
}
DI __amdgpu_buffer_rsrc_t make_rsrc(const void* p, unsigned bytes) { return __builtin_amdgcn_make_buffer_rsrc(const_cast<void*>(p), 0, bytes, 0x00020000); }
DI int uni(int v) { return __builtin_amdgcn_readfirstlane(v); }
template<bool SC1> DI void dma16(unsigned ldsaddr, unsigned voff, i32x4 rs, unsigned soff) {
  if constexpr (SC1) asm volatile("s_mov_b32 m0, %0\n\ts_nop 0\n\tbuffer_load_dwordx4 %1, %2, %3 offen sc1 lds" :: "s"(ldsaddr), "v"(voff), "s"(rs), "s"(soff));
  else asm volatile("s_mov_b32 m0, %0\n\ts_nop 0\n\tbuffer_load_dwordx4 %1, %2, %3 offen lds" :: "s"(ldsaddr), "v"(voff), "s"(rs), "s"(soff));
}
DI void touch64(unsigned ldsdummy, unsigned voff, i32x4 rs) {
  asm volatile("s_mov_b32 m0, %0\n\ts_nop 0\n\tbuffer_load_dword %1, %2, 0 offen lds" :: "s"(ldsdummy), "v"(voff), "s"(rs));
}
template<int N> DI void waitv() { asm volatile("s_waitcnt vmcnt(%0)" :: "i"(N) : "memory"); }
DI void waitl0() { asm volatile("" ::: "memory"); __builtin_amdgcn_s_waitcnt(0xC07F); asm volatile("" ::: "memory"); }
DI void bar() { asm volatile("s_barrier" ::: "memory"); }
DI void lbar() { waitl0(); bar(); }
DI unsigned cvtpk(float lo, float hi) { f32x2 v = {lo, hi}; bf16x2 b = __builtin_convertvector(v, bf16x2); return __builtin_bit_cast(unsigned, b); }
DI float bflo(unsigned u) { return __uint_as_float(u << 16); }
DI float bfhi(unsigned u) { return __uint_as_float(u & 0xffff0000u); }
DI bf16x8 asbf(u32x4 v) { return __builtin_bit_cast(bf16x8, v); }
DI f32x4 mfma(bf16x8 a, bf16x8 b, f32x4 c) { return __builtin_amdgcn_mfma_f32_16x16x32_bf16(a, b, c, 0, 0, 0); }
DI s16x4 trread(unsigned addr) { return __builtin_amdgcn_ds_read_tr16_b64_v4i16((LAS s16x4*)addr); }
DI bf16x8 join(s16x4 lo, s16x4 hi) { return __builtin_bit_cast(bf16x8, __builtin_shufflevector(lo, hi, 0, 1, 2, 3, 4, 5, 6, 7)); }
template<class T> DI T ldsld(unsigned addr) { return *(const LAS T*)addr; }
template<class T> DI void ldsst(unsigned addr, T v) { *(LAS T*)addr = v; }
DI u32x4 pack8(const f32x4 a, const f32x4 b) { return (u32x4){cvtpk(a[0], a[1]), cvtpk(a[2], a[3]), cvtpk(b[0], b[1]), cvtpk(b[2], b[3])}; }
DI float dot2(unsigned a, unsigned b, float c) { return __builtin_amdgcn_fdot2_f32_bf16(__builtin_bit_cast(bf16x2, a), __builtin_bit_cast(bf16x2, b), c, false); }
constexpr unsigned ONES2 = 0x3f803f80u;
#define RED4X4(OP) \
  "s_nop 1\n\tv_mov_b32 %4, %0\n\tv_mov_b32 %5, %1\n\tv_mov_b32 %6, %2\n\tv_mov_b32 %7, %3\n\t" \
  "v_permlane16_swap_b32 %0, %4\n\tv_permlane16_swap_b32 %1, %5\n\tv_permlane16_swap_b32 %2, %6\n\tv_permlane16_swap_b32 %3, %7\n\t" \
  OP " %0, %0, %4\n\t" OP " %1, %1, %5\n\t" OP " %2, %2, %6\n\t" OP " %3, %3, %7\n\t" \
  "v_mov_b32 %4, %0\n\tv_mov_b32 %5, %1\n\tv_mov_b32 %6, %2\n\tv_mov_b32 %7, %3\n\ts_nop 0\n\t" \
  "v_permlane32_swap_b32 %0, %4\n\tv_permlane32_swap_b32 %1, %5\n\tv_permlane32_swap_b32 %2, %6\n\tv_permlane32_swap_b32 %3, %7\n\t" \
  OP " %0, %0, %4\n\t" OP " %1, %1, %5\n\t" OP " %2, %2, %6\n\t" OP " %3, %3, %7"
DI void max4x4(float& a, float& b, float& c, float& d) { float t0, t1, t2, t3;
  asm(RED4X4("v_max_f32") : "+v"(a), "+v"(b), "+v"(c), "+v"(d), "=&v"(t0), "=&v"(t1), "=&v"(t2), "=&v"(t3)); }
DI void sum4x4(float& a, float& b, float& c, float& d) { float t0, t1, t2, t3;
  asm(RED4X4("v_add_f32") : "+v"(a), "+v"(b), "+v"(c), "+v"(d), "=&v"(t0), "=&v"(t1), "=&v"(t2), "=&v"(t3)); }
DI float sum4(float x) {
  const unsigned u = __float_as_uint(x);
  auto a = __builtin_amdgcn_permlane16_swap(u, u, false, false);
  const unsigned v = __float_as_uint(__uint_as_float(a[0]) + __uint_as_float(a[1]));
  auto b = __builtin_amdgcn_permlane32_swap(v, v, false, false);
  return __uint_as_float(b[0]) + __uint_as_float(b[1]);
}
template<int CTRL, int ROWMASK> DI float dpp_add(float x) {
  return x + __int_as_float(__builtin_amdgcn_update_dpp(0, __float_as_int(x), CTRL, ROWMASK, 0xf, true));
}
DI float wave_sum(float x) {
  x = dpp_add<0x111, 0xf>(x); x = dpp_add<0x112, 0xf>(x); x = dpp_add<0x114, 0xf>(x); x = dpp_add<0x118, 0xf>(x);
  x = dpp_add<0x142, 0xa>(x); x = dpp_add<0x143, 0xc>(x);
  return __int_as_float(__builtin_amdgcn_readlane(__float_as_int(x), 63));
}

DI bool spin_fail(unsigned spins, GAS unsigned* tmo) {
  if (spins > (1u << 16)) { __hip_atomic_store(tmo, 1u, RLX_AGENT); return true; }
  if ((spins & 255u) == 255u && uni(__hip_atomic_load(tmo, RLX_AGENT))) return true;
  __builtin_amdgcn_s_sleep(1);
  return false;
}
DI void wait_count(GAS unsigned* cnt, unsigned want, GAS unsigned* tmo) {
  for (unsigned spins = 0;; ++spins) {
    if ((unsigned)uni(__hip_atomic_load(cnt, RLX_AGENT)) >= want) break;
    if (spin_fail(spins, tmo)) break;
  }
}

template<int SW> __host__ __device__ constexpr int bmask(int pos) { return SW == 64 ? ((pos >> 1) & 3) : 0; }
__host__ __device__ constexpr int kswap(int k) { return (k & ~12) | ((k & 4) << 1) | ((k & 8) >> 1); }
DI unsigned aimg(int r, int c) { return r * 128 + ((c ^ ((r >> 1) & 7)) * 16); }

template<int SW_, int NS_, int WM_, int WN_, int ST_, int NKT_, bool BT_ = false> struct Cfg {
  static constexpr int BM = 128, SW = SW_, NS = NS_, BN = SW_ * NS_, WM = WM_, WN = WN_, ST = ST_, NKT = NKT_;
  static constexpr bool BT = BT_;
  static constexpr int MT = BM / WM / 16, NT = BN / WN / 16;
  static constexpr int A_BYTES = BM * 128, ROWB = SW * 2, STRIP_BYTES = 64 * ROWB, B_BYTES = NS * STRIP_BYTES, STAGE = A_BYTES + B_BYTES, RING = ST * STAGE;
  static constexpr int PB = B_BYTES / 1024;
  static constexpr int IA = 4, IBMAX = (PB + 3) / 4, IBMIN = PB / 4, NBIG = PB - IBMIN * 4;
  static constexpr int TPS = SW / 16 / WN;
  static constexpr bool SWZ = (SW == 64);
  static constexpr int NV = SWZ ? TPS : 1;
  static constexpr int NM = MT * NT, NR = BT ? MT + NT : MT + 2 * NT;
  static_assert(WM * WN == 4 && STRIP_BYTES % 1024 == 0 && NT == TPS * NS && ST >= 3 && NKT >= 2 * ST && 2 * NM >= NR, "config");
  static_assert((ST - 1) * (IA + IBMAX) <= 63, "vmcnt range");
};
template<class C> struct Frags { bf16x8 a[C::MT]; s16x4 bl[C::NT], bh[C::NT]; bf16x8 b[C::NT]; };

template<class C, int I> DI void read_item(Frags<C>& f, unsigned aA, const unsigned (&aB)[C::NV], int ksimm) {
  if constexpr (C::BT) {
    if constexpr (I >= 1 && I < 1 + C::MT) f.a[I - 1] = ldsld<bf16x8>(aA + (I - 1) * 2048);
    else { constexpr int j = I == 0 ? 0 : I - C::MT; f.b[j] = ldsld<bf16x8>(aB[0] + j * 2048); }
  } else if constexpr (I >= 2 && I < 2 + C::MT) {
    constexpr int m = I - 2;
    f.a[m] = ldsld<bf16x8>(aA + m * 2048);
  } else {
    constexpr int r = I < 2 ? I : I - C::MT, j = r >> 1, u = r & 1;
    constexpr int strip = j / C::TPS, tj = j % C::TPS;
    constexpr int imm = strip * C::STRIP_BYTES + u * 8 * C::ROWB + (C::SWZ ? 0 : tj * 32);
    const s16x4 v = trread(aB[C::SWZ ? tj : 0] + imm + ksimm);
    if constexpr (u == 0) f.bl[j] = v; else f.bh[j] = v;
  }
}
template<class C, int I> DI void mfma_item(f32x4 (&acc)[C::NT][C::MT], const Frags<C>& f) {
  constexpr int n = I / C::MT, m = I % C::MT;
  if constexpr (C::BT) acc[n][m] = mfma(f.b[n], f.a[m], acc[n][m]); else acc[n][m] = mfma(join(f.bl[n], f.bh[n]), f.a[m], acc[n][m]);
}
template<class C, int... Is>
DI void step(f32x4 (&acc)[C::NT][C::MT], const Frags<C>& cur, Frags<C>& nxt, unsigned aA, const unsigned (&aB)[C::NV], int ksimm, std::integer_sequence<int, Is...>) {
  ((mfma_item<C, Is>(acc, cur), FENCE(),
    [&] { if constexpr (C::NR <= C::NM) { if constexpr (Is < C::NR) read_item<C, Is>(nxt, aA, aB, ksimm); }
          else { if constexpr (2 * Is < C::NR) read_item<C, 2 * Is>(nxt, aA, aB, ksimm); if constexpr (2 * Is + 1 < C::NR) read_item<C, 2 * Is + 1>(nxt, aA, aB, ksimm); } }(),
    FENCE()), ...);
}
template<class C, int... Is> DI void read_all(Frags<C>& f, unsigned aA, const unsigned (&aB)[C::NV], std::integer_sequence<int, Is...>) { (read_item<C, Is>(f, aA, aB, 0), ...); }
template<class C> DI void zero_acc(f32x4 (&acc)[C::NT][C::MT]) {
#pragma unroll
  for (int n = 0; n < C::NT; ++n)
#pragma unroll
    for (int m = 0; m < C::MT; ++m) { acc[n][m] = (f32x4){0.f, 0.f, 0.f, 0.f}; asm volatile("" : "+v"(acc[n][m])); }
}

template<class C> struct Consumer {
  unsigned aA, aB[C::NV]; int slot; Frags<C> F0, F1;
  DI void init(unsigned lds0, int wid, int lane) {
    const int wc = wid % C::WN, wr = wid / C::WN, g = lane >> 4, i16 = lane & 15, q = i16 >> 2, p = lane & 3;
    const int row = wr * (C::BM / C::WM) + i16;
    aA = lds0 + aimg(row, g);
    const int pos = (g >> 1) * 16 + (g & 1) * 4 + q;
    const unsigned base = lds0 + C::A_BYTES + pos * C::ROWB + (p >> 1) * 16 + (p & 1) * 8;
#pragma unroll
    for (int v = 0; v < C::NV; ++v) aB[v] = base + (C::SWZ ? (((wc * C::TPS + v) ^ bmask<C::SW>(pos)) * 32) : wc * C::TPS * 32);
    if constexpr (C::BT) aB[0] = lds0 + C::A_BYTES + aimg(wc * C::TPS * 16 + i16, g);
    slot = 0;
  }
  DI void start() { lbar(); read_all<C>(F0, aA, aB, iseq<C::NR>{}); FENCE(); }
  DI void tile(f32x4 (&acc)[C::NT][C::MT]) {
    unsigned b[C::NV];
    unsigned o = slot * C::STAGE;
#pragma unroll
    for (int v = 0; v < C::NV; ++v) b[v] = aB[v] + o;
    if constexpr (C::BT) b[0] = (aB[0] ^ 64) + o;
    step<C>(acc, F0, F1, (aA ^ 64) + o, b, 32 * C::ROWB, iseq<C::NM>{});
    slot = slot + 1 == C::ST ? 0 : slot + 1;
    lbar();
    o = slot * C::STAGE;
#pragma unroll
    for (int v = 0; v < C::NV; ++v) b[v] = aB[v] + o;
    step<C>(acc, F1, F0, aA + o, b, 0, iseq<C::NM>{});
  }
  DI void run(f32x4 (&acc)[C::NT][C::MT], int tiles) {
#pragma unroll 1
    for (int t = 0; t < tiles; ++t) tile(acc);
  }
};

struct Dep {
  __amdgpu_buffer_rsrc_t rs; unsigned flagoff; GAS unsigned* tmo; unsigned seen;
  DI void ensure(unsigned n, int lane) {
    for (unsigned spins = 0; (seen & n) != n; ++spins) {
      seen |= (unsigned)__ballot(__builtin_amdgcn_raw_buffer_load_b32(rs, flagoff + (lane & 15) * 4, 0, 16) != 0u) & 0xffffu;
      if ((seen & n) == n) break;
      if (spin_fail(spins, tmo)) seen = 0xffffu;
    }
  }
};
template<class C> struct Pieces {
  int lw; unsigned lds0;
  template<bool SC1, int... Is> DI void a_img(int slot, unsigned v, i32x4 rs, unsigned off, std::integer_sequence<int, Is...>) const {
    (dma16<SC1>(lds0 + slot * C::STAGE + (lw + 4 * Is) * 1024, v, rs, off + (lw + 4 * Is) * 1024), ...); }
  template<int... Is> DI void a_rows(int slot, unsigned v, i32x4 rs, unsigned off, unsigned lda, std::integer_sequence<int, Is...>) const {
    (dma16<false>(lds0 + slot * C::STAGE + (lw + 4 * Is) * 1024, v, rs, off + (lw + 4 * Is) * 8 * lda), ...); }
  template<int... Is> DI void b_img(int slot, unsigned v, i32x4 rs, unsigned off, std::integer_sequence<int, Is...>) const {
    ([&] { if (Is < C::IBMIN || lw < C::NBIG) dma16<true>(lds0 + slot * C::STAGE + C::A_BYTES + (lw + 4 * Is) * 1024, v, rs, off + (lw + 4 * Is) * 1024); }(), ...); }
  template<int... Is> DI void b_w64(int slot, unsigned v, i32x4 rs, unsigned off, unsigned ldb, unsigned stripbytes, std::integer_sequence<int, Is...>) const {
    ([&] { constexpr int strip = Is / 2; const int pp = lw + (Is & 1) * 4;
           dma16<false>(lds0 + slot * C::STAGE + C::A_BYTES + strip * 8192 + pp * 1024, v, rs, off + strip * stripbytes + (16 * (pp >> 1) + 4 * (pp & 1)) * ldb); }(), ...); }
  template<int NA, int NB> DI void wait() const {
    if constexpr (C::NBIG == 0) waitv<NA * C::IA + NB * C::IBMAX>();
    else { if (lw < C::NBIG) waitv<NA * C::IA + NB * C::IBMAX>(); else waitv<NA * C::IA + NB * C::IBMIN>(); }
  }
};
DI unsigned voff_rows(int lw, int lane, unsigned lda) { return (lane >> 3) * lda + (((lane & 7) ^ (((lw & 1) << 2) | (lane >> 4))) * 16); }
DI unsigned voff_w64(int lane, unsigned ldb) { return (8 * (lane >> 5) + ((lane >> 3) & 3)) * ldb + (((lane & 7) ^ (((lane >> 4) & 3) << 1)) * 16); }

template<class C, class F> DI void tile_loop(const Pieces<C>& P, int& slot, int t0, int t1, F issue) {
#pragma unroll 1
  for (int t = t0; t <= t1; ++t) {
    P.template wait<C::ST - 2, C::ST - 2>(); lbar();
    issue(t - 1 + C::ST, slot);
    slot = slot + 1 == C::ST ? 0 : slot + 1;
  }
}
template<class C, int NPRE, class Src> DI void producer_loop(const Pieces<C>& P, Src& s) {
  constexpr int ST = C::ST, NKT = C::NKT;
  if constexpr (!Src::DEP) {
    s.dep_half(0, 0); s.w_half(0, 0); s.dep_half(1, 1); s.w_half(1, 1);
    P.template wait<1, 1>(); bar();
#pragma unroll
    for (int i = 2; i < ST; ++i) { s.dep_half(i, i); s.w_half(i, i); }
  } else {
    s.dep_half(0, 0);
#pragma unroll
    for (int i = NPRE; i < ST; ++i) s.w_half(i, i);
    P.template wait<0, (NPRE > 0 ? ST - NPRE : ST - 1)>(); bar();
#pragma unroll
    for (int i = 1; i < ST; ++i) s.dep_half(i, i);
  }
  int slot = 0;
  [&]<int... Is>(std::integer_sequence<int, Is...>) {
    (([&] { constexpr int t = Is + 1;
            P.template wait<ST - 2, Src::DEP ? t - 1 : ST - 2>();
            lbar(); s.dep_half(t - 1 + ST, slot); s.w_half(t - 1 + ST, slot); slot = slot + 1 == ST ? 0 : slot + 1; }()), ...);
  }(iseq<ST - 1>{});
  s.steady(slot);
  [&]<int... Is>(std::integer_sequence<int, Is...>) {
    (([&] { constexpr int left = ST - 2 - Is > 0 ? ST - 2 - Is : 0; P.template wait<left, left>(); lbar(); }()), ...);
  }(iseq<ST>{});
}
template<int NLINES, class F> DI void prewarm(unsigned ldsdummy, int lw, int lane, i32x4 rs, F off) {
#pragma unroll
  for (int i = 0; i < (NLINES + 255) / 256; ++i) {
    const unsigned idx = (i * 4 + lw) * 64 + lane;
    if ((i * 4 + 4) * 64 <= NLINES || (i * 4 + lw) * 64 < NLINES) touch64(ldsdummy, off(idx < NLINES ? idx : NLINES - 1), rs);
  }
}

constexpr unsigned MAIN_CODE_LINES = 384;
__global__ __launch_bounds__(512) void k0_prep(const u16* __restrict__ x, const u16* __restrict__ wqkv, unsigned char* __restrict__ ws) {
  const int tid = threadIdx.x, lane = tid & 63, wid = tid >> 6, b = blockIdx.x;
  if (tid < 8) *(u32x4*)(ws + WS_CTL + b * 128 + tid * 16) = (u32x4){0u, 0u, 0u, 0u};
  static_assert(CTL_BYTES == 256 * 128, "control block");
  u32x4 v[2];
#pragma unroll
  for (int c = 0; c < 2; ++c) v[c] = *(const u32x4*)(x + (size_t)(b * 8 + wid) * DM + c * 512 + lane * 8);
  unsigned pf = 0;
  if (wid == 7) {
    unsigned lo, hi;
    asm("s_getpc_b64 vcc\n\ts_add_u32 %0, vcc_lo, hedgehog_gmlp_main@rel32@lo+4\n\ts_addc_u32 %1, vcc_hi, hedgehog_gmlp_main@rel32@hi+12" : "=s"(lo), "=s"(hi) :: "vcc", "scc");
    const unsigned long long code = ((unsigned long long)hi << 32) | lo;
    const unsigned i = (b >> 3) * 16 + (lane & 15);
    if (lane < 16 && i < MAIN_CODE_LINES) pf = *(const GAS unsigned*)(code + i * 128);
  }
  if (tid < 192) pf = *(const unsigned*)((const char*)wqkv + (size_t)(b * 192 + tid) * 128);
  float f[16], s = 0;
#pragma unroll
  for (int c = 0; c < 2; ++c)
#pragma unroll
    for (int j = 0; j < 4; ++j) { f[c * 8 + 2 * j] = bflo(v[c][j]); f[c * 8 + 2 * j + 1] = bfhi(v[c][j]); }
#pragma unroll
  for (int j = 0; j < 16; ++j) s += f[j];
  const float mean = wave_sum(s) * (1.0f / DM); float q = 0;
#pragma unroll
  for (int j = 0; j < 16; ++j) { f[j] -= mean; q += f[j] * f[j]; }
  const float rstd = __builtin_amdgcn_rsqf(wave_sum(q) * (1.0f / DM) + 1e-5f);
  const int row = (b & 15) * 8 + wid;
#pragma unroll
  for (int c = 0; c < 2; ++c) { u32x4 o;
#pragma unroll
    for (int j = 0; j < 4; ++j) o[j] = cvtpk(f[c * 8 + 2 * j] * rstd, f[c * 8 + 2 * j + 1] * rstd);
    __builtin_amdgcn_raw_buffer_store_b128(o, make_rsrc(ws, (unsigned)WS_END), (unsigned)WS_XN + ((b >> 4) * 16 + c * 8 + (lane >> 3)) * 16384 + aimg(row, lane & 7), 0, 16); }
  asm volatile("" :: "v"(pf));
}

struct Ctx {
  unsigned lds0; int tid, lane, wid, pm, pn;
  unsigned char* ws; __amdgpu_buffer_rsrc_t rs;
  DI GAS unsigned* cnt(int i) const { return (GAS unsigned*)(ws + WS_CTL) + i * 64; }
  DI i32x4 ws_srd() const { return make_srd(ws, (unsigned)WS_END); }
  DI Dep dep(int line, int own) const { return Dep{rs, (unsigned)WS_CTL + line * 256, cnt(CNT_TMO), 1u << own}; }
};
constexpr int AUX_SC1 = 16;
constexpr unsigned ZST_EMPTY = 0xFFFFFFFFu;
DI void st16(const Ctx& c, unsigned off, u32x4 v) { __builtin_amdgcn_raw_buffer_store_b128(v, c.rs, off, 0, AUX_SC1); }
DI void st8(const Ctx& c, unsigned off, f32x2 v) { __builtin_amdgcn_raw_buffer_store_b64(__builtin_bit_cast(u32x2, v), c.rs, off, 0, AUX_SC1); }
DI void st4(const Ctx& c, unsigned off, unsigned v) { __builtin_amdgcn_raw_buffer_store_b32(v, c.rs, off, 0, AUX_SC1); }
DI u32x4 ld16(const Ctx& c, unsigned off) { return __builtin_amdgcn_raw_buffer_load_b128(c.rs, off, 0, AUX_SC1); }
DI f32x2 ld8(const Ctx& c, unsigned off) { return __builtin_bit_cast(f32x2, __builtin_amdgcn_raw_buffer_load_b64(c.rs, off, 0, AUX_SC1)); }
DI float ld4(const Ctx& c, unsigned off) { return __uint_as_float(__builtin_amdgcn_raw_buffer_load_b32(c.rs, off, 0, AUX_SC1)); }
DI void set_flag(const Ctx& c, int line, int word) { if (c.wid == 4 && c.lane == 0) st4(c, WS_CTL + line * 256 + word * 4, 1u); }
DI void publish(const Ctx& c, int line, int word) { waitl0(); waitv<0>(); bar(); set_flag(c, line, word); }

using C1 = Cfg<64, 3, 2, 2, 3, 16>;
using C2 = Cfg<64, 1, 2, 2, 4, 16>;
using C3 = Cfg<112, 1, 4, 1, 4, 64, true>;
using C5 = Cfg<64, 1, 2, 2, 4, 28>;
constexpr int PRE2 = 4, PRE3 = 2, PRE5 = 4;
constexpr int LDS_DUMMY = 155648, LDS_CS4 = LDS_DUMMY + 256, LDS_TOTAL = LDS_CS4 + 2048;

template<class C, bool DEP_, int LDB, class Need> struct SrcW64 {
  static constexpr bool DEP = DEP_;
  Pieces<C> P; int lane, kt0; i32x4 rsA, rsB; unsigned a0, b0, strip, va, vb; Dep dep; Need need;
  DI SrcW64(const Pieces<C>& P_, int lane_, int kt0_, i32x4 rsA_, unsigned a0_, i32x4 rsB_, unsigned b0_, unsigned strip_, Dep dep_, Need need_)
    : P(P_), lane(lane_), kt0(uni(kt0_)), rsA(rsA_), rsB(rsB_), a0(uni(a0_)), b0(uni(b0_)), strip(strip_), va(lane_ * 16), vb(voff_w64(lane_, LDB)), dep(dep_), need(need_) {}
  DI int kt(int i) const { const int k = kt0 + i; return k >= C::NKT ? k - C::NKT : k; }
  DI void dep_half(int i, int slot) { const int k = kt(i); if (DEP && i > 0) dep.ensure(need(k), lane); P.template a_img<DEP>(slot, va, rsA, a0 + k * 16384, iseq<C::IA>{}); }
  DI void w_half(int i, int slot) const { P.b_w64(slot, vb, rsB, b0 + kt(i) * (64 * LDB), LDB, strip, iseq<C::IBMAX>{}); }
  DI void steady(int& slot) { tile_loop<C>(P, slot, C::ST, C::NKT - C::ST, [&](int i, int sl) { dep_half(i, sl); w_half(i, sl); }); }
};
template<class C, int NPRE, int LDB> DI void prefetch_w64(unsigned lds0, int lw, int lane, const u16* W, unsigned bytes, unsigned b0, int kt0) {
  const Pieces<C> P{lw, lds0}; const i32x4 rs = make_srd(W, bytes); const unsigned vb = voff_w64(lane, LDB);
  b0 = uni(b0); kt0 = uni(kt0);
#pragma unroll
  for (int s = 0; s < NPRE; ++s) { const int k = kt0 + s >= C::NKT ? kt0 + s - C::NKT : kt0 + s; P.b_w64(s, vb, rs, b0 + k * (64 * LDB), LDB, 0, iseq<C::IBMAX>{}); }
}
DI int kt0_5(int pn) { return (7 * pn + 3) >> 2; }
DI unsigned win_off(int pn, int half, int kt) { return (unsigned)WS_WIN + ((pn * 2 + half) * 16 + kt) * C3::B_BYTES; }

constexpr int K1_WPHI = C1::RING;
constexpr int K1_RW = K1_WPHI + 4096;
constexpr int K1_CSP = K1_RW + 2 * C3::B_BYTES;
constexpr int K1_QX = 0, K1_KX = 16384;
constexpr int K1_PHI = 32768, PHI_RS = 136, PHI_HEAD = 128 * PHI_RS;
constexpr int K1_V = K1_PHI + 2 * PHI_HEAD, V_RS = 72, V_HEAD = 128 * V_RS;
constexpr int K1_CS4 = LDS_CS4;
constexpr int K1_KVT = C2::RING;
constexpr int K1_ZT = K1_KVT + 8192;
constexpr int K1_OST = K1_ZT + 256;
static_assert(K1_CSP <= LDS_DUMMY && K1_V + 2 * V_HEAD <= K1_KVT && K1_OST + 16384 <= LDS_DUMMY && K1_OST % 16 == 0 && K1_V % 8 == 0, "P1 map");

DI void win_fetch(const Ctx& c, const u16* w_in, int lw) {
  const i32x4 rs = make_srd(w_in, DM * FF * 2);
  const unsigned off = uni((64 * c.pm + 32 * (lw & 1)) * (FF * 2) + ((lw >> 1) * FH + c.pn * 112) * 2);
#pragma unroll
  for (int i = 0; i < 7; ++i) { const unsigned lin = i * 64 + c.lane, pos = lin / 14u, ch = lin - pos * 14u;
    dma16<false>(c.lds0 + K1_RW + lw * 7168 + i * 1024, kswap(pos) * (FF * 2) + ch * 16, rs, off); }
}
template<int J0, int J1> DI void win_put(const Ctx& c, int lw) {
  const int lane = c.lane, g = lane >> 4, i16 = lane & 15;
  const unsigned rd = c.lds0 + K1_RW + (lw >> 1) * C3::B_BYTES + ((lw & 1) * 32 + (g >> 1) * 16 + (g & 1) * 4 + (i16 >> 2)) * 224 + (lane & 3) * 8;
  u32x4 v[J1 - J0];
#pragma unroll
  for (int j = J0; j < J1; ++j) v[j - J0] = __builtin_bit_cast(u32x4, join(trread(rd + j * 32), trread(rd + 8 * 224 + j * 32)));
#pragma unroll
  for (int j = J0; j < J1; ++j) st16(c, win_off(c.pn, lw >> 1, c.pm) + aimg(j * 16 + i16, (lw & 1) * 4 + g), v[j - J0]);
#pragma unroll
  for (int j = J0; j < J1; ++j) { const u32x4 w = v[j - J0]; const float s = sum4(dot2(w[3], ONES2, dot2(w[2], ONES2, dot2(w[1], ONES2, dot2(w[0], ONES2, 0.f)))));
    if (lane < 16) ldsst<float>(c.lds0 + K1_CS4 + lw * 512 + (j * 16 + lane) * 4, s); }
}

DI float max3(float a, float b, float c) { float r; asm("v_max3_f32 %0, %1, %2, %3" : "=v"(r) : "v"(a), "v"(b), "v"(c)); return r; }
DI float nmax3(float a, float b, float c) { float r; asm("v_max3_f32 %0, -%1, -%2, -%3" : "=v"(r) : "v"(a), "v"(b), "v"(c)); return r; }
DI void feature_maps(const bf16x8 wf0, const bf16x8 wf1, const bf16x8 (&fr)[2], u32x4 (&pos)[2], u32x4 (&neg)[2]) {
  constexpr float L2E = 1.4426950408889634f;
  const f32x4 z4 = {0.f, 0.f, 0.f, 0.f};
  f32x4 pa0 = mfma(wf0, fr[0], z4), pa1 = mfma(wf1, fr[0], z4), pb0 = mfma(wf0, fr[1], z4), pb1 = mfma(wf1, fr[1], z4);
  asm volatile("s_nop 7" : "+v"(pa0), "+v"(pa1), "+v"(pb0), "+v"(pb1));
  const f32x4 p[2][2] = {{pa0, pa1}, {pb0, pb1}};
  float mx[2], nm[2];
#pragma unroll
  for (int i = 0; i < 2; ++i) {
    mx[i] = max3(max3(p[i][0][0], p[i][0][1], p[i][0][2]), max3(p[i][0][3], p[i][1][0], p[i][1][1]), max3(p[i][1][2], p[i][1][3], p[i][1][3]));
    nm[i] = max3(nmax3(p[i][0][0], p[i][0][1], p[i][0][2]), nmax3(p[i][0][3], p[i][1][0], p[i][1][1]), nmax3(p[i][1][2], p[i][1][3], p[i][1][3]));
  }
  max4x4(mx[0], nm[0], mx[1], nm[1]);
  float ep[2][8], en[2][8], sp[2] = {0.f, 0.f}, sn[2] = {0.f, 0.f};
#pragma unroll
  for (int i = 0; i < 2; ++i) {
    const float cx = -mx[i] * L2E, cn = -nm[i] * L2E;
#pragma unroll
    for (int j = 0; j < 8; ++j) { const float xv = p[i][j >> 2][j & 3];
      ep[i][j] = __builtin_amdgcn_exp2f(__builtin_fmaf(xv, L2E, cx)); en[i][j] = __builtin_amdgcn_exp2f(__builtin_fmaf(xv, -L2E, cn)); sp[i] += ep[i][j]; sn[i] += en[i][j]; }
  }
  sum4x4(sp[0], sn[0], sp[1], sn[1]);
#pragma unroll
  for (int i = 0; i < 2; ++i) {
    const float rp = __builtin_amdgcn_rcpf(sp[i]), rn = __builtin_amdgcn_rcpf(sn[i]);
#pragma unroll
    for (int j = 0; j < 4; ++j) { pos[i][j] = cvtpk(ep[i][2 * j] * rp, ep[i][2 * j + 1] * rp); neg[i][j] = cvtpk(en[i][2 * j] * rn, en[i][2 * j + 1] * rn); }
  }
}

DI void phase1(const Ctx& c, const u16* wqkv, const u16* wphi, const u16* wo, const u16* w_in, const u16* w_out) {
  const unsigned lds0 = c.lds0; const int tid = c.tid, lane = c.lane, wid = c.wid, pm = c.pm, pn = c.pn;
  const int g = lane >> 4, i16 = lane & 15, q4 = i16 >> 2, p4 = lane & 3;
  const int w4 = wid & 3, wr = w4 >> 1, wc = w4 & 1, mp = wid >> 2;
  if (wid == 3) st16(c, WS_ZST + (pm * 16 + pn) * 1024 + lane * 16, (u32x4){ZST_EMPTY, ZST_EMPTY, ZST_EMPTY, ZST_EMPTY});
  if (wid < 4) {
    const u32x4 wp = *(const u32x4*)(wphi + (size_t)pn * 2048 + tid * 8);
    f32x4 acc[C1::NT][C1::MT];
    Consumer<C1> K; K.init(lds0, wid, lane); zero_acc<C1>(acc);
    K.start();
    K.run(acc, C1::NKT);
    ldsst<u32x4>(lds0 + K1_WPHI + tid * 16, wp);
#pragma unroll
    for (int m = 0; m < 4; ++m) {
      ldsst<u32x4>(lds0 + K1_KX + (w4 * 4 + m) * 1024 + lane * 16, pack8(acc[2][m], acc[3][m]));
      const u32x4 vv = pack8(acc[4][m], acc[5][m]);
      const unsigned va = lds0 + K1_V + wc * V_HEAD + (wr * 64 + m * 16 + i16) * V_RS + g * 8;
      ldsst<u32x2>(va, (u32x2){vv[0], vv[1]}); ldsst<u32x2>(va + 32, (u32x2){vv[2], vv[3]});
      ldsst<u32x4>(lds0 + K1_QX + (w4 * 4 + m) * 1024 + lane * 16, pack8(acc[0][m], acc[1][m]));
    }
  } else {
    const Pieces<C1> P{w4, lds0};
    auto none = [](int) { return 0u; };
    SrcW64<C1, false, 3 * DM * 2, decltype(none)> src(P, lane, 0, c.ws_srd(), (unsigned)WS_XN + pm * (16 * 16384), make_srd(wqkv, DM * 3 * DM * 2), (unsigned)pn * 128,
                                                      (unsigned)DM * 2, c.dep(0, 0), none);
    producer_loop<C1, 0>(P, src);
    win_fetch(c, w_in, w4);
    const unsigned b = blockIdx.x;
    prewarm<64>(lds0 + LDS_DUMMY, w4, lane, make_srd(wo, DM * DM * 2), [&](unsigned i) { return (b * 64 + i) * 128; });
    prewarm<112>(lds0 + LDS_DUMMY, 3 - w4, lane, make_srd(w_out, FH * DM * 2), [&](unsigned i) { return (b * 112 + i) * 128; });
  }
  lbar();
  const unsigned wph = lds0 + K1_WPHI + wc * 2048 + (4 * g + q4) * 64 + p4 * 8;
  const bf16x8 wf0 = join(trread(wph), trread(wph + 1024)), wf1 = join(trread(wph + 32), trread(wph + 1024 + 32));
  { bf16x8 kf[2]; u32x4 pos[2], neg[2];
#pragma unroll
    for (int i = 0; i < 2; ++i) kf[i] = ldsld<bf16x8>(lds0 + K1_KX + (w4 * 4 + 2 * mp + i) * 1024 + lane * 16);
    FENCE();
    feature_maps(wf0, wf1, kf, pos, neg);
#pragma unroll
    for (int i = 0; i < 2; ++i) {
      const unsigned pa = lds0 + K1_PHI + wc * PHI_HEAD + (wr * 64 + (2 * mp + i) * 16 + i16) * PHI_RS + g * 8;
      ldsst<u32x2>(pa, (u32x2){pos[i][0], pos[i][1]}); ldsst<u32x2>(pa + 32, (u32x2){pos[i][2], pos[i][3]});
      ldsst<u32x2>(pa + 64, (u32x2){neg[i][0], neg[i][1]}); ldsst<u32x2>(pa + 96, (u32x2){neg[i][2], neg[i][3]});
    } }
  lbar();
  { f32x4 d[2] = {{0.f, 0.f, 0.f, 0.f}, {0.f, 0.f, 0.f, 0.f}}, dz = {0.f, 0.f, 0.f, 0.f};
    const bf16x8 ones = asbf((u32x4){ONES2, ONES2, ONES2, ONES2});
    const unsigned pr = lds0 + K1_PHI + wc * PHI_HEAD + (4 * g + q4) * PHI_RS + wr * 64 + p4 * 8;
    const unsigned vr = lds0 + K1_V + wc * V_HEAD + (4 * g + q4) * V_RS + mp * 32 + p4 * 8;
    bf16x8 pf[4][2], vf[4];
#pragma unroll
    for (int s = 0; s < 4; ++s) {
#pragma unroll
      for (int a = 0; a < 2; ++a) pf[s][a] = join(trread(pr + s * 32 * PHI_RS + a * 32), trread(pr + (s * 32 + 16) * PHI_RS + a * 32));
      vf[s] = join(trread(vr + s * 32 * V_RS), trread(vr + (s * 32 + 16) * V_RS)); }
#pragma unroll
    for (int s = 0; s < 4; ++s) { d[0] = mfma(pf[s][0], vf[s], d[0]); d[1] = mfma(pf[s][1], vf[s], d[1]); dz = mfma(mp ? pf[s][1] : pf[s][0], ones, dz); }
    st16(c, WS_KVP + ((pn * 16 + pm) * 2 + wc) * 4096 + (wr * 2 + mp) * 1024 + lane * 16, pack8(d[0], d[1]));
    if (i16 == 0) st16(c, WS_ZP + ((pn * 16 + pm) * 2 + wc) * 256 + wr * 128 + g * 32 + mp * 16, __builtin_bit_cast(u32x4, dz)); }
  waitv<0>(); bar();
  if (wid == 4 && lane == 0) __hip_atomic_fetch_add(c.cnt(CNT_KV + pn), 1u, RLX_AGENT);
  u32x4 phiq[2][2];
  { bf16x8 qf[2]; u32x4 pos[2], neg[2];
#pragma unroll
    for (int i = 0; i < 2; ++i) qf[i] = ldsld<bf16x8>(lds0 + K1_QX + (w4 * 4 + 2 * mp + i) * 1024 + lane * 16);
    FENCE();
    feature_maps(wf0, wf1, qf, pos, neg);
#pragma unroll
    for (int i = 0; i < 2; ++i) { phiq[i][0] = pos[i]; phiq[i][1] = neg[i]; } }
  if (wid == 4) wait_count(c.cnt(CNT_KV + pn), 16u, c.cnt(CNT_TMO));
  lbar();
  { const int head = wid & 1, fr = wid >> 1;
    u32x4 pv[16];
#pragma unroll
    for (int p = 0; p < 16; ++p) pv[p] = ld16(c, WS_KVP + ((pn * 16 + p) * 2 + head) * 4096 + fr * 1024 + lane * 16);
    float zs = 0.f;
    if (wid < 2) {
      float zv[16];
#pragma unroll
      for (int p = 0; p < 16; ++p) zv[p] = ld4(c, WS_ZP + ((pn * 16 + p) * 2 + head) * 256 + lane * 4);
#pragma unroll
      for (int p = 0; p < 16; ++p) zs += zv[p];
    }
    f32x4 s0 = {0.f, 0.f, 0.f, 0.f}, s1 = s0;
#pragma unroll
    for (int p = 0; p < 16; ++p) { s0 += (f32x4){bflo(pv[p][0]), bfhi(pv[p][0]), bflo(pv[p][1]), bfhi(pv[p][1])}; s1 += (f32x4){bflo(pv[p][2]), bfhi(pv[p][2]), bflo(pv[p][3]), bfhi(pv[p][3])}; }
    ldsst<u32x4>(lds0 + K1_KVT + (head * 4 + fr) * 1024 + lane * 16, pack8(s0, s1));
    if (wid < 2) ldsst<u16>(lds0 + K1_ZT + head * 128 + lane * 2, (u16)cvtpk(zs, 0.f));
  }
  if (wid >= 4) prefetch_w64<C2, PRE2, DM * 2>(lds0, w4, lane, wo, DM * DM * 2, (unsigned)pn * 128, pn);
  lbar();
  { bf16x8 kv[2][2], zf[2];
#pragma unroll
    for (int ks = 0; ks < 2; ++ks) { zf[ks] = ldsld<bf16x8>(lds0 + K1_ZT + wc * 128 + ks * 64 + g * 16);
#pragma unroll
      for (int dt = 0; dt < 2; ++dt) kv[ks][dt] = ldsld<bf16x8>(lds0 + K1_KVT + (wc * 4 + ks * 2 + dt) * 1024 + lane * 16); }
#pragma unroll
    for (int i = 0; i < 2; ++i) {
      const f32x4 z4 = {0.f, 0.f, 0.f, 0.f};
      const bf16x8 b0 = asbf(phiq[i][0]), b1 = asbf(phiq[i][1]);
      const f32x4 den = mfma(zf[1], b1, mfma(zf[0], b0, z4));
      const f32x4 n0 = mfma(kv[1][0], b1, mfma(kv[0][0], b0, z4)), n1 = mfma(kv[1][1], b1, mfma(kv[0][1], b0, z4));
      const float r = __builtin_amdgcn_rcpf(den[0] + 1e-6f);
      const int tok = wr * 64 + (2 * mp + i) * 16 + i16;
      const unsigned oa = lds0 + K1_OST + (g & 1) * 8;
      ldsst<u32x2>(oa + aimg(tok, wc * 4 + (g >> 1)), (u32x2){cvtpk(n0[0] * r, n0[1] * r), cvtpk(n0[2] * r, n0[3] * r)});
      ldsst<u32x2>(oa + aimg(tok, wc * 4 + 2 + (g >> 1)), (u32x2){cvtpk(n1[0] * r, n1[1] * r), cvtpk(n1[2] * r, n1[3] * r)});
    } }
  lbar();
#pragma unroll
  for (int i = 0; i < 2; ++i) { const int p = tid + i * 512; st16(c, WS_O + (pm * 16 + pn) * 16384 + p * 16, ldsld<u32x4>(lds0 + K1_OST + p * 16)); }
}

constexpr int Z_RS = 232;
constexpr int K3_STG = C3::RING;
constexpr int K3_RST = K3_STG + 4 * 32 * Z_RS;
constexpr int K3_CS = K3_RST + 1024;
constexpr int K3_SEEN = K3_CS + 1024;
constexpr int G_RS = 240;
static_assert(K3_SEEN + 16 <= LDS_DUMMY, "P34 map");
template<class Base> struct Src2 : Base {
  const Ctx& c;
  template<class... A> DI Src2(const Ctx& c_, A... a) : Base(a...), c(c_) {}
  DI void steady(int& slot) {
    auto is = [&](int i, int sl) { this->dep_half(i, sl); this->w_half(i, sl); };
    tile_loop<C2>(this->P, slot, C2::ST, 6, is);
    set_flag(c, FLG_W + c.pn, c.pm);
    tile_loop<C2>(this->P, slot, 7, C2::NKT - C2::ST, is);
  }
};
template<class C, bool FIRST, int NPRE>
DI void phase_res(const Ctx& c, unsigned a_off, const u16* W, unsigned w_bytes, const u16* x, u16* out) {
  const unsigned lds0 = c.lds0; const int tid = c.tid, lane = c.lane, wid = c.wid, pm = c.pm, pn = c.pn;
  if (wid >= 4) {
    const Pieces<C> P{wid - 4, lds0};
    if constexpr (FIRST) {
      auto need = [](int kt) { return 1u << kt; };
      Src2<SrcW64<C, true, DM * 2, decltype(need)>> src(c, P, lane, pn, c.ws_srd(), a_off + (unsigned)pm * (C::NKT * 16384), make_srd(W, w_bytes), (unsigned)pn * 128, 0, c.dep(FLG_O + pm, pn), need);
      producer_loop<C, NPRE>(P, src);
    } else {
      auto need = [](int kt) { return (1u << ((4u * kt) / 7u)) | (1u << ((8u * kt + 7u) / 14u)); };
      SrcW64<C, true, DM * 2, decltype(need)> src(P, lane, kt0_5(pn), c.ws_srd(), a_off + (unsigned)pm * (C::NKT * 16384), make_srd(W, w_bytes), (unsigned)pn * 128, 0, c.dep(FLG_G + pm, pn), need);
      producer_loop<C, NPRE>(P, src);
    }
    bar();
    if constexpr (FIRST) {
      Dep dw = c.dep(FLG_W + pn, pm); dw.ensure(0xffffu, lane);
      const Pieces<C3> P3{wid - 4, lds0}; const i32x4 rs = c.ws_srd();
#pragma unroll
      for (int s = 0; s < PRE3; ++s) P3.b_img(s, lane * 16, rs, uni(win_off(pn, 1, (pn + s) & 15)), iseq<C3::IBMAX>{});
      const int t = tid - 256; float cs[16];
#pragma unroll
      for (int p = 0; p < 16; ++p) cs[p] = ld4(c, WS_CSP + ((pn * 16 + p) * 224 + (t < 224 ? t : 0)) * 4);
      float sum = 0.f;
#pragma unroll
      for (int p = 0; p < 16; ++p) sum += cs[p];
      if (t < 224) ldsst<float>(lds0 + K3_CS + (t < 112 ? t : t + 16) * 4, sum);
    }
  } else {
    u32x4 rv[4];
    f32x4 acc[C::NT][C::MT];
    Consumer<C> K; K.init(lds0, wid, lane); zero_acc<C>(acc);
    if constexpr (FIRST) win_put<0, 7>(c, wid);
    K.start();
#pragma unroll
    for (int i = 0; i < 4; ++i) { const int p = tid + i * 256, row = p >> 3, cc = (p & 7) ^ ((row >> 1) & 7);
      rv[i] = FIRST ? *(const u32x4*)((const unsigned char*)x + (size_t)(pm * 128 + row) * (DM * 2) + pn * 128 + cc * 16) : ld16(c, WS_H + (pm * 16 + pn) * 16384 + p * 16); }
    if constexpr (FIRST) {
      if (tid < 224) { const unsigned a = lds0 + K1_CS4 + (tid < 112 ? tid * 4 : 1024 + (tid - 112) * 4);
        st4(c, WS_CSP + ((pn * 16 + pm) * 224 + tid) * 4, __float_as_uint(ldsld<float>(a) + ldsld<float>(a + 512))); }
      FENCE();
      K.run(acc, 5);
      waitv<0>();
      K.run(acc, C::NKT - 5);
    } else K.run(acc, C::NKT);
    const int g = lane >> 4, i16 = lane & 15, wr = wid >> 1, wc = wid & 1;
#pragma unroll
    for (int n = 0; n < 2; ++n)
#pragma unroll
      for (int m = 0; m < 4; ++m)
        ldsst<u32x2>(lds0 + aimg(wr * 64 + m * 16 + i16, wc * 4 + n * 2 + (g >> 1)) + (g & 1) * 8, (u32x2){cvtpk(acc[n][m][0], acc[n][m][1]), cvtpk(acc[n][m][2], acc[n][m][3])});
    lbar();
#pragma unroll
    for (int i = 0; i < 4; ++i) { const int p = tid + i * 256, row = p >> 3, cc = (p & 7) ^ ((row >> 1) & 7);
      const u32x4 a = ldsld<u32x4>(lds0 + p * 16); u32x4 o; float s = 0.f, qq = 0.f;
#pragma unroll
      for (int j = 0; j < 4; ++j) { o[j] = cvtpk(bflo(rv[i][j]) + bflo(a[j]), bfhi(rv[i][j]) + bfhi(a[j]));
        if (FIRST) { s = dot2(o[j], ONES2, s); qq = dot2(o[j], o[j], qq); } }
      if (FIRST) {
        st16(c, WS_H + (pm * 16 + pn) * 16384 + p * 16, o);
        s = dpp_add<0x111, 0xf>(s); qq = dpp_add<0x111, 0xf>(qq); s = dpp_add<0x112, 0xf>(s); qq = dpp_add<0x112, 0xf>(qq);
        s = dpp_add<0x114, 0xf>(s); qq = dpp_add<0x114, 0xf>(qq);
        if ((p & 7) == 7) st8(c, WS_HST + ((pm * 16 + pn) * 128 + row) * 8, (f32x2){s, qq});
      } else __builtin_amdgcn_raw_buffer_store_b128(o, make_rsrc(out, SEQ * DM * 2), (pm * 128 + row) * (DM * 2) + pn * 128 + cc * 16, 0, 16);
    }
  }
}

DI f32x2 gelu2(f32x2 x) {
  constexpr float C0 = -2.0f * 0.7978845608028654f * 1.4426950408889634f, C1_ = C0 * 0.044715f;
  const f32x2 a = x * (x * x * C1_ + C0);
  const f32x2 d = (f32x2){__builtin_amdgcn_exp2f(a[0]), __builtin_amdgcn_exp2f(a[1])} + 1.0f;
  return x * (f32x2){__builtin_amdgcn_rcpf(d[0]), __builtin_amdgcn_rcpf(d[1])};
}
DI void fold_gelu(const f32x4 (&acc)[7][2], u32x2 (&z)[2][7], unsigned lds0, int half, int wid, int lane) {
  const int g = lane >> 4, i16 = lane & 15;
  f32x2 rs[2]; f32x4 cs[7];
#pragma unroll
  for (int m = 0; m < 2; ++m) rs[m] = ldsld<f32x2>(lds0 + K3_RST + (wid * 32 + m * 16 + i16) * 8);
#pragma unroll
  for (int n = 0; n < 7; ++n) cs[n] = ldsld<f32x4>(lds0 + K3_CS + half * 512 + (n * 16 + g * 4) * 4);
  FENCE();
#pragma unroll
  for (int m = 0; m < 2; ++m) {
    const float r = rs[m][1], mr = -rs[m][0] * rs[m][1];
#pragma unroll
    for (int n = 0; n < 7; ++n) {
      const f32x4 v = acc[n][m] * r + cs[n] * mr;
      const f32x2 lo = gelu2((f32x2){v[0], v[1]}), hi = gelu2((f32x2){v[2], v[3]});
      z[m][n] = (u32x2){cvtpk(lo[0], lo[1]), cvtpk(hi[0], hi[1])};
    }
  }
#pragma unroll
  for (int m = 0; m < 2; ++m)
#pragma unroll
    for (int n = 0; n < 7; ++n) asm volatile("" : "+v"(z[m][n]));
}
struct Src34 {
  static constexpr bool DEP = true;
  const Ctx& c; Pieces<C3> P; i32x4 rsW, rsS; unsigned vi, vr, h0, w0[2], z0, s0; Dep depH, depZ;
  DI Src34(const Ctx& c_, const Pieces<C3>& P_, const u16* w_sp)
    : c(c_), P(P_), rsW(c_.ws_srd()), rsS(make_srd(w_sp, SEQ * SEQ * 2)), vi(c_.lane * 16), vr(voff_rows(P_.lw, c_.lane, SEQ * 2)),
      h0(uni((unsigned)WS_H + c_.pm * (16 * 16384))), w0{(unsigned)uni(win_off(c_.pn, 1, 0)), (unsigned)uni(win_off(c_.pn, 0, 0))}, z0(uni((unsigned)WS_Z2 + c_.pn * (32 * C3::B_BYTES))),
      s0(uni(c_.pm * 128 * (SEQ * 2))), depH(c_.dep(FLG_H + c_.pm, c_.pn)), depZ(c_.dep(FLG_Z2 + c_.pn, c_.pm)) {}
  static_assert(C3::ST == 4, "the segment boundaries below");
  template<int SEG> DI void issue(int i, int slot) {
    if constexpr (SEG < 2) {
      const int k = (c.pn + i) & 15;
      if constexpr (SEG == 0) depH.ensure(1u << k, c.lane);
      P.a_img<true>(slot, vi, rsW, h0 + k * 16384, iseq<C3::IA>{});
      P.b_img(slot, vi, rsW, w0[SEG] + k * C3::B_BYTES, iseq<C3::IBMAX>{});
    } else {
      const int k = (2 * c.pm + i) & 31;
      depZ.ensure(1u << (k >> 1), c.lane);
      P.b_img(slot, vi, rsW, z0 + k * C3::B_BYTES, iseq<C3::IBMAX>{});
      P.a_rows(slot, vr, rsS, s0 + k * 128, SEQ * 2, iseq<C3::IA>{});
    }
  }
  DI void dep_half(int i, int slot) { const int k = (c.pn + i) & 15; if (i > 0) depH.ensure(1u << k, c.lane); P.a_img<true>(slot, vi, rsW, h0 + k * 16384, iseq<C3::IA>{}); }
  DI void w_half(int i, int slot) const { P.b_img(slot, vi, rsW, w0[0] + ((c.pn + i) & 15) * C3::B_BYTES, iseq<C3::IBMAX>{}); }
  DI void steady(int& slot) {
    auto s0_ = [&](int i, int sl) { issue<0>(i, sl); }; auto s1_ = [&](int i, int sl) { issue<1>(i, sl); }; auto s2_ = [&](int i, int sl) { issue<2>(i, sl); };
    tile_loop<C3>(P, slot, 4, 12, s0_);
#pragma unroll
    for (int i = 0; i < 4; ++i) dma16<true>(c.lds0 + K3_STG + (P.lw + 4 * i) * 1024, vi, rsW, uni((unsigned)WS_HST + (c.pm * 16 + P.lw + 4 * i) * 1024));
    tile_loop<C3>(P, slot, 13, 16, s1_);
    prewarm<256>(c.lds0 + LDS_DUMMY, P.lw, c.lane, rsS, [&](unsigned i) { return (blockIdx.x * 256 + i) * 128; });
    tile_loop<C3>(P, slot, 17, 24, s1_);
    set_flag(c, FLG_Z2 + c.pn, c.pm);
    tile_loop<C3>(P, slot, 25, 28, s1_);
    tile_loop<C3>(P, slot, 29, 30, s2_);
    depZ.seen |= (unsigned)uni(ldsld<unsigned>(c.lds0 + K3_SEEN));
    tile_loop<C3>(P, slot, 31, C3::NKT - C3::ST, s2_);
  }
};
DI void phase34(const Ctx& c, const u16* w_sp, const u16* b_sp, const u16* w_out) {
  const unsigned lds0 = c.lds0; const int tid = c.tid, lane = c.lane, wid = c.wid, pm = c.pm, pn = c.pn;
  if (wid >= 4) {
    const Pieces<C3> P{wid - 4, lds0};
    Src34 src(c, P, w_sp);
    producer_loop<C3, PRE3>(P, src);
    prefetch_w64<C5, PRE5, DM * 2>(lds0, wid - 4, lane, w_out, FH * DM * 2, (unsigned)pn * 128, kt0_5(pn));
    return;
  }
  const int g = lane >> 4, i16 = lane & 15;
  f32x4 acc[7][2];
  Consumer<C3> K; K.init(lds0, wid, lane); zero_acc<C3>(acc);
  K.start();
  K.run(acc, 15);
  { f32x2 st[8];
#pragma unroll
    for (int p = 0; p < 8; ++p) st[p] = ldsld<f32x2>(lds0 + K3_STG + ((tid & 1) * 8 + p) * 1024 + (tid >> 1) * 8);
    f32x2 s = (st[0] + st[1]) + (st[2] + st[3]) + ((st[4] + st[5]) + (st[6] + st[7]));
    s[0] = dpp_add<0x111, 0xf>(s[0]); s[1] = dpp_add<0x111, 0xf>(s[1]);
    const float mean = s[0] * (1.0f / DM), var = fmaxf(s[1] * (1.0f / DM) - mean * mean, 0.f);
    if (tid & 1) ldsst<f32x2>(lds0 + K3_RST + (tid >> 1) * 8, (f32x2){mean, __builtin_amdgcn_rsqf(var + 1e-5f)});
  }
  K.run(acc, 1);
  u32x2 z[2][7];
  fold_gelu(acc, z, lds0, 1, wid, lane);
  zero_acc<C3>(acc);
  { float s0 = 0.f, s1 = 0.f, q0 = 0.f, q1 = 0.f;
#pragma unroll
    for (int n = 0; n < 7; ++n) {
      s0 = dot2(z[0][n][1], ONES2, dot2(z[0][n][0], ONES2, s0)); q0 = dot2(z[0][n][1], z[0][n][1], dot2(z[0][n][0], z[0][n][0], q0));
      s1 = dot2(z[1][n][1], ONES2, dot2(z[1][n][0], ONES2, s1)); q1 = dot2(z[1][n][1], z[1][n][1], dot2(z[1][n][0], z[1][n][0], q1)); }
    asm volatile("s_nop 2" : "+v"(s0), "+v"(s1), "+v"(q0), "+v"(q1));
    sum4x4(s0, q0, s1, q1);
    const float s = g & 1 ? s1 : s0, q = g & 1 ? q1 : q0; const unsigned qb = __float_as_uint(q);
    if (g < 2) st8(c, WS_ZST + ((pm * 16 + pn) * 128 + wid * 32 + g * 16 + i16) * 8, (f32x2){s, __uint_as_float(qb == ZST_EMPTY ? 0x7fc00000u : qb)}); }
  K.run(acc, 3);
  { f32x2 t[2], w[2][4];
    auto fetch = [&] {
#pragma unroll
      for (int m = 0; m < 2; ++m)
#pragma unroll
        for (int p = 0; p < 4; ++p) w[m][p] = ld8(c, WS_ZST + ((pm * 16 + 4 * g + p) * 128 + wid * 32 + m * 16 + i16) * 8); };
    fetch();
    FENCE();
    K.run(acc, 1);
    for (unsigned spins = 0;; ++spins) {
      bool ok = true;
#pragma unroll
      for (int m = 0; m < 2; ++m) {
#pragma unroll
        for (int p = 0; p < 4; ++p) ok &= __float_as_uint(w[m][p][1]) != ZST_EMPTY;
        t[m] = (w[m][0] + w[m][1]) + (w[m][2] + w[m][3]); }
      if (__all(ok) || spin_fail(spins, c.cnt(CNT_TMO))) break;
      fetch();
    }
    float a0 = t[0][0], a1 = t[0][1], a2 = t[1][0], a3 = t[1][1];
    sum4x4(a0, a1, a2, a3);
    const unsigned stg = lds0 + K3_STG + wid * (32 * Z_RS);
#pragma unroll
    for (int m = 0; m < 2; ++m) {
      const float mean = (m ? a2 : a0) * (1.0f / FH), var = fmaxf((m ? a3 : a1) * (1.0f / FH) - mean * mean, 0.f), rstd = __builtin_amdgcn_rsqf(var + 1e-5f), mr = -mean * rstd;
#pragma unroll
      for (int n = 0; n < 7; ++n)
        ldsst<u32x2>(stg + (m * 16 + i16) * Z_RS + n * 32 + g * 8,
                     (u32x2){cvtpk(bflo(z[m][n][0]) * rstd + mr, bfhi(z[m][n][0]) * rstd + mr), cvtpk(bflo(z[m][n][1]) * rstd + mr, bfhi(z[m][n][1]) * rstd + mr)});
    }
    waitl0();
    const unsigned rd = stg + (8 * g + (i16 >> 2)) * Z_RS + (lane & 3) * 8, z2 = WS_Z2 + (pn * 32 + 2 * pm + (wid >> 1)) * C3::B_BYTES;
#pragma unroll
    for (int j = 0; j < 7; ++j)
      st16(c, z2 + aimg(j * 16 + i16, (wid & 1) * 4 + g), __builtin_bit_cast(u32x4, join(trread(rd + j * 32), trread(rd + 4 * Z_RS + j * 32)))); }
  K.run(acc, 3);
  waitv<0>();
  K.run(acc, 5);
  { const unsigned v = __builtin_amdgcn_raw_buffer_load_b32(c.rs, WS_CTL + (FLG_Z2 + pn) * 256 + (lane & 15) * 4, 0, AUX_SC1);
    FENCE();
    K.run(acc, 1);
    const unsigned seen = (unsigned)__ballot(v != 0u) & 0xffffu;
    if (wid == 0) ldsst<unsigned>(lds0 + K3_SEEN, seen); }
  K.run(acc, 3);
  fold_gelu(acc, z, lds0, 0, wid, lane);
  zero_acc<C3>(acc);
  unsigned bs[2];
#pragma unroll
  for (int m = 0; m < 2; ++m) bs[m] = b_sp[pm * 128 + wid * 32 + m * 16 + i16];
  FENCE();
  K.run(acc, 32);
  waitl0();
  const unsigned stg = lds0 + (wid >> 1) * C5::STAGE + (wid & 1) * 32 * G_RS;
#pragma unroll
  for (int n = 0; n < 7; ++n)
#pragma unroll
    for (int m = 0; m < 2; ++m) { const unsigned a = z[m][n][0], b = z[m][n][1]; const f32x4 v = acc[n][m] + bflo(bs[m]);
      ldsst<u32x2>(stg + (m * 16 + i16) * G_RS + n * 32 + g * 8, (u32x2){cvtpk(bflo(a) * v[0], bfhi(a) * v[1]), cvtpk(bflo(b) * v[2], bfhi(b) * v[3])}); }
  waitl0();
  const unsigned gp = WS_G + pm * (28 * 16384);
#pragma unroll
  for (int i = 0; i < 7; ++i) { const unsigned ci = i * 64 + lane, r = ci / 14u, ch = ci - r * 14u, cc = pn * 14 + ch, row = wid * 32 + r;
    st16(c, gp + (cc >> 3) * 16384 + aimg(row, cc & 7), ldsld<u32x4>(stg + r * G_RS + ch * 16)); }
}

}

struct HedgehogArgs { const unsigned short *x, *wqkv, *wphi, *wo, *w_in, *w_sp, *b_sp, *w_out; unsigned char* ws; unsigned short* out; };
extern "C" __global__ __launch_bounds__(512, 2) void hedgehog_gmlp_main(HedgehogArgs a) {
  extern __shared__ __attribute__((aligned(16))) unsigned char shm[];
  Ctx c;
  c.lds0 = (unsigned)(size_t)(LAS unsigned char*)shm;
  c.tid = threadIdx.x; c.lane = c.tid & 63; c.wid = uni(c.tid >> 6);
  { const int b = blockIdx.x, xl = b & 7, j = b >> 3;
    c.pm = (xl & 3) * 4 + (j & 3); c.pn = (xl >> 2) * 8 + (j >> 2); }
  c.ws = a.ws; c.rs = make_rsrc(a.ws, (unsigned)WS_END);
  if (c.wid == 1) {
    const unsigned long long pc = __builtin_amdgcn_s_getpc() & ~127ull;
    const unsigned i = (blockIdx.x >> 3) * 12 + c.lane;
    if (c.lane < 12) touch64(c.lds0 + LDS_DUMMY, (i < MAIN_CODE_LINES - 4 ? i : MAIN_CODE_LINES - 5) * 128, make_srd((const void*)pc, (MAIN_CODE_LINES - 4) * 128));
  }
  phase1(c, a.wqkv, a.wphi, a.wo, a.w_in, a.w_out);
  publish(c, FLG_O + c.pm, c.pn);
  phase_res<C2, true, PRE2>(c, (unsigned)WS_O, a.wo, DM * DM * 2, a.x, nullptr);
  publish(c, FLG_H + c.pm, c.pn);
  phase34(c, a.w_sp, a.b_sp, a.w_out);
  publish(c, FLG_G + c.pm, c.pn);
  phase_res<C5, false, PRE5>(c, (unsigned)WS_G, a.w_out, FH * DM * 2, nullptr, a.out);
}

extern "C" void kernel_launch(void* const* d_in, const int* in_sizes, int n_in, void* d_out, int out_size, void* d_ws, size_t ws_size, hipStream_t stream) {
  HedgehogArgs a;
  a.x = (const u16*)d_in[0]; a.wqkv = (const u16*)d_in[1]; a.wphi = (const u16*)d_in[2]; a.wo = (const u16*)d_in[3];
  a.w_in = (const u16*)d_in[5]; a.w_sp = (const u16*)d_in[6]; a.b_sp = (const u16*)d_in[7]; a.w_out = (const u16*)d_in[8];
  a.ws = (unsigned char*)d_ws; a.out = (u16*)d_out;
  (void)in_sizes; (void)n_in; (void)out_size; (void)ws_size;
  k0_prep<<<256, 512, 0, stream>>>(a.x, a.wqkv, a.ws);
  (void)hipFuncSetAttribute((const void*)hedgehog_gmlp_main, hipFuncAttributeMaxDynamicSharedMemorySize, LDS_TOTAL);
  hedgehog_gmlp_main<<<256, 512, LDS_TOTAL, stream>>>(a);
}
```
